```python
import torch

def _softmax(x, axis=-1):
    return torch.softmax(x, dim=axis)
def _gelu(x):
    return torch.nn.functional.gelu(x, approximate="tanh")
def _silu(x):
    return torch.nn.functional.silu(x)
def _sigmoid(x):
    return torch.sigmoid(x)
def _layernorm(x, axis=-1, eps=1e-5):
    m = x.mean(dim=axis, keepdim=True)
    v = x.var(dim=axis, keepdim=True, unbiased=False)
    return (x - m) / torch.sqrt(v + eps)
def _scatter_add(tgt, idx, src, axis=0):
    return tgt.clone().index_add_(axis, idx, src)
def _segment_sum(src, seg, n):
    out = torch.zeros((n,) + src.shape[1:], dtype=src.dtype)
    return out.index_add_(0, seg, src)
def _pad(x, pw):
    flat = tuple(v for pair in reversed(pw) for v in pair)
    return torch.nn.functional.pad(x, flat)

kernel_name = 'parallel_sum__ssm_conv_s4__dense_activation_gelu__prompt'

def setup_inputs():

    _w = (lambda a: (lambda t: t.to(torch.bfloat16) if t.is_floating_point() else t)(torch.as_tensor(a)))
    def _A_setup():
        B, L, D, N = 1, 2048, 576, 128
        A_re = -(lambda _x: torch.exp(torch.as_tensor(_x)))((lambda *s: torch.randn(*s, dtype=torch.bfloat16) * 6.4)(D, N))
        A_im = torch.pi * (lambda *s: torch.randn(*s, dtype=torch.bfloat16) * 0.05)(D, N)
        B_re = (lambda *s: torch.randn(*s, dtype=torch.bfloat16) * 0.1)(D, N)
        B_im = (lambda *s: torch.randn(*s, dtype=torch.bfloat16) * 0.1)(D, N)
        C_re = (lambda *s: torch.randn(*s, dtype=torch.bfloat16) * 0.1)(D, N)
        C_im = (lambda *s: torch.randn(*s, dtype=torch.bfloat16) * 0.1)(D, N)
        log_dt = (lambda *s: torch.randn(*s, dtype=torch.bfloat16) * 0.05)(D) * 0.5 - 4.0
        Dskip = (lambda *s: torch.randn(*s, dtype=torch.bfloat16) * 0.05)(D)
        return {
            "u": _w((lambda *s: torch.randn(*s, dtype=torch.bfloat16) * 0.05)(B, L, D)),
            "A_re": _w(A_re),
            "A_im": _w(A_im),
            "B_re": _w(B_re),
            "B_im": _w(B_im),
            "C_re": _w(C_re),
            "C_im": _w(C_im),
            "log_dt": _w(log_dt),
            "Dskip": _w(Dskip),
        }
    _A_in = _A_setup()

    def _B_setup():
        B, S, D, F = 1, 2048, 576, 4608
        return {
            "x": _w((lambda *s: torch.randn(*s, dtype=torch.bfloat16) * 0.05)(B, S, D)),
            "w1": _w((lambda *s: torch.randn(*s, dtype=torch.bfloat16) * 0.8)(D, F) * (D ** -0.5)),
            "b1": _w((lambda *s: torch.randn(*s, dtype=torch.bfloat16) * 0.2)(F) * 0.01),
            "w2": _w((lambda *s: torch.randn(*s, dtype=torch.bfloat16) * 0.2)(F, D) * (F ** -0.5)),
            "b2": _w((lambda *s: torch.randn(*s, dtype=torch.bfloat16) * 0.2)(D) * 0.01),
        }
    _B_in = _B_setup()

    return {
        "A_u": _A_in["u"],
        "A_A_re": _A_in["A_re"],
        "A_A_im": _A_in["A_im"],
        "A_B_re": _A_in["B_re"],
        "A_B_im": _A_in["B_im"],
        "A_C_re": _A_in["C_re"],
        "A_C_im": _A_in["C_im"],
        "A_log_dt": _A_in["log_dt"],
        "A_Dskip": _A_in["Dskip"],
        "B_x": _B_in["x"],
        "B_w1": _B_in["w1"],
        "B_b1": _B_in["b1"],
        "B_w2": _B_in["w2"],
        "B_b2": _B_in["b2"],
    }

def reference(A_u, A_A_re, A_A_im, A_B_re, A_B_im, A_C_re, A_C_im, A_log_dt, A_Dskip, B_x, B_w1, B_b1, B_w2, B_b2):
    def _A_ref(u, A_re, A_im, B_re, B_im, C_re, C_im, log_dt, Dskip):
        Bsz, L, D = u.shape
        dt = torch.exp(log_dt)[:, None]
        A = A_re + 1j * A_im
        Bc = B_re + 1j * B_im
        Cc = C_re + 1j * C_im
        logdA = dt * A
        dB = dt * Bc
        l = (lambda x, dt: x.to(dt))(torch.arange(L), A_re.dtype)
        P = torch.exp(logdA[:, :, None] * l[None, None, :])
        K = torch.einsum("dn,dnl,dn->dl", Cc, P, dB).real
        L2 = 2 * L
        Kp = _pad(K, ((0, 0), (0, L)))
        up = (lambda x, *d: x.permute(*d))(u, 0, 2, 1)
        up = _pad(up, ((0, 0), (0, 0), (0, L)))
        Kf = (lambda x, axes=(-2,-1): torch.fft.rfft2(x.float(), dim=axes))(Kp[:, None, :], axes=(-2, -1))
        Uf = (lambda x, axes=(-2,-1): torch.fft.rfft2(x.float(), dim=axes))(up[:, :, None, :], axes=(-2, -1))
        Yf = Uf * Kf[None, :, :, :]
        y = (lambda x, s, axes=(-2,-1): torch.fft.irfft2(x, s=s, dim=axes).to(torch.bfloat16))(Yf, (1, L2), axes=(-2, -1))[:, :, 0, :L]
        y = (lambda x, *d: x.permute(*d))(y, 0, 2, 1) + u * Dskip[None, None, :]
        return y

    def _B_ref(x, w1, b1, w2, b2):
        h = torch.einsum("bsd,df->bsf", x, w1) + b1
        h = _gelu(h)
        return torch.einsum("bsf,fd->bsd", h, w2) + b2

    x = A_u
    xn = _layernorm(x, axis=-1)
    return x + _A_ref(xn, A_A_re, A_A_im, A_B_re, A_B_im, A_C_re, A_C_im, A_log_dt, A_Dskip) + _B_ref(xn, B_w1, B_b1, B_w2, B_b2)
```

```cpp
#include <hip/hip_runtime.h>
#include <hip/hip_bf16.h>
#include <stdint.h>

typedef unsigned short u16;
typedef unsigned int u32;
typedef unsigned long long u64;
typedef __bf16 bf16x8 __attribute__((ext_vector_type(8)));
typedef __bf16 bf16x2 __attribute__((ext_vector_type(2)));
typedef short s16x4 __attribute__((ext_vector_type(4)));
typedef float f32x4 __attribute__((ext_vector_type(4)));
typedef float f32x2 __attribute__((ext_vector_type(2)));
typedef u32 u32x4 __attribute__((ext_vector_type(4)));
typedef u32 u32x2 __attribute__((ext_vector_type(2)));
#define LAS __attribute__((address_space(3)))
#define GAS __attribute__((address_space(1)))

constexpr int L = 2048, D = 576, NM = 128, F = 4608;

constexpr size_t WS_CNT = 0;
constexpr size_t WS_XNF = 16384;
constexpr size_t WS_UT2 = WS_XNF + (size_t)L * D * 2;
constexpr size_t WS_KIM = WS_UT2 + (size_t)L * D * 2;
constexpr int KIM_B = 9216, KIM_C1 = 4640;
constexpr size_t WS_A2 = WS_KIM + (size_t)D * KIM_B;
constexpr size_t WS_W2P = WS_A2 + (size_t)L * D * 2;
constexpr size_t WS_HF = WS_W2P + (size_t)F * D * 2;
constexpr size_t WS_SLAB = WS_HF + (size_t)L * F * 2;
constexpr int SLAB_B = 18432;
constexpr size_t WS_END = WS_SLAB + (size_t)64 * 16 * SLAB_B;
#define STAMP_AT(kern, slot)

__device__ __forceinline__ float bl(u32 v) { return __uint_as_float(v << 16); }
__device__ __forceinline__ float bh(u32 v) { return __uint_as_float(v & 0xffff0000u); }
__device__ __forceinline__ float b2f(u16 v) { return __uint_as_float(((u32)v) << 16); }
__device__ __forceinline__ u32 pk(float lo, float hi) {
  bf16x2 r = {(__bf16)lo, (__bf16)hi};
  return __builtin_bit_cast(u32, r);
}
__device__ __forceinline__ float rb(float f) { return (float)(__bf16)f; }
__device__ __forceinline__ f32x4 mfma16(u32x4 a, u32x4 b, f32x4 c) {
  return __builtin_amdgcn_mfma_f32_16x16x32_bf16(__builtin_bit_cast(bf16x8, a), __builtin_bit_cast(bf16x8, b), c, 0, 0, 0);
}
__device__ __forceinline__ u32x2 tr_read(const char LAS* p) {
  return __builtin_bit_cast(u32x2, __builtin_amdgcn_ds_read_tr16_b64_v4i16((s16x4 LAS*)p));
}
__device__ __forceinline__ void glds16(const char* g, char LAS* l) {
  __builtin_amdgcn_global_load_lds((const GAS void*)g, (LAS void*)l, 16, 0, 0);
}
template <int N> __device__ __forceinline__ void waitv() { asm volatile("s_waitcnt vmcnt(%0)" ::"i"(N) : "memory"); }
template <int N> __device__ __forceinline__ void waitl() { asm volatile("s_waitcnt lgkmcnt(%0)" ::"i"(N) : "memory"); }
__device__ __forceinline__ void bar() {
  asm volatile("" ::: "memory");
  __builtin_amdgcn_s_barrier();
  asm volatile("" ::: "memory");
}
template <int I, int N, class Fn> __device__ __forceinline__ void static_for(Fn&& fn) {
  if constexpr (I < N) {
    fn(std::integral_constant<int, I>{});
    static_for<I + 1, N>(fn);
  }
}

struct cf { float re, im; };
__device__ __forceinline__ cf cmul(cf a, cf b) {
  return {__builtin_fmaf(a.re, b.re, -a.im * b.im), __builtin_fmaf(a.re, b.im, a.im * b.re)};
}
template <int E> __device__ __forceinline__ cf zpow2(float p2, float qh, float ql) {
  constexpr float s = (float)(1 << E);
  float mag = __builtin_amdgcn_exp2f(p2 * s);
  float ph = __builtin_amdgcn_fractf(qh * s) + ql * s;
  return {mag * __builtin_amdgcn_cosf(ph), mag * __builtin_amdgcn_sinf(ph)};
}
__device__ __forceinline__ int kpos(int j) {
  int b = (int)(pk((float)j, 0.f) & 0xffffu) - (0x4300 - 128);
  return j < 128 ? j : b;
}

constexpr int LN_S = 1168, KG_S = 528;
__global__ __launch_bounds__(256) void k_prep(const u16* __restrict__ x, const u16* __restrict__ Are, const u16* __restrict__ Aim,
                                              const u16* __restrict__ Bre, const u16* __restrict__ Bim, const u16* __restrict__ Cre,
                                              const u16* __restrict__ Cim, const u16* __restrict__ logdt, char* __restrict__ ws) {
  __shared__ __attribute__((aligned(16))) char smem[53 * KG_S + 1536];
  char LAS* sm = (char LAS*)smem;
  const int tid = threadIdx.x, lane = tid & 63, wid = __builtin_amdgcn_readfirstlane(tid >> 6);
  STAMP_AT(0, 0);
  if (blockIdx.x < 128) {
    const int lt = blockIdx.x, r = tid >> 4, lam = tid & 15;
    if (lt == 0 && tid < 64) *(u32*)(ws + WS_CNT + tid * 128) = 0u;
    const u32x2* src = (const u32x2*)(x + (size_t)(lt * 16 + r) * D) + lam;
    float v[36];
    float s = 0.f;
#pragma unroll
    for (int i = 0; i < 9; ++i) {
      u32x2 t = src[16 * i];
      v[4 * i] = bl(t[0]); v[4 * i + 1] = bh(t[0]); v[4 * i + 2] = bl(t[1]); v[4 * i + 3] = bh(t[1]);
    }
#pragma unroll
    for (int i = 0; i < 36; ++i) s += v[i];
#pragma unroll
    for (int o = 8; o; o >>= 1) s += __shfl_xor(s, o);
    const float mean = s * (1.f / D);
    float q = 0.f;
#pragma unroll
    for (int i = 0; i < 36; ++i) { float d = v[i] - mean; q = __builtin_fmaf(d, d, q); }
#pragma unroll
    for (int o = 8; o; o >>= 1) q += __shfl_xor(q, o);
    const float m = rb(mean);
    const float sd = rb(__builtin_sqrtf(rb(rb(q * (1.f / D)) + 1e-5f)));
    const float rs = 1.f / sd;
#pragma unroll
    for (int i = 0; i < 9; ++i) {
      u32 t0 = pk(v[4 * i] - m, v[4 * i + 1] - m), t1 = pk(v[4 * i + 2] - m, v[4 * i + 3] - m);
      u32x2 o = {pk(bl(t0) * rs, bh(t0) * rs), pk(bl(t1) * rs, bh(t1) * rs)};
      *(u32x2 LAS*)(sm + r * LN_S + (lam + 16 * i) * 8) = o;
    }
    __syncthreads();
    const int c = lane & 15, g = lane >> 4;
    char* xo = ws + WS_XNF + (size_t)lt * 18432;
    char* uo = ws + WS_UT2 + (size_t)lt * 18432;
    for (int k = wid; k < 18; k += 4) {
      u32x2 a = *(u32x2 LAS*)(sm + c * LN_S + k * 64 + g * 8);
      u32x2 b = *(u32x2 LAS*)(sm + c * LN_S + k * 64 + 32 + g * 8);
      *(u32x4*)(xo + k * 1024 + lane * 16) = u32x4{a[0], a[1], b[0], b[1]};
      const char LAS* tp = sm + (8 * (g & 1) + (c >> 2)) * LN_S + k * 64 + (g >> 1) * 32 + (c & 3) * 8;
      u32x2 t0 = tr_read(tp), t1 = tr_read(tp + 4 * LN_S);
      *(u32x4*)(uo + k * 1024 + (16 * (g >> 1) + c) * 32 + (g & 1) * 16) = u32x4{t0[0], t0[1], t1[0], t1[1]};
    }
  } else {
    const int d = blockIdx.x - 128;
    const int n = (wid & 1) * 64 + lane, idx = d * NM + n;
    char LAS* LO = sm;
    char LAS* HI = sm + 32 * KG_S;
    char LAS* TB = sm + 53 * KG_S;
    const float dt = rb(__expf(b2f(logdt[d])));
    const float p2 = dt * b2f(Are[idx]) * 1.44269504088896340736f;
    const float q = dt * b2f(Aim[idx]);
    constexpr float CH = 0.15915494309189535f;
    constexpr float CL = (float)(0.15915494309189533577 - (double)CH);
    const float qh = q * CH;
    const float ql = __builtin_fmaf(q, CL, __builtin_fmaf(q, CH, -qh));
    if (wid < 2) {
      cf Z[16];
      auto put = [&](int i, cf v) { *(u32 LAS*)(LO + i * KG_S + n * 4) = pk(v.re, v.im); };
      Z[0] = {1.f, 0.f};
      Z[1] = zpow2<3>(p2, qh, ql);
      Z[2] = zpow2<4>(p2, qh, ql);
      Z[4] = zpow2<5>(p2, qh, ql);
      Z[8] = zpow2<6>(p2, qh, ql);
      cf a7 = zpow2<7>(p2, qh, ql);
      Z[3] = cmul(Z[2], Z[1]);
#pragma unroll
      for (int i = 1; i < 4; ++i) Z[4 + i] = cmul(Z[4], Z[i]);
#pragma unroll
      for (int i = 1; i < 8; ++i) Z[8 + i] = cmul(Z[8], Z[i]);
#pragma unroll
      for (int i = 0; i < 16; ++i) put(i, Z[i]);
      put(16, a7);
#pragma unroll
      for (int i = 1; i < 16; ++i) put(16 + i, cmul(a7, Z[i]));
    } else {
      cf H[16];
      auto put = [&](int i, cf v) { *(u32 LAS*)(HI + i * KG_S + n * 4) = pk(v.re, -v.im); };
      cf dB = {dt * b2f(Bre[idx]), dt * b2f(Bim[idx])};
      cf Cc = {b2f(Cre[idx]), b2f(Cim[idx])};
      H[0] = cmul(Cc, dB);
      cf a0 = zpow2<0>(p2, qh, ql), a1 = zpow2<1>(p2, qh, ql), a2 = zpow2<2>(p2, qh, ql);
      cf a8 = zpow2<8>(p2, qh, ql), a9 = zpow2<9>(p2, qh, ql), a10 = zpow2<10>(p2, qh, ql), a11 = zpow2<11>(p2, qh, ql);
      H[1] = cmul(a0, H[0]);
      H[2] = cmul(a1, H[0]);
      H[3] = cmul(a1, H[1]);
#pragma unroll
      for (int i = 0; i < 4; ++i) H[4 + i] = cmul(a2, H[i]);
#pragma unroll
      for (int i = 0; i < 4; ++i) H[8 + i] = cmul(a8, H[2 * i]);
      H[12] = cmul(a9, H[0]);
      H[13] = cmul(a9, H[4]);
      H[14] = cmul(a9, H[8]);
      H[15] = cmul(a9, H[10]);
#pragma unroll
      for (int i = 0; i < 16; ++i) put(i, H[i]);
      put(16, cmul(a10, H[0]));
      put(17, cmul(a10, H[8]));
      put(18, cmul(a10, H[12]));
      put(19, cmul(a10, H[14]));
      put(20, cmul(a11, H[0]));
    }
    __syncthreads();
    {
      const int c = lane & 15, g = lane >> 4;
      const int hrow = min(16 * (wid >> 1) + c, 20), lrow = 16 * (wid & 1) + c;
      f32x4 acc = {0.f, 0.f, 0.f, 0.f};
#pragma unroll
      for (int k = 0; k < 8; ++k) {
        u32x4 a = *(u32x4 LAS*)(HI + hrow * KG_S + k * 64 + g * 16);
        u32x4 b = *(u32x4 LAS*)(LO + lrow * KG_S + k * 64 + g * 16);
        acc = mfma16(a, b, acc);
      }
#pragma unroll
      for (int e = 0; e < 4; ++e) {
        const int hi = 16 * (wid >> 1) + 4 * g + e;
        const int hv = hi < 8 ? hi : hi < 12 ? 256 + 2 * (hi - 8) : hi < 16 ? 512 + 256 * ((hi >> 1) & 1) + 4 * (hi & 1) : 1024 + 256 * (hi - 16);
        if (hi < 20 || (hi == 20 && lrow == 0)) *(u16 LAS*)(TB + 2 * kpos(hv + 8 * lrow)) = (u16)pk(acc[e], 0.f);
      }
    }
    __syncthreads();
    {
      u32 kv[9];
#pragma unroll
      for (int e = 0; e < 9; ++e) {
        const int j = 8 * tid + 7 - e;
        kv[e] = *(u16 LAS*)(TB + 2 * kpos(max(j, 0)));
      }
      if (tid == 0) kv[8] = 0u;
      char* ko = ws + WS_KIM + (size_t)d * KIM_B;
      *(u32x4*)(ko + (28 + tid) * 16) = u32x4{kv[1] | (kv[2] << 16), kv[3] | (kv[4] << 16), kv[5] | (kv[6] << 16), kv[7] | (kv[8] << 16)};
      *(u32x4*)(ko + KIM_C1 + (28 + tid) * 16) = u32x4{kv[0] | (kv[1] << 16), kv[2] | (kv[3] << 16), kv[4] | (kv[5] << 16), kv[6] | (kv[7] << 16)};
      if (tid < 64) {
        const int ci = tid < 28 ? tid : tid < 62 ? 256 + tid : 512 + tid;
        *(u32x4*)(ko + ci * 16) = u32x4{0u, 0u, 0u, 0u};
      }
    }
  }
  STAMP_AT(0, 1);
}

#define H_AUX 0
constexpr int SLOT = 9216;
constexpr int G1_PF = 6, G1_R = 9;
constexpr int UIM_B = 4160;
constexpr int L2_AUX = G1_R * SLOT;
constexpr int L2_UIM = L2_AUX + 4 * KIM_B;
constexpr int L2_CNT = L2_UIM + 4 * UIM_B;
constexpr int L2_END = L2_CNT + 16;

__device__ __forceinline__ float gelu(float v) {
  constexpr float C0 = -2.f * 1.44269504088896340736f * 0.7978845608028654f, C1 = C0 * 0.044715f;
  float z = v * __builtin_fmaf(v * v, C1, C0);
  return v * __builtin_amdgcn_rcpf(1.f + __builtin_amdgcn_exp2f(z));
}
__device__ __forceinline__ u32x2 gelu4(f32x4 a) { return u32x2{pk(gelu(a[0]), gelu(a[1])), pk(gelu(a[2]), gelu(a[3]))}; }

constexpr int ld_younger(int j, int pf, int last) {
  int c = 0;
  for (int i = j + 2; i <= last && i <= j - 2 + pf; i += 2) ++c;
  return c;
}

__global__ __launch_bounds__(512, 2) void k_ffn1(const u16* __restrict__ w1, const u16* __restrict__ b1, const u16* __restrict__ w2,
                                                 const u16* __restrict__ Dskip, char* __restrict__ ws) {
  extern __shared__ __attribute__((aligned(16))) char smem[];
  char LAS* sm = (char LAS*)smem;
  const int tid = threadIdx.x, lane = tid & 63, wid = __builtin_amdgcn_readfirstlane(tid >> 6);
  const int xg = blockIdx.x & 7, idx = blockIdx.x >> 3;
  const int lb = 4 * (xg >> 2) + (idx >> 3), fb = 8 * (xg & 3) + (idx & 7);
  const int c = lane & 15, g = lane >> 4;
  STAMP_AT(1, 0);
  if (wid < 4) {
    __builtin_amdgcn_s_setprio(2);
    constexpr int XP = 3, WD = 3, WR = 4;
    const int lt0 = 16 * lb + 4 * wid;
    const __amdgpu_buffer_rsrc_t rx = __builtin_amdgcn_make_buffer_rsrc(ws + WS_XNF + (size_t)lt0 * 18432, 0, 4 * 18432, 0x00020000);
    u32x4 xf[XP + 1][4];
#pragma unroll
    for (int k = 0; k < XP; ++k)
#pragma unroll
      for (int i = 0; i < 4; ++i) xf[k][i] = __builtin_amdgcn_raw_buffer_load_b128(rx, lane * 16, i * 18432 + k * 1024, 0);
    f32x4 acc[9][4];
#pragma unroll
    for (int t = 0; t < 9; ++t) {
      u32x2 b = *(const u32x2*)(b1 + fb * 144 + 16 * t + 4 * g);
      f32x4 bv = {bl(b[0]), bh(b[0]), bl(b[1]), bh(b[1])};
#pragma unroll
      for (int i = 0; i < 4; ++i) acc[t][i] = bv;
    }
    const char LAS* wp = sm + (lane >> 2) * 288 + (lane & 3) * 8;
    u32x4 wf[WR];
    auto wread = [&](int n) {
      const int ks = n / 9, t = n % 9;
      const char LAS* p = wp + (ks % G1_R) * SLOT + t * 32;
      u32x2 a = tr_read(p), b = tr_read(p + 16 * 288);
      wf[n % WR] = u32x4{a[0], a[1], b[0], b[1]};
    };
    bar();
    STAMP_AT(1, 1);
#pragma unroll
    for (int n = 0; n < WD; ++n) wread(n);
#pragma unroll
    for (int ks = 0; ks < 18; ++ks) {
      if (ks < 17) { waitl<2 * WD>(); bar(); }
#pragma unroll
      for (int t = 0; t < 9; ++t) {
        const int n = 9 * ks + t;
        if (n + WD < 162) wread(n + WD);
        if (t < 4 && ks + XP < 18) xf[(ks + XP) % (XP + 1)][t] = __builtin_amdgcn_raw_buffer_load_b128(rx, lane * 16, t * 18432 + (ks + XP) * 1024, 0);
#pragma unroll
        for (int i = 0; i < 4; ++i) acc[t][i] = mfma16(wf[n % WR], xf[ks % (XP + 1)][i], acc[t][i]);
        __builtin_amdgcn_sched_barrier(0);
      }
    }
    STAMP_AT(1, 2);
    __builtin_amdgcn_s_setprio(0);
    const __amdgpu_buffer_rsrc_t rh = __builtin_amdgcn_make_buffer_rsrc(ws + WS_HF + ((size_t)lt0 * 144 + 9 * (fb >> 1)) * 1024, 0, 4 * 147456, 0x00020000);
#pragma unroll
    for (int i = 0; i < 4; ++i)
#pragma unroll
      for (int r = 0; r < 4; ++r) {
        u32x2 a = gelu4(acc[2 * r][i]), b = gelu4(acc[2 * r + 1][i]);
        __builtin_amdgcn_raw_buffer_store_b128(u32x4{a[0], a[1], b[0], b[1]}, rh, lane * 16, i * 147456 + (4 * (fb & 1) + r) * 1024, H_AUX);
      }
#pragma unroll
    for (int i = 0; i < 2; ++i) {
      u32x2 a = gelu4(acc[8][2 * i]), b = gelu4(acc[8][2 * i + 1]);
      __builtin_amdgcn_raw_buffer_store_b128(u32x4{a[0], a[1], b[0], b[1]}, rh, lane * 16, (2 * i + (fb & 1)) * 147456 + 8 * 1024, H_AUX);
    }
    STAMP_AT(1, 3);
  } else {
    const bool conv = idx < 18;
    const int Q = xg * 18 + idx;
    if (wid < 6) {
      const int p = wid - 4;
      u32 voff[9];
#pragma unroll
      for (int n = 0; n < 9; ++n) { const int ci = 64 * n + lane; voff[n] = (ci / 18) * (F * 2) + (ci % 18) * 16; }
      const char* wsrc = (const char*)(w1 + fb * 144) + (size_t)p * 32 * F * 2;
      auto issue = [&](int j2, int slot) {
        const char* s = wsrc + (size_t)j2 * 64 * F * 2;
#pragma unroll
        for (int n = 0; n < 9; ++n) glds16(s + voff[n], sm + slot * SLOT + n * 1024);
      };
#pragma unroll
      for (int j = 0; j < G1_PF - 1; ++j)
        if ((j & 1) == p) issue(j >> 1, j % G1_R);
      static_for<-1, 17>([&](auto K) {
        constexpr int ks = decltype(K)::value;
        if (((ks + 1) & 1) == p) waitv<9 * ld_younger(ks + 1, G1_PF, 17)>();
        bar();
        constexpr int j = ks + G1_PF;
        if (j < 18 && (j & 1) == p) issue(j >> 1, j % G1_R);
      });
    } else if (conv) {
      const int p = wid - 6;
      bar(); bar();
      u32x4 uv[8];
      const char* us = ws + WS_UT2 + (size_t)(64 * p + (lane >> 3)) * 18432 + Q * 128 + (lane & 7) * 16;
#pragma unroll
      for (int i = 0; i < 8; ++i) uv[i] = *(const u32x4*)(us + (size_t)i * 8 * 18432);
      bar(); bar(); bar();
#pragma unroll
      for (int i = 0; i < 8; ++i)
        *(u32x4 LAS*)(sm + L2_UIM + ((lane >> 1) & 3) * UIM_B + 64 + (64 * p + 8 * i + (lane >> 3)) * 32 + (lane & 1) * 16) = uv[i];
      if (lane < 16) *(u32x4 LAS*)(sm + L2_UIM + (lane >> 2) * UIM_B + (lane & 3) * 16) = u32x4{0u, 0u, 0u, 0u};
      if (lane == 0) *(u32 LAS*)(sm + L2_CNT) = 0u;
      bar();
      const char* ks = ws + WS_KIM + (size_t)(4 * Q + 2 * p) * KIM_B + lane * 16;
#pragma unroll
      for (int i = 0; i < 18; ++i) {
        glds16(ks + i * 1024, sm + L2_AUX + 2 * p * KIM_B + i * 1024);
        if (i % 6 == 5) bar();
      }
      bar(); bar(); bar(); bar();
      waitv<0>(); waitl<0>();
      bar(); bar(); bar(); bar(); bar();
    } else {
      const int p = wid - 6;
      const int u0 = (((xg >> 2) * 14 + (idx - 18)) * 2 + p) * 6;
      char LAS* sc = sm + L2_AUX + p * 6 * 4096;
      const int r8 = lane >> 3, pos = lane & 7;
      const u32 voff = r8 * (D * 2) + (((pos >> 1) ^ (r8 >> 1)) * 2 + (pos & 1)) * 16;
      bar(); bar();
#pragma unroll
      for (int i = 0; i < 6; ++i) {
        const int u = min(u0 + i, 323), kk = 36 * (xg & 3) + u / 9, dg = u % 9, P = kk / 9, r = kk % 9;
        const int fA = 288 * P + (r < 4 ? 32 * r : r < 8 ? 144 + 32 * (r - 4) : 128), fB = r < 8 ? fA + 16 : fA + 144;
#pragma unroll
        for (int n = 0; n < 4; ++n)
          glds16((const char*)(w2 + (size_t)((n < 2 ? fA : fB) + 8 * (n & 1)) * D + dg * 64) + voff, sc + i * 4096 + n * 1024);
        if (i & 1) bar();
      }
      bar(); bar(); bar(); bar();
      waitv<0>();
      const int rr = lane >> 2;
#pragma unroll
      for (int i = 0; i < 6; ++i) {
        const int u = min(u0 + i, 323), kk = 36 * (xg & 3) + u / 9, dg = u % 9;
        char* dst = ws + WS_W2P + ((size_t)kk * 36 + dg * 4) * 1024 + lane * 16;
#pragma unroll
        for (int t = 0; t < 4; ++t) {
          const char LAS* tp = sc + i * 4096 + rr * 128 + ((t ^ ((rr >> 1) & 3)) * 32) + (lane & 3) * 8;
          u32x2 a = tr_read(tp), b = tr_read(tp + 2048);
          *(u32x4*)(dst + t * 1024) = u32x4{a[0], a[1], b[0], b[1]};
        }
        bar();
      }
      bar(); bar(); bar();
    }
    STAMP_AT(1, 2);
    if (conv) {
      const int h = wid - 4;
      const char LAS* kp = sm + L2_AUX + h * KIM_B + (c & 1) * KIM_C1 + (4 * (c >> 1) + g) * 16;
      char LAS* ub = sm + L2_UIM + h * UIM_B + 64;
      const char LAS* up = ub + 2 * (8 * (28 + (c >> 2) - g) + 2 * (c & 3) - 6);
      f32x4 acc[8];
#pragma unroll
      for (int T = 0; T < 8; ++T) acc[T] = f32x4{0.f, 0.f, 0.f, 0.f};
#pragma unroll 2
      for (int be = 0; be < 8; ++be) {
        u32x4 kf[8], uf[8];
#pragma unroll
        for (int a = 0; a < 8; ++a) {
          kf[a] = *(const u32x4 LAS*)(kp + be * 64 + a * 512);
          const u32 LAS* q = (const u32 LAS*)(up - be * 64 + a * 512);
          uf[a] = u32x4{q[0], q[1], q[2], q[3]};
        }
#pragma unroll
        for (int T = 0; T < 8; ++T)
#pragma unroll
          for (int a = 0; a <= T; ++a) acc[T] = mfma16(uf[T - a], kf[a], acc[T]);
      }
      const float dsk = b2f(Dskip[4 * Q + h]);
      const bool odd = c & 1;
#pragma unroll
      for (int T = 0; T < 8; ++T) {
        const u32 p01 = pk(acc[T][0], acc[T][1]), p23 = pk(acc[T][2], acc[T][3]);
        const u32 own = odd ? p23 : p01;
        const u32 rcv = (u32)__builtin_amdgcn_mov_dpp((int)(odd ? p01 : p23), 0xB1, 0xF, 0xF, true);
        const u32 ev = odd ? rcv : own, od = odd ? own : rcv;
        char LAS* yp = ub + 2 * (8 * (32 * T + g + 4 * (c >> 1)) + 4 * (c & 1));
        const u32x2 uu = *(const u32x2 LAS*)yp;
        const u32 s0 = pk(bl(uu[0]) * dsk, bh(uu[0]) * dsk), s1 = pk(bl(uu[1]) * dsk, bh(uu[1]) * dsk);
        *(u32x2 LAS*)yp = u32x2{pk(bl(ev) + bl(s0), bl(od) + bh(s0)), pk(bh(ev) + bl(s1), bh(od) + bh(s1))};
      }
      waitl<0>();
      volatile u32 LAS* cnt = (volatile u32 LAS*)(sm + L2_CNT);
      if (lane == 0) __hip_atomic_fetch_add((u32 LAS*)(sm + L2_CNT), 1u, __ATOMIC_RELAXED, __HIP_MEMORY_SCOPE_WORKGROUP);
      while (*cnt < 4u) __builtin_amdgcn_s_sleep(1);
      asm volatile("" ::: "memory");
      char* ao = ws + WS_A2 + (size_t)Q * 16384 + (512 * h + lane) * 8;
      const char LAS* tp = sm + L2_UIM + (c >> 2) * UIM_B + 64 + 2 * (512 * h + 16 * g) + (c & 3) * 8;
#pragma unroll
      for (int i = 0; i < 8; ++i) *(u32x2*)(ao + i * 512) = tr_read(tp + i * 128);
    }
    STAMP_AT(1, 3);
  }
}

constexpr int G2_PF = 5, G2_R = 7;
constexpr int L3_OWN = G2_R * 2 * SLOT;
constexpr int L3_END = L3_OWN + SLAB_B;

__global__ __launch_bounds__(512, 2) void k_ffn2(const u16* __restrict__ x, const u16* __restrict__ b2, char* __restrict__ ws,
                                                 u16* __restrict__ out) {
  extern __shared__ __attribute__((aligned(16))) char smem[];
  char LAS* sm = (char LAS*)smem;
  const int tid = threadIdx.x, lane = tid & 63, wid = __builtin_amdgcn_readfirstlane(tid >> 6);
  const int xg = blockIdx.x & 7, idx = blockIdx.x >> 3;
  const int s = xg & 3, lb = 8 * (xg >> 2) + (idx >> 2), dj = idx & 3, tile = lb * 4 + dj;
  const int c = lane & 15, g = lane >> 4;
  const __amdgpu_buffer_rsrc_t rs = __builtin_amdgcn_make_buffer_rsrc(ws + WS_SLAB + (size_t)tile * 16 * SLAB_B, 0, 16 * SLAB_B, 0x00020000);
  STAMP_AT(2, 0);
  if (wid < 4) {
    __builtin_amdgcn_s_setprio(2);
    constexpr int XP = 6, WD = 4, WR = 5;
    const __amdgpu_buffer_rsrc_t rh = __builtin_amdgcn_make_buffer_rsrc(ws + WS_HF + ((size_t)(8 * lb + 2 * wid) * 144 + 36 * s) * 1024, 0, 2 * 147456, 0x00020000);
    u32x4 xf[XP + 1][2];
#pragma unroll
    for (int k = 0; k < XP; ++k)
#pragma unroll
      for (int i = 0; i < 2; ++i) xf[k][i] = __builtin_amdgcn_raw_buffer_load_b128(rh, lane * 16, i * 147456 + k * 1024, 0);
    f32x4 acc[9][2];
#pragma unroll
    for (int t = 0; t < 9; ++t) acc[t][0] = acc[t][1] = f32x4{0.f, 0.f, 0.f, 0.f};
    const char LAS* wp = sm + lane * 16;
    u32x4 wf[WR];
    auto wread = [&](int n) { wf[n % WR] = *(const u32x4 LAS*)(wp + (n % (18 * G2_R)) * 1024); };
    bar();
    STAMP_AT(2, 1);
#pragma unroll
    for (int n = 0; n < WD; ++n) wread(n);
#pragma unroll
    for (int ks = 0; ks < 36; ++ks) {
      if ((ks & 1) == 0 && ks < 34) { waitl<WD>(); bar(); }
      u32x4 x0 = xf[ks % (XP + 1)][0], x1 = xf[ks % (XP + 1)][1];
      if (ks % 9 == 8) {
        const u32x4 a = x0, b = x1;
        x0 = u32x4{a[0], a[1], b[0], b[1]};
        x1 = u32x4{a[2], a[3], b[2], b[3]};
      }
#pragma unroll
      for (int t = 0; t < 9; ++t) {
        const int n = 9 * ks + t;
        if (n + WD < 324) wread(n + WD);
        if (t < 2 && ks + XP < 36) xf[(ks + XP) % (XP + 1)][t] = __builtin_amdgcn_raw_buffer_load_b128(rh, lane * 16, t * 147456 + (ks + XP) * 1024, 0);
        acc[t][0] = mfma16(wf[n % WR], x0, acc[t][0]);
        acc[t][1] = mfma16(wf[n % WR], x1, acc[t][1]);
        __builtin_amdgcn_sched_barrier(0);
      }
    }
    STAMP_AT(2, 2);
    if (wid == s) {
#pragma unroll
      for (int i = 0; i < 2; ++i)
#pragma unroll
        for (int t = 0; t < 9; ++t) *(f32x4 LAS*)(sm + L3_OWN + (i * 9 + t) * 1024 + lane * 16) = acc[t][i];
    } else {
#pragma unroll
      for (int i = 0; i < 2; ++i)
#pragma unroll
        for (int t = 0; t < 9; ++t)
          __builtin_amdgcn_raw_buffer_store_b128(__builtin_bit_cast(u32x4, acc[t][i]), rs, lane * 16, (wid * 4 + s) * SLAB_B + (i * 9 + t) * 1024, 16);
    }
  } else {
    const int p = wid - 4;
    const char* wsrc = ws + WS_W2P + ((size_t)(36 * s + (p & 1)) * 36 + 9 * dj) * 1024 + lane * 16;
    auto issue = [&](int j) {
#pragma unroll
      for (int n = 0; n < 9; ++n) glds16(wsrc + (size_t)j * 73728 + n * 1024, sm + ((j % G2_R) * 2 + (p & 1)) * SLOT + n * 1024);
    };
    const int par = p >> 1;
#pragma unroll
    for (int j = 0; j < G2_PF - 1; ++j)
      if ((j & 1) == par) issue(j);
    static_for<-1, 17>([&](auto K) {
      constexpr int st = decltype(K)::value;
      if (((st + 1) & 1) == par) waitv<9 * ld_younger(st + 1, G2_PF, 17)>();
      bar();
      constexpr int j = st + G2_PF;
      if (j < 18 && (j & 1) == par) issue(j);
    });
  }
  u32x2 base[3], bias[3];
#pragma unroll
  for (int k = 0; k < 3; ++k) {
    const int it = min(wid + 8 * k, 17), i = it / 9, t = it % 9;
    const int l = 128 * lb + 32 * s + 16 * i + c, d = 144 * dj + 16 * t + 4 * g;
    const u32x2 xv = *(const u32x2*)(x + (size_t)l * D + d);
    const u32x2 av = *(const u32x2*)(ws + WS_A2 + ((size_t)(d >> 2) * L + l) * 8);
    bias[k] = *(const u32x2*)(b2 + d);
    base[k] = u32x2{pk(bl(xv[0]) + bl(av[0]), bh(xv[0]) + bh(av[0])), pk(bl(xv[1]) + bl(av[1]), bh(xv[1]) + bh(av[1]))};
  }
  waitv<0>(); waitl<0>();
  bar();
  STAMP_AT(2, 3);
  if (tid == 0) {
    GAS u32* cnt = (GAS u32*)(ws + WS_CNT + tile * 128);
    __hip_atomic_fetch_add(cnt, 1u, __ATOMIC_RELAXED, __HIP_MEMORY_SCOPE_AGENT);
    for (u32 spin = 0; spin < (1u << 22); ++spin) {
      if (__hip_atomic_load(cnt, __ATOMIC_RELAXED, __HIP_MEMORY_SCOPE_AGENT) >= 4u) break;
      __builtin_amdgcn_s_sleep(1);
    }
  }
  bar();
  STAMP_AT(2, 4);
  f32x4 part[3][4];
#pragma unroll
  for (int k = 0; k < 3; ++k) {
    const int it = min(wid + 8 * k, 17);
#pragma unroll
    for (int e = 1; e < 4; ++e)
      part[k][e] = __builtin_bit_cast(f32x4, __builtin_amdgcn_raw_buffer_load_b128(rs, lane * 16, (s * 4 + ((s + e) & 3)) * SLAB_B + it * 1024, 16));
    part[k][0] = *(const f32x4 LAS*)(sm + L3_OWN + it * 1024 + lane * 16);
  }
#pragma unroll
  for (int k = 0; k < 3; ++k) {
    const int it = wid + 8 * k, i = it / 9, t = it % 9;
    if (it < 18) {
      const f32x4 v = (part[k][0] + part[k][1]) + (part[k][2] + part[k][3]);
      const u32 r0 = pk(v[0], v[1]), r1 = pk(v[2], v[3]);
      const u32 q0 = pk(bl(r0) + bl(bias[k][0]), bh(r0) + bh(bias[k][0])), q1 = pk(bl(r1) + bl(bias[k][1]), bh(r1) + bh(bias[k][1]));
      const int l = 128 * lb + 32 * s + 16 * i + c, d = 144 * dj + 16 * t + 4 * g;
      *(u32x2*)(out + (size_t)l * D + d) =
          u32x2{pk(bl(base[k][0]) + bl(q0), bh(base[k][0]) + bh(q0)), pk(bl(base[k][1]) + bl(q1), bh(base[k][1]) + bh(q1))};
    }
  }
  STAMP_AT(2, 5);
}

extern "C" void kernel_launch(void* const* d_in, const int* in_sizes, int n_in, void* d_out, int out_size, void* d_ws,
                              size_t ws_size, hipStream_t stream) {
  const u16* const* in = (const u16* const*)d_in;
  char* ws = (char*)d_ws;
  (void)hipFuncSetAttribute((const void*)k_ffn1, hipFuncAttributeMaxDynamicSharedMemorySize, L2_END);
  (void)hipFuncSetAttribute((const void*)k_ffn2, hipFuncAttributeMaxDynamicSharedMemorySize, L3_END);
  k_prep<<<128 + D, 256, 0, stream>>>(in[0], in[1], in[2], in[3], in[4], in[5], in[6], in[7], ws);
  k_ffn1<<<256, 512, L2_END, stream>>>(in[10], in[11], in[12], in[8], ws);
  k_ffn2<<<256, 512, L3_END, stream>>>(in[0], in[13], ws, (u16*)d_out);
}
```

```cpp
#include <hip/hip_runtime.h>
#include <hip/hip_bf16.h>
#include <stdint.h>
#include <type_traits>

typedef unsigned short u16;
typedef unsigned int u32;
typedef unsigned long long u64;
typedef __bf16 bf16x8 __attribute__((ext_vector_type(8)));
typedef __bf16 bf16x2 __attribute__((ext_vector_type(2)));
typedef short s16x4 __attribute__((ext_vector_type(4)));
typedef float f32x4 __attribute__((ext_vector_type(4)));
typedef float f32x2 __attribute__((ext_vector_type(2)));
typedef u32 u32x4 __attribute__((ext_vector_type(4)));
typedef u32 u32x2 __attribute__((ext_vector_type(2)));
#define LAS __attribute__((address_space(3)))
#define GAS __attribute__((address_space(1)))
#define SB __builtin_amdgcn_sched_barrier(0)

constexpr int L = 2048, D = 576, NM = 128, F = 4608;

constexpr size_t WS_CNT = 0;
constexpr int CNT_X = 0, CNT_H = 64, CNT_P = 96, CNT_A = 100, CNT_N = 104;
constexpr size_t WS_XNF = 16384;
constexpr size_t WS_UT2 = WS_XNF + (size_t)L * D * 2;
constexpr int KT_B = 1536;
constexpr size_t WS_KT = WS_UT2 + (size_t)L * D * 2;
constexpr size_t WS_A2 = WS_KT + (size_t)D * KT_B;
constexpr size_t WS_W2P = WS_A2 + (size_t)L * D * 2;
constexpr size_t WS_HF = WS_W2P + (size_t)F * D * 2;
constexpr size_t WS_SLAB = WS_HF + (size_t)L * F * 2;
constexpr int SLAB_B = 9216;
constexpr size_t WS_END = WS_SLAB + (size_t)64 * 16 * SLAB_B;
#define STAMP_AT(kern, slot)
#define LSTAMP(i)
#define ST_AUX 16

__device__ __forceinline__ float bl(u32 v) { return __uint_as_float(v << 16); }
__device__ __forceinline__ float bh(u32 v) { return __uint_as_float(v & 0xffff0000u); }
__device__ __forceinline__ float b2f(u16 v) { return __uint_as_float(((u32)v) << 16); }
__device__ __forceinline__ u32 pk(float lo, float hi) {
  bf16x2 r = {(__bf16)lo, (__bf16)hi};
  return __builtin_bit_cast(u32, r);
}
__device__ __forceinline__ float rb(float f) { return (float)(__bf16)f; }
__device__ __forceinline__ f32x4 mfma16(u32x4 a, u32x4 b, f32x4 c) {
  return __builtin_amdgcn_mfma_f32_16x16x32_bf16(__builtin_bit_cast(bf16x8, a), __builtin_bit_cast(bf16x8, b), c, 0, 0, 0);
}
__device__ __forceinline__ u32x2 tr_read(const char LAS* p) {
  return __builtin_bit_cast(u32x2, __builtin_amdgcn_ds_read_tr16_b64_v4i16((s16x4 LAS*)p));
}
__device__ __forceinline__ __amdgpu_buffer_rsrc_t rsrc(const void* p, int bytes) {
  return __builtin_amdgcn_make_buffer_rsrc((void*)p, 0, bytes, 0x00020000);
}
template <int AUX> __device__ __forceinline__ void st128(u32x4 v, __amdgpu_buffer_rsrc_t r, int voff, int soff) {
  __builtin_amdgcn_raw_buffer_store_b128(v, r, voff, soff, AUX);
  asm volatile("s_nop 1" ::"v"(v) : "memory");
}
__device__ __forceinline__ void glds16(const char* base, u32 voff, u32 dst) {
  asm volatile("s_mov_b32 m0, %2\n\ts_nop 0\n\tglobal_load_lds_dwordx4 %0, %1" ::"v"(voff), "s"(base), "s"(dst) : "memory");
}
__device__ __forceinline__ void glds16c(const char* base, u32 voff, u32 dst) {
  asm volatile("s_mov_b32 m0, %2\n\ts_nop 0\n\tglobal_load_lds_dwordx4 %0, %1 sc1" ::"v"(voff), "s"(base), "s"(dst) : "memory");
}
template <int N> __device__ __forceinline__ void waitv() { asm volatile("s_waitcnt vmcnt(%0)" ::"i"(N) : "memory"); }
template <int N> __device__ __forceinline__ void waitl() { asm volatile("s_waitcnt lgkmcnt(%0)" ::"i"(N) : "memory"); }
__device__ __forceinline__ void bar() {
  asm volatile("" ::: "memory");
  __builtin_amdgcn_s_barrier();
  asm volatile("" ::: "memory");
}
template <int N> __device__ __forceinline__ void bars() {
#pragma unroll
  for (int i = 0; i < N; ++i) bar();
}
template <int I, int N, class Fn> __device__ __forceinline__ void static_for(Fn&& fn) {
  if constexpr (I < N) {
    fn(std::integral_constant<int, I>{});
    static_for<I + 1, N>(fn);
  }
}

__device__ __forceinline__ f32x2 cmul(f32x2 a, f32x2 b) {
  return __builtin_elementwise_fma(f32x2{-a[1], a[1]}, f32x2{b[1], b[0]}, f32x2{a[0], a[0]} * b);
}
template <int E> __device__ __forceinline__ f32x2 zpow2(float p2, float qh, float ql) {
  constexpr float s = (float)(1 << E);
  const float mag = __builtin_amdgcn_exp2f(p2 * s);
  const float ph = __builtin_fmaf(ql, s, __builtin_amdgcn_fractf(qh * s));
  return f32x2{__builtin_amdgcn_cosf(ph), __builtin_amdgcn_sinf(ph)} * f32x2{mag, mag};
}
__device__ __forceinline__ int kpos(int j) {
  const int r = (int)rb((float)j);
  const int hi = r < 256 ? (r & 7) : r < 512 ? 8 + ((r >> 1) & 3) : r < 1024 ? 8 + ((r >> 7) & 6) + ((r >> 2) & 1) : 12 + (r >> 8);
  return hi * 64 + ((r >> 2) & 62);
}

constexpr int LN_S = 1168, KG_S = 528, KG_B = 53 * KG_S;
template <int CTRL> __device__ __forceinline__ float dpp_add(float v) {
  return v + __builtin_bit_cast(float, __builtin_amdgcn_update_dpp(0, __builtin_bit_cast(int, v), CTRL, 0xF, 0xF, true));
}
__device__ __forceinline__ float sum32(float v) {
  v = dpp_add<0x128>(v);
  v = dpp_add<0x124>(v);
  v = dpp_add<0x122>(v);
  v = dpp_add<0x121>(v);
  return v + __builtin_bit_cast(float, __builtin_amdgcn_ds_swizzle(__builtin_bit_cast(int, v), 0x401F));
}
__global__ __launch_bounds__(1024) void k_prep(const u16* __restrict__ x, const u16* __restrict__ Are, const u16* __restrict__ Aim,
                                               const u16* __restrict__ Bre, const u16* __restrict__ Bim, const u16* __restrict__ Cre,
                                               const u16* __restrict__ Cim, const u16* __restrict__ logdt, char* __restrict__ ws) {
  __shared__ __attribute__((aligned(16))) char smem[8 * LN_S + 3 * KG_B];
  char LAS* sm = (char LAS*)smem;
  const int tid = threadIdx.x, lane = tid & 63, wid = __builtin_amdgcn_readfirstlane(tid >> 6);
  const int b = blockIdx.x;
  STAMP_AT(0, 0);
  if (wid < 4) {
    __builtin_amdgcn_s_setprio(2);
    const int r = tid >> 5, lam = tid & 31;
    const u32* src = (const u32*)(x + (size_t)(b * 8 + r) * D) + lam;
    f32x2 v[9];
#pragma unroll
    for (int i = 0; i < 9; ++i) {
      const u32 t = src[32 * i];
      v[i] = f32x2{bl(t), bh(t)};
    }
    if (b == 0 && tid < CNT_N) *(u32*)(ws + WS_CNT + tid * 128) = 0u;
    f32x2 s2 = v[0];
#pragma unroll
    for (int i = 1; i < 9; ++i) s2 += v[i];
    const float mean = sum32(s2[0] + s2[1]) * (1.f / D);
    STAMP_AT(0, 2);
    f32x2 q2 = {0.f, 0.f};
#pragma unroll
    for (int i = 0; i < 9; ++i) { f32x2 d = v[i] - f32x2{mean, mean}; q2 = __builtin_elementwise_fma(d, d, q2); }
    const float q = sum32(q2[0] + q2[1]);
    const float m = rb(mean);
    const float sd = rb(__builtin_sqrtf(rb(rb(q * (1.f / D)) + 1e-5f)));
    const float rs = __builtin_amdgcn_rcpf(sd);
#pragma unroll
    for (int i = 0; i < 9; ++i) {
      const f32x2 d = v[i] - f32x2{m, m};
      const u32 t = pk(d[0], d[1]);
      const f32x2 n = f32x2{bl(t), bh(t)} * f32x2{rs, rs};
      *(u32 LAS*)(sm + r * LN_S + (lam + 32 * i) * 4) = pk(n[0], n[1]);
    }
    waitl<0>();
    bar();
    STAMP_AT(0, 3);
    const __amdgpu_buffer_rsrc_t xo = rsrc(ws + WS_XNF + (size_t)(b >> 1) * 18432 + (b & 1) * 128, 18432);
    const __amdgpu_buffer_rsrc_t uo = rsrc(ws + WS_UT2 + (size_t)b * 9216, 9216);
    const char LAS* xp = sm + (lane & 7) * LN_S + (lane >> 5) * 64 + ((lane >> 3) & 3) * 8;
    const char LAS* tp = sm + ((lane >> 2) & 3) * LN_S + (lane >> 4) * 32 + (lane & 3) * 8;
    const int xoff = (lane >> 5) * 1024 + ((lane >> 3) & 3) * 256 + (lane & 7) * 16;
#pragma unroll
    for (int kk = 0; kk < 3; ++kk) {
      const int k = wid + 4 * kk;
      if (k < 9) {
        const u32x2 a = *(const u32x2 LAS*)(xp + k * 128), bb = *(const u32x2 LAS*)(xp + k * 128 + 32);
        const u32x2 t0 = tr_read(tp + k * 128), t1 = tr_read(tp + k * 128 + 4 * LN_S);
        st128<ST_AUX>(u32x4{a[0], a[1], bb[0], bb[1]}, xo, xoff, k * 2048);
        st128<ST_AUX>(u32x4{t0[0], t0[1], t1[0], t1[1]}, uo, lane * 16, k * 1024);
      }
    }
  } else {
    const int j = (wid >> 2) - 1, w4 = wid & 3;
    const int d = b + 256 * j;
    char LAS* LO = sm + 8 * LN_S + j * KG_B;
    char LAS* HI = LO + 32 * KG_S;
    if (d < D) {
      const int n = (w4 & 1) * 64 + lane, idx = d * NM + n;
      const u16 vdt = logdt[d], var = Are[idx], vai = Aim[idx], vbr = Bre[idx], vbi = Bim[idx], vcr = Cre[idx], vci = Cim[idx];
      asm volatile("" ::"v"(vdt), "v"(var), "v"(vai), "v"(vbr), "v"(vbi), "v"(vcr), "v"(vci));
      const float dt = rb(__expf(b2f(vdt)));
      const float p2 = dt * b2f(var) * 1.44269504088896340736f;
      const float q = dt * b2f(vai);
      constexpr float CH = 0.15915494309189535f;
      constexpr float CL = (float)(0.15915494309189533577 - (double)CH);
      const float qh = q * CH;
      const float ql = __builtin_fmaf(q, CL, __builtin_fmaf(q, CH, -qh));
      STAMP_AT(0, 2);
      if (w4 < 2) {
        f32x2 Z[16];
        auto put = [&](int i, f32x2 v) { *(u32 LAS*)(LO + i * KG_S + n * 4) = pk(v[0], v[1]); };
        Z[0] = f32x2{1.f, 0.f};
        Z[1] = zpow2<3>(p2, qh, ql);
        Z[4] = zpow2<5>(p2, qh, ql);
        const f32x2 a7 = zpow2<7>(p2, qh, ql);
        Z[2] = cmul(Z[1], Z[1]);
        Z[8] = cmul(Z[4], Z[4]);
        Z[3] = cmul(Z[2], Z[1]);
#pragma unroll
        for (int i = 1; i < 4; ++i) Z[4 + i] = cmul(Z[4], Z[i]);
#pragma unroll
        for (int i = 1; i < 8; ++i) Z[8 + i] = cmul(Z[8], Z[i]);
#pragma unroll
        for (int i = 0; i < 16; ++i) put(i, Z[i]);
        put(16, a7);
#pragma unroll
        for (int i = 1; i < 16; ++i) put(16 + i, cmul(a7, Z[i]));
      } else {
        f32x2 H[16];
        auto put = [&](int i, f32x2 v) { *(u32 LAS*)(HI + i * KG_S + n * 4) = pk(v[0], -v[1]); };
        const f32x2 dB = {dt * b2f(vbr), dt * b2f(vbi)};
        const f32x2 Cc = {b2f(vcr), b2f(vci)};
        H[0] = cmul(Cc, dB);
        const f32x2 a0 = zpow2<0>(p2, qh, ql), a8 = zpow2<8>(p2, qh, ql), a10 = zpow2<10>(p2, qh, ql);
        const f32x2 a1 = cmul(a0, a0), a2 = cmul(a1, a1), a9 = cmul(a8, a8), a11 = cmul(a10, a10);
        H[1] = cmul(a0, H[0]);
        H[2] = cmul(a1, H[0]);
        H[3] = cmul(a1, H[1]);
#pragma unroll
        for (int i = 0; i < 4; ++i) H[4 + i] = cmul(a2, H[i]);
#pragma unroll
        for (int i = 0; i < 4; ++i) H[8 + i] = cmul(a8, H[2 * i]);
        H[12] = cmul(a9, H[0]);
        H[13] = cmul(a9, H[4]);
        H[14] = cmul(a9, H[8]);
        H[15] = cmul(a9, H[10]);
#pragma unroll
        for (int i = 0; i < 16; ++i) put(i, H[i]);
        put(16, cmul(a10, H[0]));
        put(17, cmul(a10, H[8]));
        put(18, cmul(a10, H[12]));
        put(19, cmul(a10, H[14]));
        put(20, cmul(a11, H[0]));
      }
    }
    waitl<0>();
    bar();
    STAMP_AT(0, 3);
    if (d < D) {
      const int c = lane & 15, g = lane >> 4;
      const int hi = 16 * (w4 >> 1) + c, hrow = min(hi, 20), lrow = 16 * (w4 & 1) + c;
      f32x4 acc = {0.f, 0.f, 0.f, 0.f};
#pragma unroll
      for (int k = 0; k < 8; ++k) {
        const u32x4 a = *(u32x4 LAS*)(LO + lrow * KG_S + k * 64 + g * 16);
        const u32x4 bb = *(u32x4 LAS*)(HI + hrow * KG_S + k * 64 + g * 16);
        acc = mfma16(a, bb, acc);
      }
      __builtin_amdgcn_raw_buffer_store_b64(u32x2{pk(acc[0], acc[1]), pk(acc[2], acc[3])}, rsrc(ws + WS_KT + (size_t)d * KT_B, KT_B),
                                            hi * 64 + (w4 & 1) * 32 + g * 8, 0, ST_AUX);
    }
  }
  STAMP_AT(0, 1);
}

constexpr int SLOT = 9216, STG = 2 * SLOT;
#define G1_PF 6
#define CONV_PRIO 3
#define G2_PF 6
#define G2_XP 10
#define G2_X0 6
constexpr int G1_R = 11, G2_R = 7;
constexpr int KIM_B = 9216, KIM_C1 = 4640, UIM_B = 4160;
constexpr int LM_AUX = G1_R * SLOT;
constexpr int LM_UIM = LM_AUX + 4 * KIM_B;
constexpr int LM_KT = LM_UIM + 4 * UIM_B;
constexpr int LM_CNT = LM_KT + 4 * KT_B;
constexpr int LM_END = LM_CNT + 16;
constexpr int LM_OWN = G2_R * STG;
static_assert(LM_END <= 163840 && LM_OWN + STG <= LM_CNT && G2_R * STG <= LM_UIM, "LDS map");

__device__ __forceinline__ u32 gelu2(float a, float b) {
  constexpr float C0 = -2.f * 1.44269504088896340736f * 0.7978845608028654f, C1 = C0 * 0.044715f;
  const f32x2 v = {a, b};
  const f32x2 z = v * __builtin_elementwise_fma(v * v, f32x2{C1, C1}, f32x2{C0, C0});
  const f32x2 d = f32x2{__builtin_amdgcn_exp2f(z[0]), __builtin_amdgcn_exp2f(z[1])} + f32x2{1.f, 1.f};
  const f32x2 r = v * f32x2{__builtin_amdgcn_rcpf(d[0]), __builtin_amdgcn_rcpf(d[1])};
  return pk(r[0], r[1]);
}
__device__ __forceinline__ u32x4 gelu8(f32x4 a, f32x4 b) {
  return u32x4{gelu2(a[0], a[1]), gelu2(a[2], a[3]), gelu2(b[0], b[1]), gelu2(b[2], b[3])};
}
__device__ __forceinline__ void cnt_add(char* ws, int i) {
  __hip_atomic_fetch_add((GAS u32*)(ws + WS_CNT + i * 128), 1u, __ATOMIC_RELAXED, __HIP_MEMORY_SCOPE_AGENT);
}
__device__ __forceinline__ void cnt_wait(char* ws, int i, u32 target) {
  GAS u32* p = (GAS u32*)(ws + WS_CNT + i * 128);
  for (u32 spin = 0; spin < (1u << 20); ++spin) {
    const u32 v = __builtin_amdgcn_readfirstlane(__hip_atomic_load(p, __ATOMIC_RELAXED, __HIP_MEMORY_SCOPE_AGENT));
    if (v >= target) break;
    __builtin_amdgcn_s_sleep(1);
  }
  asm volatile("" ::: "memory");
}
__device__ __forceinline__ u32 lds_add(char LAS* p) {
  return __hip_atomic_fetch_add((u32 LAS*)p, 1u, __ATOMIC_RELAXED, __HIP_MEMORY_SCOPE_WORKGROUP);
}

constexpr int ld_upto(int st, int st0, int first, int pf) {
  const int ramp = first + 2 * (st - st0), steady = st + pf, v = ramp < steady ? ramp : steady;
  return v < 17 ? v : 17;
}

__global__ __launch_bounds__(512, 2) void k_ffn(const u16* __restrict__ x, const u16* __restrict__ w1, const u16* __restrict__ b1,
                                                const u16* __restrict__ w2, const u16* __restrict__ b2, const u16* __restrict__ Dskip,
                                                char* ws, u16* __restrict__ out) {
  extern __shared__ __attribute__((aligned(16))) char smem[];
  char LAS* sm = (char LAS*)smem;
  const u32 smb = (u32)(uintptr_t)sm;
  const int tid = threadIdx.x, lane = tid & 63, wid = __builtin_amdgcn_readfirstlane(tid >> 6);
  const int xg = blockIdx.x & 7, idx = blockIdx.x >> 3, s = xg & 3;
  const int lb = 4 * (xg >> 2) + (idx >> 3), fb = 8 * s + (idx & 7);
  const int l2 = 8 * (xg >> 2) + (idx >> 2), dj = idx & 3, tile = l2 * 4 + dj;
  const int c = lane & 15, g = lane >> 4;
  const __amdgpu_buffer_rsrc_t rs = rsrc(ws + WS_SLAB + (size_t)tile * 16 * SLAB_B, 16 * SLAB_B);
  u32x2 xv[2][2] = {}, av[2][2] = {}, bias[2] = {};
  auto load_base = [&]() {
    const __amdgpu_buffer_rsrc_t ra = rsrc(ws + WS_A2, L * D * 2);
#pragma unroll
    for (int k = 0; k < 2; ++k) {
      const int t = k ? 8 : wid;
      if (k == 0 || wid == 4) {
        const int d = 144 * dj + 16 * t + 4 * g;
        bias[k] = *(const u32x2*)(b2 + d);
#pragma unroll
        for (int i = 0; i < 2; ++i) {
          const int l = 128 * l2 + 32 * s + 16 * i + c;
          xv[k][i] = *(const u32x2*)(x + (size_t)l * D + d);
          av[k][i] = __builtin_amdgcn_raw_buffer_load_b64(ra, ((d >> 2) * L + l) * 8, 0, 16);
        }
      }
    }
  };
  STAMP_AT(1, 0);
  if (wid < 4) {
    __builtin_amdgcn_s_setprio(2);
    {
      constexpr int XP = 3, WD = 3, WR = 4;
      const int lt0 = 16 * lb + 4 * wid;
      const __amdgpu_buffer_rsrc_t rx = rsrc(ws + WS_XNF + (size_t)lt0 * 18432, 4 * 18432);
      u32x2 bq[9];
#pragma unroll
      for (int t = 0; t < 9; ++t) bq[t] = *(const u32x2*)(b1 + fb * 144 + 16 * t + 4 * g);
      SB;
      u32x4 xf[XP + 1][4];
#pragma unroll
      for (int k = 0; k < XP - 1; ++k)
#pragma unroll
        for (int i = 0; i < 4; ++i) xf[k][i] = __builtin_amdgcn_raw_buffer_load_b128(rx, lane * 16, i * 18432 + k * 1024, 0);
      SB;
      f32x4 acc[9][4];
#pragma unroll
      for (int t = 0; t < 9; ++t) {
#pragma unroll
        for (int i = 0; i < 4; ++i) {
          acc[t][i] = f32x4{bl(bq[t][0]), bh(bq[t][0]), bl(bq[t][1]), bh(bq[t][1])};
          asm volatile("" : "+v"(acc[t][i]));
        }
      }
      const char LAS* wp = sm + (lane >> 2) * 288 + (lane & 3) * 8;
      u32x2 wa[WR], wb[WR];
      const char LAS* wph = wp + 6 * SLOT;
      asm volatile("" : "+v"(wph));
      auto wptr = [&](int n) {
        const int sl = (n / 9) % G1_R;
        return (sl < 6 ? wp + sl * SLOT : wph + (sl - 6) * SLOT) + (n % 9) * 32;
      };
      bar();
      STAMP_AT(1, 1);
#pragma unroll
      for (int n = 0; n < WD; ++n) { wa[n] = tr_read(wptr(n)); wb[n] = tr_read(wptr(n) + 16 * 288); }
#pragma unroll
      for (int ks = 0; ks < 18; ++ks) {
#pragma unroll
        for (int t = 0; t < 9; ++t) {
          const int n = 9 * ks + t;
          const u32x4 w = {wa[n % WR][0], wa[n % WR][1], wb[n % WR][0], wb[n % WR][1]};
          acc[t][0] = mfma16(w, xf[ks % (XP + 1)][0], acc[t][0]);
          SB;
          if (t == 9 - WD && ks < 17) bar();
          if (n == 161) bar();
          if (n + WD < 162) wa[(n + WD) % WR] = tr_read(wptr(n + WD));
          SB;
          acc[t][1] = mfma16(w, xf[ks % (XP + 1)][1], acc[t][1]);
          SB;
          if (n + WD < 162) wb[(n + WD) % WR] = tr_read(wptr(n + WD) + 16 * 288);
          SB;
          acc[t][2] = mfma16(w, xf[ks % (XP + 1)][2], acc[t][2]);
          SB;
          if (ks == 0 && t < 8) xf[XP - 1 + (t >> 2)][t & 3] = __builtin_amdgcn_raw_buffer_load_b128(rx, lane * 16, (t & 3) * 18432 + (XP - 1 + (t >> 2)) * 1024, 0);
          if (ks > 0 && t < 4 && ks + XP < 18) xf[(ks + XP) % (XP + 1)][t] = __builtin_amdgcn_raw_buffer_load_b128(rx, lane * 16, t * 18432 + (ks + XP) * 1024, 0);
          SB;
          acc[t][3] = mfma16(w, xf[ks % (XP + 1)][3], acc[t][3]);
          SB;
        }
      }
      STAMP_AT(1, 2);
      const __amdgpu_buffer_rsrc_t rh = rsrc(ws + WS_HF + ((size_t)lt0 * 144 + 9 * (fb >> 1)) * 1024, 4 * 147456);
#pragma unroll
      for (int i = 0; i < 4; ++i)
#pragma unroll
        for (int r = 0; r < 4; ++r)
          st128<16>(gelu8(acc[2 * r][i], acc[2 * r + 1][i]), rh, lane * 16, i * 147456 + (4 * (fb & 1) + r) * 1024);
#pragma unroll
      for (int i = 0; i < 2; ++i)
        st128<16>(gelu8(acc[8][2 * i], acc[8][2 * i + 1]), rh, lane * 16, (2 * i + (fb & 1)) * 147456 + 8 * 1024);
      STAMP_AT(1, 3);
    }
    waitv<0>();
    bar();
    STAMP_AT(1, 4);
    if (tid == 0) cnt_add(ws, CNT_H + xg * 4 + (idx >> 3));
    {
      constexpr int XP = G2_XP, X0 = G2_X0, WD = 5, WR = 6, TAIL = 4;
      const __amdgpu_buffer_rsrc_t rh = rsrc(ws + WS_HF + ((size_t)(8 * l2 + 2 * wid) * 144 + 36 * s) * 1024, 2 * 147456);
      f32x4 acc[9][2];
#pragma unroll
      for (int t = 0; t < 9; ++t)
#pragma unroll
        for (int i = 0; i < 2; ++i) {
          acc[t][i] = f32x4{0.f, 0.f, 0.f, 0.f};
          asm volatile("" : "+v"(acc[t][i]));
        }
      const char LAS* wp = sm + lane * 16;
      u32x4 wf[WR];
      constexpr auto ord_ks = [](int n) { return n < 9 * (36 - TAIL) ? n / 9 : 36 - TAIL + (n - 9 * (36 - TAIL)) % TAIL; };
      constexpr auto ord_t = [](int n) { return n < 9 * (36 - TAIL) ? n % 9 : (n - 9 * (36 - TAIL)) / TAIL; };
      const char LAS* wph = wp + 65536;
      asm volatile("" : "+v"(wph));
      auto wread = [&](int n) {
        const int o = (9 * ord_ks(n) + ord_t(n)) % (18 * G2_R);
        wf[n % WR] = *(const u32x4 LAS*)(o < 64 ? wp + o * 1024 : wph + (o - 64) * 1024);
      };
#pragma unroll
      for (int n = 0; n < WD; ++n) wread(n);
      cnt_wait(ws, CNT_H + xg * 4 + (idx >> 3), 8u);
      STAMP_AT(1, 5);
      u32x4 xf[XP + 1][2];
#pragma unroll
      for (int k = 0; k < X0; ++k)
#pragma unroll
        for (int i = 0; i < 2; ++i) xf[k][i] = __builtin_amdgcn_raw_buffer_load_b128(rh, lane * 16, i * 147456 + k * 1024, 16);
      SB;
      static_for<0, 324>([&](auto N) {
        constexpr int n = decltype(N)::value;
        constexpr int ks = ord_ks(n), t = ord_t(n);
        if (ks % 9 == 8 && t == 0) {
          const u32x4 a = xf[ks % (XP + 1)][0], b = xf[ks % (XP + 1)][1];
          xf[ks % (XP + 1)][0] = u32x4{a[0], a[1], b[0], b[1]};
          xf[ks % (XP + 1)][1] = u32x4{a[2], a[3], b[2], b[3]};
        }
        acc[t][0] = mfma16(wf[n % WR], xf[ks % (XP + 1)][0], acc[t][0]);
        SB;
        if ((n + WD) % 18 == 0 && n + WD >= 90 && n + WD < 9 * (36 - TAIL)) bar();
        if (n + WD == 9 * (36 - TAIL)) bars<TAIL / 2>();
        if (n + WD < 324) wread(n + WD);
        SB;
        acc[t][1] = mfma16(wf[n % WR], xf[ks % (XP + 1)][1], acc[t][1]);
        SB;
        if (ks < XP - X0 && (t & 1) && t < 8) {
          constexpr int kn = X0 + 2 * ks + (t >> 2);
          xf[kn % (XP + 1)][(t >> 1) & 1] = __builtin_amdgcn_raw_buffer_load_b128(rh, lane * 16, ((t >> 1) & 1) * 147456 + kn * 1024, 16);
        }
        if (ks >= XP - X0 && (t == 2 || t == 6) && ks + XP < 36)
          xf[(ks + XP) % (XP + 1)][(t >> 2) & 1] = __builtin_amdgcn_raw_buffer_load_b128(rh, lane * 16, (t >> 2) * 147456 + (ks + XP) * 1024, 16);
        if (ks == 30 && t == 2) load_base();
        if (ks == 36 - TAIL && t > 0 && n >= 9 * (36 - TAIL)) {
          constexpr int tt = t > 0 ? t - 1 : 0;
          if (wid == s) {
            *(f32x4 LAS*)(sm + LM_OWN + (2 * tt) * 1024 + lane * 16) = acc[tt][0];
            *(f32x4 LAS*)(sm + LM_OWN + (2 * tt + 1) * 1024 + lane * 16) = acc[tt][1];
          } else {
            const u32x4 v = {pk(acc[tt][0][0], acc[tt][0][1]), pk(acc[tt][0][2], acc[tt][0][3]), pk(acc[tt][1][0], acc[tt][1][1]), pk(acc[tt][1][2], acc[tt][1][3])};
            st128<16>(v, rs, lane * 16, (wid * 4 + s) * SLAB_B + tt * 1024);
          }
        }
        SB;
      });
      STAMP_AT(2, 2);
      if (wid == s) {
        *(f32x4 LAS*)(sm + LM_OWN + 16 * 1024 + lane * 16) = acc[8][0];
        *(f32x4 LAS*)(sm + LM_OWN + 17 * 1024 + lane * 16) = acc[8][1];
      } else {
        const u32x4 v = {pk(acc[8][0][0], acc[8][0][1]), pk(acc[8][0][2], acc[8][0][3]), pk(acc[8][1][0], acc[8][1][1]), pk(acc[8][1][2], acc[8][1][3])};
        st128<16>(v, rs, lane * 16, (wid * 4 + s) * SLAB_B + 8 * 1024);
      }
    }
  } else {
    const bool conv = idx < 18;
    u32 ltv = 0;
    LSTAMP(63);
    const int Q = xg * 18 + idx;
    if (wid < 6) {
      const int p = wid - 4;
      u32 voff[5];
#pragma unroll
      for (int n = 0; n < 5; ++n) { const int ci = 64 * (5 * p + n) + lane; voff[n] = (ci / 18) * (F * 2) + (ci % 18) * 16; }
      const char* wsrc = (const char*)(w1 + fb * 144);
      auto issue = [&](int j) {
        const char* sp = wsrc + (size_t)j * 32 * F * 2;
        const u32 dst = smb + (j % G1_R) * SLOT + p * 5120;
#pragma unroll
        for (int n = 0; n < 4; ++n) glds16(sp, voff[n], dst + n * 1024);
        if (p == 0) glds16(sp, voff[4], dst + 4096);
      };
      issue(0);
      issue(1);
      static_for<-1, 17>([&](auto K) {
        constexpr int ks = decltype(K)::value;
        constexpr int yng = ld_upto(ks - 1, -2, 1, G1_PF) - (ks + 1);
        if (p == 0) waitv<5 * yng>(); else waitv<4 * yng>();
        LSTAMP(2 * (ks + 1));
        bar();
        LSTAMP(2 * (ks + 1) + 1);
#pragma unroll
        for (int j = ld_upto(ks - 1, -2, 1, G1_PF) + 1; j <= ld_upto(ks, -2, 1, G1_PF); ++j) issue(j);
      });
    } else if (conv) {
      const int p = wid - 6;
      bars<3>();
      u32x4 uv[8];
      const char* us = ws + WS_UT2 + (size_t)(128 * p + (lane >> 2)) * 9216 + Q * 64 + (lane & 3) * 16;
#pragma unroll
      for (int i = 0; i < 8; ++i) uv[i] = *(const u32x4*)(us + (size_t)i * 16 * 9216);
#pragma unroll
      for (int i = 0; i < 3; ++i) glds16(ws + WS_KT + (size_t)(4 * Q + 2 * p) * KT_B + i * 1024, lane * 16, smb + LM_KT + 2 * p * KT_B + i * 1024);
      bar();
      if (lane < 16) *(u32x4 LAS*)(sm + LM_UIM + (lane >> 2) * UIM_B + (lane & 3) * 16) = u32x4{0u, 0u, 0u, 0u};
      if (lane == 0) *(u32x2 LAS*)(sm + LM_CNT) = u32x2{0u, 0u};
      if (lane < 56) {
#pragma unroll
        for (int i = 0; i < 2; ++i)
          *(u32x4 LAS*)(sm + LM_AUX + (2 * p + i) * KIM_B + (lane & 1) * KIM_C1 + (lane >> 1) * 16) = u32x4{0u, 0u, 0u, 0u};
      }
      int pos[9], pm[3];
#pragma unroll
      for (int e = 0; e < 9; ++e) {
        pos[e] = kpos(max(8 * lane + 7 - e, 0));
        asm volatile("" : "+v"(pos[e]));
        if (e % 3 == 2) bar();
      }
#pragma unroll
      for (int e = 0; e < 3; ++e) { pm[e] = kpos(8 * (64 + lane) + 4 * e); asm volatile("" : "+v"(pm[e])); }
      bar();
#pragma unroll
      for (int i = 0; i < 8; ++i)
        *(u32x4 LAS*)(sm + LM_UIM + (lane & 3) * UIM_B + 64 + (128 * p + 16 * i + (lane >> 2)) * 16) = uv[i];
      waitv<0>();
      bar();
#pragma unroll
      for (int i = 0; i < 2; ++i) {
        const char LAS* tb = sm + LM_KT + (2 * p + i) * KT_B;
        char LAS* ko = sm + LM_AUX + (2 * p + i) * KIM_B + (28 + lane) * 16;
        auto put = [&](int mi, const u32(&kv)[9]) {
          *(u32x4 LAS*)(ko + mi * 1024) = u32x4{kv[1] | (kv[2] << 16), kv[3] | (kv[4] << 16), kv[5] | (kv[6] << 16), kv[7] | (kv[8] << 16)};
          *(u32x4 LAS*)(ko + mi * 1024 + KIM_C1) = u32x4{kv[0] | (kv[1] << 16), kv[2] | (kv[3] << 16), kv[4] | (kv[5] << 16), kv[6] | (kv[7] << 16)};
        };
        {
          u32 kv[9];
#pragma unroll
          for (int e = 0; e < 9; ++e) kv[e] = *(const u16 LAS*)(tb + pos[e]);
          if (lane == 0) kv[8] = 0u;
          put(0, kv);
        }
        bar();
        {
          const u32 a = *(const u16 LAS*)(tb + pm[0]), b = *(const u16 LAS*)(tb + pm[1]), e = *(const u16 LAS*)(tb + pm[2]);
          const u32 kv[9] = {e, e, b, b, b, a, a, a, a};
          put(1, kv);
        }
#pragma unroll
        for (int mi = 2; mi < 4; ++mi) {
          const u32 a = *(const u16 LAS*)(tb + 768 + 128 * mi + 2 * lane), e = *(const u16 LAS*)(tb + 770 + 128 * mi + 2 * lane);
          const u32 tie = (lane & 1) ? e : a;
          const u32 kv[9] = {e, e, e, tie, a, a, a, a, a};
          put(mi, kv);
        }
        bar();
      }
      waitl<0>();
      bars<5>();
    } else {
      const int p = wid - 6;
      const int u0 = (((xg >> 2) * 14 + (idx - 18)) * 2 + p) * 6;
      const int sc = LM_AUX + p * 6 * 4096;
      const int r8 = lane >> 3, pos = lane & 7;
      const u32 voff = r8 * (D * 2) + (((pos >> 1) ^ (r8 >> 1)) * 2 + (pos & 1)) * 16;
      bars<3>();
#pragma unroll
      for (int i = 0; i < 6; ++i) {
        const int u = min(u0 + i, 323), kk = 36 * s + u / 9, dg = u % 9, P = kk / 9, r = kk % 9;
        const int fA = 288 * P + (r < 4 ? 32 * r : r < 8 ? 144 + 32 * (r - 4) : 128), fB = r < 8 ? fA + 16 : fA + 144;
#pragma unroll
        for (int n = 0; n < 4; ++n)
          glds16((const char*)(w2 + (size_t)((n < 2 ? fA : fB) + 8 * (n & 1)) * D + dg * 64), voff, smb + sc + i * 4096 + n * 1024);
        if (i & 1) bar();
      }
      bars<3>();
      waitv<0>();
      const int rr = lane >> 2;
#pragma unroll
      for (int i = 0; i < 6; ++i) {
        const int u = min(u0 + i, 323), kk = 36 * s + u / 9, dg = u % 9;
        const __amdgpu_buffer_rsrc_t dst = rsrc(ws + WS_W2P + ((size_t)kk * 36 + dg * 4) * 1024, 4096);
#pragma unroll
        for (int t = 0; t < 4; ++t) {
          const char LAS* tp = sm + sc + i * 4096 + rr * 128 + ((t ^ ((rr >> 1) & 3)) * 32) + (lane & 3) * 8;
          const u32x2 a = tr_read(tp), b = tr_read(tp + 2048);
          st128<16>(u32x4{a[0], a[1], b[0], b[1]}, dst, lane * 16, t * 1024);
        }
        if (i & 1) bar();
      }
      bars<2>();
      waitv<0>();
      bar();
      if (tid == 384) cnt_add(ws, CNT_P + s);
      bars<3>();
    }
    const int p = wid - 4, n0 = (p & 1) * 5;
    const char* wsrc = ws + WS_W2P + ((size_t)(36 * s + (p >> 1)) * 36 + 9 * dj + n0) * 1024;
    auto issue2 = [&](int j) {
      const u32 dst = smb + (j % G2_R) * STG + (p >> 1) * SLOT + n0 * 1024;
#pragma unroll
      for (int n = 0; n < 4; ++n) glds16c(wsrc + (size_t)j * 73728 + n * 1024, lane * 16, dst + n * 1024);
      if (n0 == 0) glds16c(wsrc + (size_t)j * 73728 + 4096, lane * 16, dst + 4096);
    };
    bar();
    STAMP_AT(1, 2);
    cnt_wait(ws, CNT_P + s, 28u);
#pragma unroll
    for (int j = 0; j < 5; ++j) issue2(j);
    if (!conv) { issue2(5); issue2(6); }
    const float dsk = b2f(Dskip[conv ? 4 * Q + p : 0]);
    waitv<0>();
    STAMP_AT(1, 3);
    bar();
    LSTAMP(62);
    STAMP_AT(1, 4);
    if (conv) {
      __builtin_amdgcn_s_setprio(CONV_PRIO);
      const char LAS* kp = sm + LM_AUX + p * KIM_B + (c & 1) * KIM_C1 + (4 * (c >> 1) + g) * 16;
      char LAS* ub = sm + LM_UIM + p * UIM_B + 64;
      const char LAS* up = ub + 2 * (8 * (28 + (c >> 2) - g) + 2 * (c & 3) - 6);
      f32x4 acc[8];
#pragma unroll
      for (int T = 0; T < 8; ++T) acc[T] = f32x4{0.f, 0.f, 0.f, 0.f};
      u32x4 kf[2][8], uf[2][8];
      auto rd = [&](int be, int a) {
        kf[be & 1][a] = *(const u32x4 LAS*)(kp + be * 64 + a * 512);
        const u32 LAS* q = (const u32 LAS*)(up - be * 64 + a * 512);
        uf[be & 1][a] = u32x4{q[0], q[1], q[2], q[3]};
      };
#pragma unroll
      for (int a = 0; a < 8; ++a) rd(0, a);
#pragma unroll
      for (int be = 0; be < 8; ++be) {
#pragma unroll
        for (int T = 7; T >= 0; --T) {
          if (be < 7) rd(be + 1, 7 - T);
#pragma unroll
          for (int a = 0; a <= T; ++a) acc[T] = mfma16(uf[be & 1][T - a], kf[be & 1][a], acc[T]);
          SB;
        }
      }
      const bool odd = c & 1;
#pragma unroll
      for (int T = 0; T < 8; ++T) {
        const u32 p01 = pk(acc[T][0], acc[T][1]), p23 = pk(acc[T][2], acc[T][3]);
        const u32 own = odd ? p23 : p01;
        const u32 rcv = (u32)__builtin_amdgcn_mov_dpp((int)(odd ? p01 : p23), 0xB1, 0xF, 0xF, true);
        const u32 ev = odd ? rcv : own, od = odd ? own : rcv;
        char LAS* yp = ub + 2 * (8 * (32 * T + g + 4 * (c >> 1)) + 4 * (c & 1));
        const u32x2 uu = *(const u32x2 LAS*)yp;
        const u32 s0 = pk(bl(uu[0]) * dsk, bh(uu[0]) * dsk), s1 = pk(bl(uu[1]) * dsk, bh(uu[1]) * dsk);
        *(u32x2 LAS*)yp = u32x2{pk(bl(ev) + bl(s0), bl(od) + bh(s0)), pk(bh(ev) + bl(s1), bh(od) + bh(s1))};
      }
      waitl<0>();
      volatile u32 LAS* cnt = (volatile u32 LAS*)(sm + LM_CNT);
      if (lane == 0) lds_add(sm + LM_CNT);
      while (__builtin_amdgcn_readfirstlane(*cnt) < 4u) __builtin_amdgcn_s_sleep(1);
      asm volatile("" ::: "memory");
      issue2(5);
      issue2(6);
      const __amdgpu_buffer_rsrc_t ao = rsrc(ws + WS_A2 + (size_t)Q * 16384 + 4096 * p, 4096);
      const char LAS* tp = sm + LM_UIM + (c >> 2) * UIM_B + 64 + 2 * (512 * p + 16 * g) + (c & 3) * 8;
#pragma unroll
      for (int i = 0; i < 8; ++i) __builtin_amdgcn_raw_buffer_store_b64(tr_read(tp + i * 128), ao, lane * 8, i * 512, 16);
      waitv<0>();
      if (lane == 0 && lds_add(sm + LM_CNT + 4) == 3u) cnt_add(ws, CNT_A + Q / 36);
    }
    STAMP_AT(1, 5);
    static_for<4, 17>([&](auto K) {
      constexpr int st = decltype(K)::value;
      constexpr int yng = ld_upto(st - 1, 3, 6, G2_PF) - (st + 1);
      if (st == 14 && wid == 7) cnt_wait(ws, CNT_A + dj, 36u);
      if (n0 == 0) waitv<5 * yng>(); else waitv<4 * yng>();
      LSTAMP(36 + 2 * (st - 4));
      bar();
      LSTAMP(36 + 2 * (st - 4) + 1);
#pragma unroll
      for (int j = ld_upto(st - 1, 3, 6, G2_PF) + 1; j <= ld_upto(st, 3, 6, G2_PF); ++j) issue2(j);
    });
    load_base();
  }
  u32x2 base[2][2];
#pragma unroll
  for (int k = 0; k < 2; ++k)
#pragma unroll
    for (int i = 0; i < 2; ++i)
      base[k][i] = u32x2{pk(bl(xv[k][i][0]) + bl(av[k][i][0]), bh(xv[k][i][0]) + bh(av[k][i][0])),
                         pk(bl(xv[k][i][1]) + bl(av[k][i][1]), bh(xv[k][i][1]) + bh(av[k][i][1]))};
  waitv<0>(); waitl<0>();
  bar();
  STAMP_AT(2, 3);
  if (tid == 0) cnt_add(ws, CNT_X + tile);
  cnt_wait(ws, CNT_X + tile, 4u);
  STAMP_AT(2, 4);
  u32x4 part[2][3];
#pragma unroll
  for (int k = 0; k < 2; ++k)
    if (k == 0 || wid == 4) {
#pragma unroll
      for (int e = 0; e < 3; ++e)
        part[k][e] = __builtin_amdgcn_raw_buffer_load_b128(rs, lane * 16, (s * 4 + ((s + 1 + e) & 3)) * SLAB_B + (k ? 8 : wid) * 1024, 16);
    }
#pragma unroll
  for (int k = 0; k < 2; ++k)
    if (k == 0 || wid == 4) {
      const int t = k ? 8 : wid;
#pragma unroll
      for (int i = 0; i < 2; ++i) {
        f32x4 v = *(const f32x4 LAS*)(sm + LM_OWN + (2 * t + i) * 1024 + lane * 16);
#pragma unroll
        for (int e = 0; e < 3; ++e)
          v += f32x4{bl(part[k][e][2 * i]), bh(part[k][e][2 * i]), bl(part[k][e][2 * i + 1]), bh(part[k][e][2 * i + 1])};
        const u32 r0 = pk(v[0], v[1]), r1 = pk(v[2], v[3]);
        const u32 q0 = pk(bl(r0) + bl(bias[k][0]), bh(r0) + bh(bias[k][0])), q1 = pk(bl(r1) + bl(bias[k][1]), bh(r1) + bh(bias[k][1]));
        const int l = 128 * l2 + 32 * s + 16 * i + c, d = 144 * dj + 16 * t + 4 * g;
        *(u32x2*)(out + (size_t)l * D + d) =
            u32x2{pk(bl(base[k][i][0]) + bl(q0), bh(base[k][i][0]) + bh(q0)), pk(bl(base[k][i][1]) + bl(q1), bh(base[k][i][1]) + bh(q1))};
      }
    }
  STAMP_AT(2, 5);
}

extern "C" void kernel_launch(void* const* d_in, const int* in_sizes, int n_in, void* d_out, int out_size, void* d_ws,
                              size_t ws_size, hipStream_t stream) {
  const u16* const* in = (const u16* const*)d_in;
  char* ws = (char*)d_ws;
  (void)hipFuncSetAttribute((const void*)k_ffn, hipFuncAttributeMaxDynamicSharedMemorySize, LM_END);
  k_prep<<<256, 1024, 0, stream>>>(in[0], in[1], in[2], in[3], in[4], in[5], in[6], in[7], ws);
  k_ffn<<<256, 512, LM_END, stream>>>(in[0], in[10], in[11], in[12], in[13], in[8], ws, (u16*)d_out);
}
```

```cpp
#include <hip/hip_runtime.h>
#include <hip/hip_bf16.h>
#include <stdint.h>
#include <type_traits>

typedef unsigned short u16;
typedef unsigned int u32;
typedef unsigned long long u64;
typedef __bf16 bf16x8 __attribute__((ext_vector_type(8)));
typedef __bf16 bf16x2 __attribute__((ext_vector_type(2)));
typedef short s16x4 __attribute__((ext_vector_type(4)));
typedef float f32x4 __attribute__((ext_vector_type(4)));
typedef float f32x2 __attribute__((ext_vector_type(2)));
typedef u32 u32x4 __attribute__((ext_vector_type(4)));
typedef u32 u32x2 __attribute__((ext_vector_type(2)));
#define LAS __attribute__((address_space(3)))
#define GAS __attribute__((address_space(1)))
#define POLL_SLEEP 4
#define SB __builtin_amdgcn_sched_barrier(0)

constexpr int L = 2048, D = 576, NM = 128, F = 4608;

constexpr int FL_EPOCH = 0;
constexpr int FL_LN = 1;
constexpr int FL_A = FL_LN + 256;
constexpr int FL_H = FL_A + 144;
constexpr int FL_X = FL_H + 256;
constexpr size_t WS_XNF = 131072;
constexpr size_t WS_UT = WS_XNF + (size_t)L * D * 2;
constexpr size_t WS_A2 = WS_UT + (size_t)L * D * 2;
constexpr size_t WS_HF = WS_A2 + (size_t)L * D * 2;
constexpr size_t WS_SLAB = WS_HF + (size_t)L * F * 2;
constexpr int SLAB_B = 9216, HB = 73728;
constexpr size_t WS_END = WS_SLAB + (size_t)64 * 16 * SLAB_B;
#define LSTAMP(i)
#define RSTAMP(i)

__device__ __forceinline__ float bl(u32 v) { return __uint_as_float(v << 16); }
__device__ __forceinline__ float bh(u32 v) { return __uint_as_float(v & 0xffff0000u); }
__device__ __forceinline__ float b2f(u16 v) { return __uint_as_float(((u32)v) << 16); }
__device__ __forceinline__ u32 pk(float lo, float hi) {
  bf16x2 r = {(__bf16)lo, (__bf16)hi};
  return __builtin_bit_cast(u32, r);
}
__device__ __forceinline__ float fadd(float a, float b) {
  float r;
  asm("v_add_f32 %0, %1, %2" : "=v"(r) : "v"(a), "v"(b));
  return r;
}
__device__ __forceinline__ float rb(float f) { return (float)(__bf16)f; }
__device__ __forceinline__ f32x4 mfma16(u32x4 a, u32x4 b, f32x4 c) {
  return __builtin_amdgcn_mfma_f32_16x16x32_bf16(__builtin_bit_cast(bf16x8, a), __builtin_bit_cast(bf16x8, b), c, 0, 0, 0);
}
__device__ __forceinline__ u32x2 tr_read(const char LAS* p) {
  return __builtin_bit_cast(u32x2, __builtin_amdgcn_ds_read_tr16_b64_v4i16((s16x4 LAS*)p));
}
__device__ __forceinline__ __amdgpu_buffer_rsrc_t rsrc(const void* p, int bytes) {
  return __builtin_amdgcn_make_buffer_rsrc((void*)p, 0, bytes, 0x00020000);
}
constexpr int SC1 = 16;
__device__ __forceinline__ void st128(u32x4 v, __amdgpu_buffer_rsrc_t r, int voff, int soff) {
  __builtin_amdgcn_raw_buffer_store_b128(v, r, voff, soff, SC1);
  asm volatile("s_nop 1" ::"v"(v) : "memory");
}
__device__ __forceinline__ u32x4 ld128(__amdgpu_buffer_rsrc_t r, int voff, int soff) {
  return __builtin_amdgcn_raw_buffer_load_b128(r, voff, soff, SC1);
}
__device__ __forceinline__ void glds16(const char* base, u32 voff, u32 dst) {
  asm volatile("s_mov_b32 m0, %2\n\ts_nop 0\n\tglobal_load_lds_dwordx4 %0, %1" ::"v"(voff), "s"(base), "s"(dst) : "memory");
}
template <int N> __device__ __forceinline__ void waitv() { asm volatile("s_waitcnt vmcnt(%0)" ::"i"(N) : "memory"); }
template <int N> __device__ __forceinline__ void waitl() { asm volatile("s_waitcnt lgkmcnt(%0)" ::"i"(N) : "memory"); }
__device__ __forceinline__ void bar() {
  asm volatile("" ::: "memory");
  __builtin_amdgcn_s_barrier();
  asm volatile("" ::: "memory");
}
template <int N> __device__ __forceinline__ void bars() {
#pragma unroll
  for (int i = 0; i < N; ++i) bar();
}
template <int I, int N, class Fn> __device__ __forceinline__ void static_for(Fn&& fn) {
  if constexpr (I < N) {
    fn(std::integral_constant<int, I>{});
    static_for<I + 1, N>(fn);
  }
}
__device__ __forceinline__ void flag_set(char* ws, int off, u64 tag) {
  __hip_atomic_store((GAS u64*)(ws + off * 128), tag, __ATOMIC_RELAXED, __HIP_MEMORY_SCOPE_AGENT);
}
__device__ __forceinline__ void flag_wait(char* ws, int off, int n, u64 tag, int lane) {
  GAS u64* p = (GAS u64*)(ws + (off + min(lane, n - 1)) * 128);
  for (u32 spin = 0; spin < (1u << 20); ++spin) {
    if (__all(__hip_atomic_load(p, __ATOMIC_RELAXED, __HIP_MEMORY_SCOPE_AGENT) == tag)) break;
    __builtin_amdgcn_s_sleep(POLL_SLEEP);
  }
  asm volatile("" ::: "memory");
}
__device__ __forceinline__ u32 lds_add(char LAS* p) {
  return __hip_atomic_fetch_add((u32 LAS*)p, 1u, __ATOMIC_RELAXED, __HIP_MEMORY_SCOPE_WORKGROUP);
}
__device__ __forceinline__ void lds_wait4(char LAS* p) {
  while (__builtin_amdgcn_readfirstlane(*(volatile u32 LAS*)p) < 4u) __builtin_amdgcn_s_sleep(1);
  asm volatile("" ::: "memory");
}

__device__ __forceinline__ float fmul(float a, float b) {
  float r;
  asm("v_mul_f32 %0, %1, %2" : "=v"(r) : "v"(a), "v"(b));
  return r;
}
__device__ __forceinline__ f32x2 cmul(f32x2 a, f32x2 b) {
  float re, im;
  asm("v_mul_f32 %0, %2, %4\n\tv_mul_f32 %1, %2, %5\n\tv_fma_f32 %0, -%3, %5, %0\n\tv_fma_f32 %1, %3, %4, %1"
      : "=&v"(re), "=&v"(im) : "v"(a[0]), "v"(a[1]), "v"(b[0]), "v"(b[1]));
  return f32x2{re, im};
}
template <int E> __device__ __forceinline__ f32x2 zpow2(float p2, float qh, float ql) {
  constexpr float s = (float)(1 << E);
  const float mag = __builtin_amdgcn_exp2f(p2 * s);
  const float ph = __builtin_fmaf(ql, s, __builtin_amdgcn_fractf(qh * s));
  float re, im;
  asm("s_nop 1\n\tv_mul_f32 %0, %2, %4\n\tv_mul_f32 %1, %3, %4"
      : "=&v"(re), "=&v"(im) : "v"(__builtin_amdgcn_cosf(ph)), "v"(__builtin_amdgcn_sinf(ph)), "v"(mag));
  return f32x2{re, im};
}
template <int CTRL> __device__ __forceinline__ float dpp_add(float v) {
  return v + __builtin_bit_cast(float, __builtin_amdgcn_update_dpp(0, __builtin_bit_cast(int, v), CTRL, 0xF, 0xF, true));
}
__device__ __forceinline__ float sum32(float v) {
  v = dpp_add<0x128>(v);
  v = dpp_add<0x124>(v);
  v = dpp_add<0x122>(v);
  v = dpp_add<0x121>(v);
  return v + __builtin_bit_cast(float, __builtin_amdgcn_ds_swizzle(__builtin_bit_cast(int, v), 0x401F));
}
__device__ __forceinline__ u32 gelu2(float a, float b) {
  constexpr float C0 = -2.f * 1.44269504088896340736f * 0.7978845608028654f, C1 = C0 * 0.044715f;
  const f32x2 v = {a, b};
  const f32x2 z = v * __builtin_elementwise_fma(v * v, f32x2{C1, C1}, f32x2{C0, C0});
  const f32x2 d = f32x2{__builtin_amdgcn_exp2f(z[0]), __builtin_amdgcn_exp2f(z[1])} + f32x2{1.f, 1.f};
  const f32x2 r = v * f32x2{__builtin_amdgcn_rcpf(d[0]), __builtin_amdgcn_rcpf(d[1])};
  return pk(r[0], r[1]);
}
__device__ __forceinline__ u32x4 gelu8(f32x4 a, f32x4 b) {
  return u32x4{gelu2(a[0], a[1]), gelu2(a[2], a[3]), gelu2(b[0], b[1]), gelu2(b[2], b[3])};
}

constexpr int SLOT = 9216;
constexpr int KIM_C1 = 4640, UIM_B = 5952, KG_S = 272, LN_S = 1168;
constexpr int LM_KIM = 10 * SLOT;
constexpr int LM_UIM = 14 * SLOT;
constexpr int LM_LN = LM_UIM + 4 * UIM_B;
constexpr int LM_CNT = LM_LN + 8 * LN_S;
constexpr int LM_END = LM_CNT + 32;
static_assert(LM_END <= 163840 && 4 * UIM_B >= 18432 && 21 * KG_S <= UIM_B && 32 * KG_S <= SLOT && UIM_B % 256 == 64, "LDS map");
constexpr int slot2(int j) { return j % 14 < 10 ? (j % 14 + 8) % 10 : j % 14; }

#define G2_XP 12
#define G2_X0 8
#define CODE_LINES 590
constexpr int G1_N0 = 4, G1_N1 = 8, G1_NB = 7;
constexpr int G1_POS[G1_NB + 1] = {4, 6, 8, 10, 12, 14, 16, 18};
constexpr int G1_UPTO[G1_NB] = {11, 14, 16, 18, 20, 22, 24};
constexpr bool g1_bar_before(int ks) {
  for (int i = 0; i < G1_NB; ++i)
    if (G1_POS[i] == ks) return true;
  return false;
}
constexpr int g2_upto(int st) { return st < 2 ? 6 : st + 6 < 17 ? st + 6 : 17; }

__global__ __launch_bounds__(512, 2) void k_all(const u16* __restrict__ x, const u16* __restrict__ Are, const u16* __restrict__ Aim,
                                                const u16* __restrict__ Bre, const u16* __restrict__ Bim, const u16* __restrict__ Cre,
                                                const u16* __restrict__ Cim, const u16* __restrict__ logdt, const u16* __restrict__ Dskip,
                                                const u16* __restrict__ w1, const u16* __restrict__ b1, const u16* __restrict__ w2,
                                                const u16* __restrict__ b2, char* ws, u16* __restrict__ out) {
  extern __shared__ __attribute__((aligned(16))) char smem[];
  char LAS* sm = (char LAS*)smem;
  const u32 smb = (u32)(uintptr_t)sm;
  const int tid = threadIdx.x, lane = tid & 63, wid = __builtin_amdgcn_readfirstlane(tid >> 6);
  const int b = blockIdx.x, xg = b & 7, idx = b >> 3, s = xg & 3;
  const int lb = 4 * (xg >> 2) + (idx >> 3), fb = 8 * s + (idx & 7);
  const int l2 = 8 * (xg >> 2) + (idx >> 2), dj = idx & 3, tile = l2 * 4 + dj;
  const int cl = xg * 4 + (idx >> 3);
  const int c = lane & 15, g = lane >> 4;
  const char* pc0;
  asm volatile("s_getpc_b64 %0" : "=s"(pc0));
  u64 tag;
  RSTAMP(62);
  LSTAMP(63);
  u32x2 xv[2][2] = {}, av[2][2] = {}, bias[2] = {};
  auto load_base = [&]() {
    const __amdgpu_buffer_rsrc_t ra = rsrc(ws + WS_A2, L * D * 2);
#pragma unroll
    for (int k = 0; k < 2; ++k) {
      const int t = k ? 8 : wid;
      if (k == 0 || wid == 4) {
        const int d = 144 * dj + 16 * t + 4 * g;
        bias[k] = *(const u32x2*)(b2 + d);
#pragma unroll
        for (int i = 0; i < 2; ++i) {
          const int l = 128 * l2 + 32 * s + 16 * i + c;
          xv[k][i] = *(const u32x2*)(x + (size_t)l * D + d);
          av[k][i] = __builtin_amdgcn_raw_buffer_load_b64(ra, ((d >> 2) * L + l) * 8, 0, SC1);
        }
      }
    }
  };
  if (wid < 4) {
    __builtin_amdgcn_s_setprio(2);
    u32x2 bq[9];
    {
      const int r = tid >> 5, lam = tid & 31;
      const u32* src = (const u32*)(x + (size_t)(b * 8 + r) * D) + lam;
      u32 raw[9];
#pragma unroll
      for (int i = 0; i < 9; ++i) raw[i] = src[32 * i];
      const u64 ep = __hip_atomic_load((GAS u64*)(ws + FL_EPOCH), __ATOMIC_RELAXED, __HIP_MEMORY_SCOPE_AGENT);
#pragma unroll
      for (int t = 0; t < 9; ++t) bq[t] = *(const u32x2*)(b1 + fb * 144 + 16 * t + 4 * g);
      if (tid == 0) *(u32x4 LAS*)(sm + LM_CNT) = u32x4{0u, 0u, 0u, 0u};
      waitl<0>();
      bar();
      f32x2 v[9];
#pragma unroll
      for (int i = 0; i < 9; ++i) v[i] = f32x2{bl(raw[i]), bh(raw[i])};
      f32x2 s2 = v[0];
#pragma unroll
      for (int i = 1; i < 9; ++i) s2 += v[i];
      const float mean = sum32(s2[0] + s2[1]) * (1.f / D);
      LSTAMP(0);
      f32x2 q2 = {0.f, 0.f};
#pragma unroll
      for (int i = 0; i < 9; ++i) { f32x2 d = v[i] - f32x2{mean, mean}; q2 = __builtin_elementwise_fma(d, d, q2); }
      const float q = sum32(q2[0] + q2[1]);
      const float m = rb(mean);
      const float sd = rb(__builtin_sqrtf(rb(rb(q * (1.f / D)) + 1e-5f)));
      const float rs = __builtin_amdgcn_rcpf(sd);
#pragma unroll
      for (int i = 0; i < 9; ++i) {
        const f32x2 d = v[i] - f32x2{m, m};
        const u32 t = pk(d[0], d[1]);
        const f32x2 n = f32x2{bl(t), bh(t)} * f32x2{rs, rs};
        *(u32 LAS*)(sm + LM_LN + r * LN_S + (lam + 32 * i) * 4) = pk(n[0], n[1]);
      }
      const u64 t64 = ep * 0x9E3779B97F4A7C15ull + 0xD1B54A32D192ED03ull;
      tag = ((u64)__builtin_amdgcn_readfirstlane((u32)(t64 >> 32)) << 32) | __builtin_amdgcn_readfirstlane((u32)t64);
      if (tid == 0) *(u64 LAS*)(sm + LM_CNT + 16) = tag;
      waitl<0>();
      if (lane == 0) lds_add(sm + LM_CNT);
      lds_wait4(sm + LM_CNT);
      LSTAMP(1);
      const __amdgpu_buffer_rsrc_t xo = rsrc(ws + WS_XNF + (size_t)(b >> 1) * 18432 + (b & 1) * 128, 18432);
      const __amdgpu_buffer_rsrc_t uo = rsrc(ws + WS_UT + (size_t)b * 9216, 9216);
      const char LAS* xp = sm + LM_LN + (lane & 7) * LN_S + (lane >> 5) * 64 + ((lane >> 3) & 3) * 8;
      const char LAS* tp = sm + LM_LN + ((lane >> 2) & 3) * LN_S + (lane >> 4) * 32 + (lane & 3) * 8;
      const int xoff = (lane >> 5) * 1024 + ((lane >> 3) & 3) * 256 + (lane & 7) * 16;
#pragma unroll
      for (int kk = 0; kk < 3; ++kk) {
        const int k = wid + 4 * kk;
        if (k < 9) {
          const u32x2 a = *(const u32x2 LAS*)(xp + k * 128), bb = *(const u32x2 LAS*)(xp + k * 128 + 32);
          const u32x2 t0 = tr_read(tp + k * 128), t1 = tr_read(tp + k * 128 + 4 * LN_S);
          st128(u32x4{a[0], a[1], bb[0], bb[1]}, xo, xoff, k * 2048);
          st128(u32x4{t0[0], t0[1], t1[0], t1[1]}, uo, lane * 16, k * 1024);
        }
      }
    }
    {
      constexpr int XP = 3, WD = 3, WR = 4;
      const int lt0 = 16 * lb + 4 * wid;
      const __amdgpu_buffer_rsrc_t rx = rsrc(ws + WS_XNF + (size_t)lt0 * 18432, 4 * 18432);
      f32x4 acc[9][4];
#pragma unroll
      for (int t = 0; t < 9; ++t) {
#pragma unroll
        for (int i = 0; i < 4; ++i) {
          acc[t][i] = f32x4{bl(bq[t][0]), bh(bq[t][0]), bl(bq[t][1]), bh(bq[t][1])};
          asm volatile("" : "+v"(acc[t][i]));
        }
      }
      waitv<0>();
      LSTAMP(2);
      if (lane == 0 && lds_add(sm + LM_CNT + 4) == 3u) flag_set(ws, FL_LN + b, tag);
      flag_wait(ws, FL_LN + 32 * lb + 8 * wid, 8, tag, lane);
      LSTAMP(3);
      u32x4 xf[XP + 1][4];
#pragma unroll
      for (int k = 0; k < XP; ++k)
#pragma unroll
        for (int i = 0; i < 4; ++i) xf[k][i] = ld128(rx, lane * 16, i * 18432 + k * 1024);
      SB;
      const char LAS* wp = sm + (lane >> 2) * 288 + (lane & 3) * 8;
      u32x2 wa[WR], wb[WR];
      const char LAS* wph = wp + 6 * SLOT;
      asm volatile("" : "+v"(wph));
      auto wptr = [&](int n) {
        const int sl = (n / 9) % 10;
        return (sl < 6 ? wp + sl * SLOT : wph + (sl - 6) * SLOT) + (n % 9) * 32;
      };
      lds_wait4(sm + LM_CNT + 12);
      LSTAMP(4);
      RSTAMP(55);
#pragma unroll
      for (int n = 0; n < WD; ++n) { wa[n] = tr_read(wptr(n)); wb[n] = tr_read(wptr(n) + 16 * 288); }
#pragma unroll
      for (int ks = 0; ks < 18; ++ks) {
#pragma unroll
        for (int t = 0; t < 9; ++t) {
          const int n = 9 * ks + t;
          const u32x4 w = {wa[n % WR][0], wa[n % WR][1], wb[n % WR][0], wb[n % WR][1]};
          acc[t][0] = mfma16(w, xf[ks % (XP + 1)][0], acc[t][0]);
          SB;
          if (t == 9 - WD && g1_bar_before(ks + 1)) bar();
          if (n == 161) bar();
          if (n + WD < 162) wa[(n + WD) % WR] = tr_read(wptr(n + WD));
          SB;
          acc[t][1] = mfma16(w, xf[ks % (XP + 1)][1], acc[t][1]);
          SB;
          if (n + WD < 162) wb[(n + WD) % WR] = tr_read(wptr(n + WD) + 16 * 288);
          SB;
          acc[t][2] = mfma16(w, xf[ks % (XP + 1)][2], acc[t][2]);
          SB;
          if (t < 4 && ks + XP < 18) xf[(ks + XP) % (XP + 1)][t] = ld128(rx, lane * 16, t * 18432 + (ks + XP) * 1024);
          SB;
          acc[t][3] = mfma16(w, xf[ks % (XP + 1)][3], acc[t][3]);
          SB;
        }
      }
      asm volatile("s_nop 15");
      SB;
      LSTAMP(5);
      RSTAMP(56);
      const __amdgpu_buffer_rsrc_t rh = rsrc(ws + WS_HF + (size_t)(cl * 8 + (idx & 7)) * HB, HB);
#pragma unroll
      for (int i = 0; i < 4; ++i)
#pragma unroll
        for (int r = 0; r < 4; ++r) st128(gelu8(acc[2 * r][i], acc[2 * r + 1][i]), rh, lane * 16, (4 * wid + i) * 4096 + r * 1024);
#pragma unroll
      for (int i = 0; i < 2; ++i) st128(gelu8(acc[8][2 * i], acc[8][2 * i + 1]), rh, lane * 16, 65536 + (2 * wid + i) * 1024);
      LSTAMP(6);
    }
    bar();
    {
      constexpr int XP = G2_XP, X0 = G2_X0, WD = 5, WR = 6, TAIL = 4;
      const __amdgpu_buffer_rsrc_t rh = rsrc(ws + WS_HF + (size_t)cl * 8 * HB, 8 * HB);
      const int pr = 4 * ((idx >> 2) & 1) + wid;
      const __amdgpu_buffer_rsrc_t rs = rsrc(ws + WS_SLAB + (size_t)tile * 16 * SLAB_B, 16 * SLAB_B);
      f32x4 acc[9][2];
#pragma unroll
      for (int t = 0; t < 9; ++t)
#pragma unroll
        for (int i = 0; i < 2; ++i) {
          acc[t][i] = f32x4{0.f, 0.f, 0.f, 0.f};
          asm volatile("" : "+v"(acc[t][i]));
        }
      const char LAS* wp = sm + (lane >> 2) * 288 + (lane & 3) * 8;
      u32x2 wa[WR], wb[WR];
      constexpr auto ord_ks = [](int n) { return n < 9 * (36 - TAIL) ? n / 9 : 36 - TAIL + (n - 9 * (36 - TAIL)) % TAIL; };
      constexpr auto ord_t = [](int n) { return n < 9 * (36 - TAIL) ? n % 9 : (n - 9 * (36 - TAIL)) / TAIL; };
      const char LAS* wph = wp + 7 * SLOT;
      asm volatile("" : "+v"(wph));
      auto wptr = [&](int n) {
        const int sl = slot2(ord_ks(n));
        return (sl < 7 ? wp + sl * SLOT : wph + (sl - 7) * SLOT) + ord_t(n) * 32;
      };
#pragma unroll
      for (int n = 0; n < WD; ++n) { wa[n] = tr_read(wptr(n)); wb[n] = tr_read(wptr(n) + 16 * 288); }
      u32x4 xf[XP + 1][2];
      auto hload = [&](int k, int i) {
        const int r = k % 9, P = ((idx >> 1) + k / 9) & 3;
        const int so = r < 8 ? (2 * P + (((r >> 2) ^ idx) & 1)) * HB + pr * 8192 + i * 4096 : (2 * P + i) * HB + 65536 + pr * 1024;
        xf[k % (XP + 1)][i] = ld128(rh, lane * 16 + (r < 8 ? (r & 3) * 1024 : 0), so);
      };
      waitv<0>();
      LSTAMP(7);
      const int flh = FL_H + cl * 8;
      if (lane == 0 && lds_add(sm + LM_CNT + 8) == 3u) flag_set(ws, flh + (idx & 7), tag);
      lds_wait4(sm + LM_CNT + 8);
#pragma unroll
      for (int k = 0; k < 4; ++k) { hload(k, 0); hload(k, 1); }
      flag_wait(ws, flh, 8, tag, lane);
      LSTAMP(8);
      RSTAMP(57);
#pragma unroll
      for (int k = 4; k < X0; ++k) { hload(k, 0); hload(k, 1); }
      SB;
      auto flush = [&](auto OWN, int tt) {
        if (decltype(OWN)::value) {
          *(f32x4 LAS*)(sm + LM_UIM + (2 * tt) * 1024 + lane * 16) = acc[tt][0];
          *(f32x4 LAS*)(sm + LM_UIM + (2 * tt + 1) * 1024 + lane * 16) = acc[tt][1];
        } else {
          const u32x4 v = {pk(acc[tt][0][0], acc[tt][0][1]), pk(acc[tt][0][2], acc[tt][0][3]), pk(acc[tt][1][0], acc[tt][1][1]), pk(acc[tt][1][2], acc[tt][1][3])};
          st128(v, rs, lane * 16, (wid * 4 + s) * SLAB_B + tt * 1024);
        }
      };
      auto step = [&](auto N, auto OWN) {
        constexpr int n = decltype(N)::value;
        constexpr int ks = ord_ks(n), t = ord_t(n);
        if (ks % 9 == 8 && t == 0) {
          const u32x4 a = xf[ks % (XP + 1)][0], bb = xf[ks % (XP + 1)][1];
          xf[ks % (XP + 1)][0] = u32x4{a[0], a[1], bb[0], bb[1]};
          xf[ks % (XP + 1)][1] = u32x4{a[2], a[3], bb[2], bb[3]};
        }
        const u32x4 w = {wa[n % WR][0], wa[n % WR][1], wb[n % WR][0], wb[n % WR][1]};
        acc[t][0] = mfma16(w, xf[ks % (XP + 1)][0], acc[t][0]);
        SB;
        if ((n + WD) % 36 == 18 && n + WD >= 54 && n + WD <= 270) bar();
        if (n + WD < 324) wa[(n + WD) % WR] = tr_read(wptr(n + WD));
        SB;
        acc[t][1] = mfma16(w, xf[ks % (XP + 1)][1], acc[t][1]);
        SB;
        if (n + WD < 324) wb[(n + WD) % WR] = tr_read(wptr(n + WD) + 16 * 288);
        if (ks < XP - X0 && (t & 1) && t < 8) hload(X0 + 2 * ks + (t >> 2), (t >> 1) & 1);
        if (ks >= XP - X0 && (t == 2 || t == 6) && ks + XP < 36) hload(ks + XP, (t >> 2) & 1);
        if (ks == 30 && t == 2) load_base();
        if (ks == 37 - TAIL && t > 0 && n >= 9 * (36 - TAIL)) flush(OWN, t > 0 ? t - 1 : 0);
        SB;
      };
      static_for<0, 9 * (36 - TAIL) + TAIL>([&](auto N) { step(N, std::false_type{}); });
      if (wid == s) {
        static_for<9 * (36 - TAIL) + TAIL, 324>([&](auto N) { step(N, std::true_type{}); });
        flush(std::true_type{}, 8);
      } else {
        static_for<9 * (36 - TAIL) + TAIL, 324>([&](auto N) { step(N, std::false_type{}); });
        flush(std::false_type{}, 8);
      }
      LSTAMP(9);
      RSTAMP(58);
    }
  } else {
    const bool conv = idx < 18;
    const int Q = xg * 18 + idx, p = wid - 4, n0 = (p & 1) * 5;
    u32 par[2][6], vdt, vds;
    {
      const int ch = conv ? 4 * Q + p : 0;
      const u32 o0 = (ch * NM + lane) * 2, o1 = ch * 2;
      const u16* src[6] = {Are, Aim, Bre, Bim, Cre, Cim};
#pragma unroll
      for (int i = 0; i < 6; ++i)
        asm volatile("global_load_ushort %0, %2, %3\n\tglobal_load_ushort %1, %2, %3 offset:128" : "=&v"(par[0][i]), "=&v"(par[1][i]) : "v"(o0), "s"(src[i]));
      asm volatile("global_load_ushort %0, %2, %3\n\tglobal_load_ushort %1, %2, %4" : "=&v"(vdt), "=&v"(vds) : "v"(o1), "s"(logdt), "s"(Dskip));
    }
    bar();
    u32 cr[9], cc[9];
#pragma unroll
    for (int n = 0; n < 9; ++n) { const int ci = 64 * n + lane; cr[n] = ci / 18; cc[n] = (ci % 18) * 16; }
    u32 v1[5], v2[9];
#pragma unroll
    for (int n = 0; n < 5; ++n) v1[n] = n0 ? cr[n < 4 ? 5 + n : 8] * (F * 2) + cc[n < 4 ? 5 + n : 8] : cr[n] * (F * 2) + cc[n];
    const char* w1s = (const char*)(w1 + fb * 144);
    auto issue1 = [&](int k) {
      const char* sp = w1s + (size_t)k * 32 * F * 2;
      const u32 dst = smb + (k % 10) * SLOT + n0 * 1024;
#pragma unroll
      for (int n = 0; n < 4; ++n) glds16(sp, v1[n], dst + n * 1024);
      if (n0 == 0) glds16(sp, v1[4], dst + 4096);
    };
#pragma unroll
    for (int k = 0; k < G1_N0 / 2; ++k) issue1(2 * k + (p >> 1));
    u32 cpf;
    {
      const u32 co = min(idx + 32 * (16 * p + (lane & 15)), CODE_LINES - 1) * 128;
      asm volatile("global_load_dword %0, %1, %2" : "=v"(cpf) : "v"(co), "s"(pc0));
    }
    LSTAMP(0);
#pragma unroll
    for (int n = 0; n < 9; ++n) v2[n] = cr[n] * (D * 2) + cc[n];
    const u32 v24s = v2[4] + (lane >= 32 ? 128 * D * 2 : 0);
    const char* w2s = (const char*)(w2 + (size_t)1152 * s * D + 144 * dj);
    auto ksrc = [&](int k) {
      const int r = k % 9;
      return w2s + (size_t)(288 * (((idx >> 1) + k / 9) & 3) + (r < 8 ? 144 * (((r >> 2) ^ idx) & 1) + 32 * (r & 3) : 128)) * (D * 2);
    };
    auto issue2 = [&](int k) {
      const char* sp = ksrc(k);
      const u32 dst = smb + slot2(k) * SLOT;
      if (n0 == 0) {
#pragma unroll
        for (int n = 0; n < 4; ++n) glds16(sp, v2[n], dst + n * 1024);
        glds16(sp, k % 9 == 8 ? v24s : v2[4], dst + 4096);
      } else {
#pragma unroll
        for (int n = 5; n < 9; ++n) glds16(sp + (k % 9 == 8 ? 128 * D * 2 : 0), v2[n], dst + n * 1024);
      }
    };
    auto issue_own = [&]() {
      const char* sp = ksrc(10 + p);
#pragma unroll
      for (int n = 0; n < 9; ++n) glds16(sp, v2[n], smb + (10 + p) * SLOT + n * 1024);
    };
    auto batch2 = [&]() {
      waitv<0>();
      asm volatile("" ::"v"(cpf));
      if (lane == 0) lds_add(sm + LM_CNT + 12);
#pragma unroll
      for (int k = G1_N0 / 2; k < G1_N1 / 2; ++k) issue1(2 * k + (p >> 1));
    };
    char LAS* kim = sm + LM_KIM + p * SLOT;
    char LAS* uim = sm + LM_UIM + p * UIM_B;
    if (conv) {
      LSTAMP(3);
      waitv<4 * (G1_N0 / 2) + 1>();
#pragma unroll
      for (int i = 0; i < 6; ++i) asm volatile("" : "+v"(par[0][i]), "+v"(par[1][i]));
      asm volatile("" : "+v"(vdt), "+v"(vds));
      const float dt = rb(__expf(bl(vdt)));
      f32x4 ka[2][2];
#pragma unroll
      for (int i = 0; i < 4; ++i) ka[i >> 1][i & 1] = f32x4{0.f, 0.f, 0.f, 0.f};
#pragma unroll 1
      for (int h = 0; h < 2; ++h) {
        float pv[6];
#pragma unroll
        for (int i = 0; i < 6; ++i) pv[i] = bl(h ? par[1][i] : par[0][i]);
        const float p2 = dt * pv[0] * 1.44269504088896340736f;
        const float q = dt * pv[1];
        constexpr float CH = 0.15915494309189535f;
        constexpr float CL = (float)(0.15915494309189533577 - (double)CH);
        const float qh = q * CH;
        const float ql = __builtin_fmaf(q, CL, __builtin_fmaf(q, CH, -qh));
        asm volatile("" ::"v"(ql));
        LSTAMP(50 + h);
        {
          f32x2 Z[16];
          auto put = [&](int i, f32x2 v) { *(u32 LAS*)(kim + i * KG_S + lane * 4) = pk(v[0], v[1]); };
          Z[0] = f32x2{1.f, 0.f};
          Z[1] = zpow2<3>(p2, qh, ql);
          Z[4] = zpow2<5>(p2, qh, ql);
          const f32x2 a7 = zpow2<7>(p2, qh, ql);
          Z[2] = cmul(Z[1], Z[1]);
          Z[8] = cmul(Z[4], Z[4]);
          Z[3] = cmul(Z[2], Z[1]);
#pragma unroll
          for (int i = 1; i < 4; ++i) Z[4 + i] = cmul(Z[4], Z[i]);
#pragma unroll
          for (int i = 1; i < 8; ++i) Z[8 + i] = cmul(Z[8], Z[i]);
#pragma unroll
          for (int i = 0; i < 16; ++i) put(i, Z[i]);
          put(16, a7);
#pragma unroll
          for (int i = 1; i < 16; ++i) put(16 + i, cmul(a7, Z[i]));
        }
        {
          f32x2 H[16];
          auto put = [&](int i, f32x2 v) { *(u32 LAS*)(uim + i * KG_S + lane * 4) = pk(v[0], -v[1]); };
          H[0] = cmul(f32x2{pv[4], pv[5]}, f32x2{fmul(dt, pv[2]), fmul(dt, pv[3])});
          const f32x2 a0 = zpow2<0>(p2, qh, ql), a8 = zpow2<8>(p2, qh, ql), a10 = zpow2<10>(p2, qh, ql);
          const f32x2 a1 = cmul(a0, a0), a2 = cmul(a1, a1), a9 = cmul(a8, a8), a11 = cmul(a10, a10);
          H[1] = cmul(a0, H[0]);
          H[2] = cmul(a1, H[0]);
          H[3] = cmul(a1, H[1]);
#pragma unroll
          for (int i = 0; i < 4; ++i) H[4 + i] = cmul(a2, H[i]);
#pragma unroll
          for (int i = 0; i < 4; ++i) H[8 + i] = cmul(a8, H[2 * i]);
          H[12] = cmul(a9, H[0]);
          H[13] = cmul(a9, H[4]);
          H[14] = cmul(a9, H[8]);
          H[15] = cmul(a9, H[10]);
#pragma unroll
          for (int i = 0; i < 16; ++i) put(i, H[i]);
          put(16, cmul(a10, H[0]));
          put(17, cmul(a10, H[8]));
          put(18, cmul(a10, H[12]));
          put(19, cmul(a10, H[14]));
          put(20, cmul(a11, H[0]));
        }
        lds_wait4(sm + LM_CNT);
#pragma unroll
        for (int k = 0; k < 4; ++k) {
          u32x4 lo[2], hi[2];
#pragma unroll
          for (int i = 0; i < 2; ++i) {
            lo[i] = *(const u32x4 LAS*)(kim + (16 * i + c) * KG_S + k * 64 + g * 16);
            hi[i] = *(const u32x4 LAS*)(uim + min(16 * i + c, 20) * KG_S + k * 64 + g * 16);
          }
#pragma unroll
          for (int i = 0; i < 4; ++i) ka[i >> 1][i & 1] = mfma16(lo[i & 1], hi[i >> 1], ka[i >> 1][i & 1]);
        }
        if (h == 0) batch2();
      }
      LSTAMP(1);
#pragma unroll
      for (int i = 0; i < 4; ++i)
        *(u32x2 LAS*)(uim + (16 * (i >> 1) + c) * 64 + (i & 1) * 32 + g * 8) =
            u32x2{pk(ka[i >> 1][i & 1][0], ka[i >> 1][i & 1][1]), pk(ka[i >> 1][i & 1][2], ka[i >> 1][i & 1][3])};
      if (lane < 56) *(u32x4 LAS*)(kim + (lane & 1) * KIM_C1 + (lane >> 1) * 16) = u32x4{0u, 0u, 0u, 0u};
      char LAS* ko = kim + (28 + lane) * 16;
      auto put = [&](int mi, const u32(&kv)[9]) {
        *(u32x4 LAS*)(ko + mi * 1024) = u32x4{kv[1] | (kv[2] << 16), kv[3] | (kv[4] << 16), kv[5] | (kv[6] << 16), kv[7] | (kv[8] << 16)};
        *(u32x4 LAS*)(ko + mi * 1024 + KIM_C1) = u32x4{kv[0] | (kv[1] << 16), kv[2] | (kv[3] << 16), kv[4] | (kv[5] << 16), kv[6] | (kv[7] << 16)};
      };
      {
        u32 kv[9];
#pragma unroll
        for (int e = 0; e < 9; ++e) {
          constexpr int EX[9] = {448, 384, 320, 256, 192, 128, 64, 0, 446}, R2[9] = {450, 640, 576, 576, 576, 512, 448, 448, 448};
          int o = 2 * lane + (lane < (e == 8 ? 33 : 32) ? EX[e] : R2[e]);
          if (e == 0 && lane == 63) o = 768;
          kv[e] = *(const u16 LAS*)(uim + o);
        }
        if (lane == 0) kv[8] = 0u;
        put(0, kv);
      }
      {
        const int oa = 768 + 128 * (lane >> 5) + 2 * (lane & 31), oe = 768 + 128 * ((lane + 1) >> 5) + 2 * ((lane + 1) & 31);
        const u32 a = *(const u16 LAS*)(uim + oa), bb = *(const u16 LAS*)(uim + oa + 64), e = *(const u16 LAS*)(uim + oe);
        const u32 kv[9] = {e, e, bb, bb, bb, a, a, a, a};
        put(1, kv);
      }
#pragma unroll
      for (int mi = 2; mi < 4; ++mi) {
        const u32 a = *(const u16 LAS*)(uim + 768 + 128 * mi + 2 * lane), e = *(const u16 LAS*)(uim + 770 + 128 * mi + 2 * lane);
        const u32 tie = (lane & 1) ? e : a;
        const u32 kv[9] = {e, e, e, tie, a, a, a, a, a};
        put(mi, kv);
      }
      waitl<0>();
      if (lane < 4) *(u32x4 LAS*)(uim + lane * 16) = u32x4{0u, 0u, 0u, 0u};
      waitl<0>();
    } else {
      batch2();
#pragma unroll
      for (int i = 0; i < 6; ++i) asm volatile("" ::"v"(par[0][i]), "v"(par[1][i]));
      asm volatile("" ::"v"(vdt), "v"(vds));
    }
    LSTAMP(2);
    lds_wait4(sm + LM_CNT);
    tag = *(const u64 LAS*)(sm + LM_CNT + 16);
    u32x4 uv[4], yv[8];
    if (conv) {
      flag_wait(ws, FL_LN + p * 64, 64, tag, lane);
      const __amdgpu_buffer_rsrc_t ru = rsrc(ws + WS_UT, L * D * 2);
#pragma unroll
      for (int j = 0; j < 4; ++j) uv[j] = ld128(ru, (64 * p + (lane >> 2)) * 9216 + Q * 64 + (lane & 3) * 16, j * 16 * 9216);
    }
    waitv<0>();
    static_for<0, G1_NB>([&](auto K) {
      constexpr int i = decltype(K)::value;
      constexpr int prev = i ? G1_UPTO[i ? i - 1 : 0] : G1_N1 - 1;
      static_assert(G1_UPTO[i] - 10 < G1_POS[i] - 1 || G1_UPTO[i] < 10, "ring schedule");
      constexpr int need = G1_POS[i + 1] - 1;
      constexpr int y0 = prev / 2 - need / 2, y1 = (prev + 1) / 2 - (need + 1) / 2;
      if (need >= G1_N1) {
        if (p == 0) waitv<5 * y0>(); else if (p == 1) waitv<4 * y0>(); else if (p == 2) waitv<5 * y1>(); else waitv<4 * y1>();
      }
      LSTAMP(4 + 2 * i);
      bar();
      LSTAMP(5 + 2 * i);
      if (i == 0 && conv) {
#pragma unroll
        for (int j = 0; j < 4; ++j) *(u32x4 LAS*)(sm + LM_UIM + (lane & 3) * UIM_B + 64 + (64 * p + 16 * j + (lane >> 2)) * 16) = uv[j];
        waitl<0>();
      }
#pragma unroll
      for (int u = prev + 1; u <= G1_UPTO[i]; ++u)
        if ((u & 1) == (p >> 1)) { if (u < 18) issue1(u); else issue2(u - 18); }
    });
    bar();
    LSTAMP(22);
#pragma unroll
    for (int u = G1_UPTO[G1_NB - 1] + 1; u < 28; ++u)
      if ((u & 1) == (p >> 1)) issue2(u - 18);
    if (!conv) {
      waitv<0>();
      bar();
      LSTAMP(23);
      issue_own();
    } else {
      const float dsk = bl(vds);
      const char LAS* kp = kim + (c & 1) * KIM_C1 + (4 * (c >> 1) + g) * 16;
      char LAS* ub = uim + 64;
      const char LAS* up[4];
#pragma unroll
      for (int i = 0; i < 4; ++i) {
        const char LAS* q = ub + 2 * (8 * ((c >> 2) - g) + 2 * (c & 3) - 6) + i * 1024;
        asm volatile("" : "+v"(q));
        up[i] = q + 448;
      }
      char LAS* yp = ub + 2 * (8 * (g + 4 * (c >> 1)) + 4 * (c & 1));
      u32x2 sk[8];
#pragma unroll
      for (int T = 0; T < 8; ++T) {
        const u32x2 uu = *(const u32x2 LAS*)(yp + T * 512);
        sk[T] = u32x2{pk(fmul(bl(uu[0]), dsk), fmul(bh(uu[0]), dsk)), pk(fmul(bl(uu[1]), dsk), fmul(bh(uu[1]), dsk))};
        asm volatile("" : "+v"(sk[T]));
      }
      f32x4 acc[8];
#pragma unroll
      for (int T = 0; T < 8; ++T) { acc[T] = f32x4{0.f, 0.f, 0.f, 0.f}; asm volatile("" : "+v"(acc[T])); }
      u32x4 kf[2][8], uf[2][8];
      auto rdk = [&](int be, int a) { kf[be & 1][a] = *(const u32x4 LAS*)(kp + be * 64 + a * 512); };
      auto rdu = [&](int be, int a) {
        const u32 LAS* q = (const u32 LAS*)(up[a >> 1] - be * 64 + (a & 1) * 512);
        uf[be & 1][a] = u32x4{q[0], q[1], q[2], q[3]};
      };
#pragma unroll
      for (int a = 0; a < 8; ++a) { rdk(0, a); rdu(0, a); }
      waitv<0>(); waitl<0>();
      __builtin_amdgcn_s_setprio(3);
      SB;
      bar();
      SB;
      LSTAMP(23);
      static_for<0, 288>([&](auto N) {
        constexpr int n = decltype(N)::value, be = n / 36, m = n % 36;
        constexpr int a = m < 8 ? 0 : m < 15 ? 1 : m < 21 ? 2 : m < 26 ? 3 : m < 30 ? 4 : m < 33 ? 5 : m < 35 ? 6 : 7;
        constexpr int T = a + m - (8 * a - a * (a - 1) / 2);
        acc[T] = mfma16(uf[be & 1][T - a], kf[be & 1][a], acc[T]);
        SB;
        if (be < 7 && m < 16 && (m & 1) == 0) rdu(be + 1, m >> 1);
        if (be < 7 && m >= 16 && m < 24) rdk(be + 1, (m - 16) & 7);
        if (n == 251) { waitl<0>(); issue_own(); }
        SB;
      });
#pragma unroll
      for (int T = 0; T < 8; ++T) yv[T] = u32x4{pk(acc[T][0], acc[T][1]), pk(acc[T][2], acc[T][3]), sk[T][0], sk[T][1]};
      __builtin_amdgcn_s_setprio(0);
    }
    LSTAMP(24);
    u64 fa = 0;
    static_for<1, 8>([&](auto K) {
      constexpr int st = 2 * decltype(K)::value;
      constexpr int yng = st == 14 ? 0 : g2_upto(st - 2) - (st + 2);
      if (st == 14 && wid == 7 && !__all(fa == tag)) flag_wait(ws, FL_A + dj * 36, 36, tag, lane);
      if (st > 2) { if (n0 == 0) waitv<5 * yng>(); else waitv<4 * yng>(); }
      LSTAMP(24 + st);
      bar();
      LSTAMP(25 + st);
      if (conv && st == 6) {
        const __amdgpu_buffer_rsrc_t ao = rsrc(ws + WS_A2 + (size_t)Q * 16384 + 4096 * p, 4096);
        const char LAS* tp = sm + LM_UIM + (c >> 2) * UIM_B + 64 + 2 * (512 * p + 16 * g) + (c & 3) * 8;
#pragma unroll
        for (int i = 0; i < 8; ++i) __builtin_amdgcn_raw_buffer_store_b64(tr_read(tp + i * 128), ao, lane * 8, i * 512, SC1);
      }
      if (conv && st == 8 && tid == 256) flag_set(ws, FL_A + Q, tag);
#pragma unroll
      for (int j = g2_upto(st - 2) + 1; j <= g2_upto(st); ++j) {
        if (p < 2) issue2(2 * j); else issue2(2 * j + 1);
      }
      if (st == 12 && wid == 7) fa = __hip_atomic_load((GAS u64*)(ws + (FL_A + dj * 36 + min(lane, 35)) * 128), __ATOMIC_RELAXED, __HIP_MEMORY_SCOPE_AGENT);
      if (conv && st <= 4) {
        const bool odd = c & 1;
        char LAS* yp = uim + 64 + 2 * (8 * (g + 4 * (c >> 1)) + 4 * (c & 1));
#pragma unroll
        for (int T = 2 * st - 4; T < 2 * st; ++T) {
          const u32 own = odd ? yv[T][1] : yv[T][0];
          const u32 rcv = (u32)__builtin_amdgcn_mov_dpp((int)(odd ? yv[T][0] : yv[T][1]), 0xB1, 0xF, 0xF, true);
          const u32 ev = odd ? rcv : own, od = odd ? own : rcv;
          *(u32x2 LAS*)(yp + T * 512) = u32x2{pk(fadd(bl(ev), bl(yv[T][2])), fadd(bl(od), bh(yv[T][2]))),
                                              pk(fadd(bh(ev), bl(yv[T][3])), fadd(bh(od), bh(yv[T][3])))};
        }
        waitl<0>();
      }
          });
    load_base();
  }
  u32x2 base[2][2];
#pragma unroll
  for (int k = 0; k < 2; ++k)
#pragma unroll
    for (int i = 0; i < 2; ++i)
      base[k][i] = u32x2{pk(fadd(bl(xv[k][i][0]), bl(av[k][i][0])), fadd(bh(xv[k][i][0]), bh(av[k][i][0]))),
                         pk(fadd(bl(xv[k][i][1]), bl(av[k][i][1])), fadd(bh(xv[k][i][1]), bh(av[k][i][1])))};
  waitv<0>(); waitl<0>();
  bar();
  LSTAMP(52);
  if (tid == 0) flag_set(ws, FL_X + tile * 4 + s, tag);
  if (tid == 64 && b == 0) flag_set(ws, FL_EPOCH, tag);
  flag_wait(ws, FL_X + tile * 4, 4, tag, lane);
  LSTAMP(53);
  const __amdgpu_buffer_rsrc_t rs = rsrc(ws + WS_SLAB + (size_t)tile * 16 * SLAB_B, 16 * SLAB_B);
  u32x4 part[2][3];
#pragma unroll
  for (int k = 0; k < 2; ++k)
    if (k == 0 || wid == 4) {
#pragma unroll
      for (int e = 0; e < 3; ++e) part[k][e] = ld128(rs, lane * 16, (s * 4 + ((s + 1 + e) & 3)) * SLAB_B + (k ? 8 : wid) * 1024);
    }
#pragma unroll
  for (int k = 0; k < 2; ++k)
    if (k == 0 || wid == 4) {
      const int t = k ? 8 : wid;
#pragma unroll
      for (int i = 0; i < 2; ++i) {
        f32x4 v = *(const f32x4 LAS*)(sm + LM_UIM + (2 * t + i) * 1024 + lane * 16);
#pragma unroll
        for (int e = 0; e < 3; ++e)
          v += f32x4{bl(part[k][e][2 * i]), bh(part[k][e][2 * i]), bl(part[k][e][2 * i + 1]), bh(part[k][e][2 * i + 1])};
        const u32 r0 = pk(v[0], v[1]), r1 = pk(v[2], v[3]);
        const u32 q0 = pk(bl(r0) + bl(bias[k][0]), bh(r0) + bh(bias[k][0])), q1 = pk(bl(r1) + bl(bias[k][1]), bh(r1) + bh(bias[k][1]));
        const int l = 128 * l2 + 32 * s + 16 * i + c, d = 144 * dj + 16 * t + 4 * g;
        *(u32x2*)(out + (size_t)l * D + d) =
            u32x2{pk(bl(base[k][i][0]) + bl(q0), bh(base[k][i][0]) + bh(q0)), pk(bl(base[k][i][1]) + bl(q1), bh(base[k][i][1]) + bh(q1))};
      }
    }
  LSTAMP(54);
  RSTAMP(59);
}

extern "C" void kernel_launch(void* const* d_in, const int* in_sizes, int n_in, void* d_out, int out_size, void* d_ws,
                              size_t ws_size, hipStream_t stream) {
  const u16* const* in = (const u16* const*)d_in;
  (void)hipFuncSetAttribute((const void*)k_all, hipFuncAttributeMaxDynamicSharedMemorySize, LM_END);
  k_all<<<256, 512, LM_END, stream>>>(in[0], in[1], in[2], in[3], in[4], in[5], in[6], in[7], in[8], in[10], in[11], in[12], in[13],
                                      (char*)d_ws + ((ws_size / 2) & ~(size_t)0x1FFFFF), (u16*)d_out);
}
```

```cpp
#define PAD_AT 55
#define PAD_US 30
#include <hip/hip_runtime.h>
#include <hip/hip_bf16.h>
#include <stdint.h>
#include <type_traits>

typedef unsigned short u16;
typedef unsigned int u32;
typedef unsigned long long u64;
typedef __bf16 bf16x8 __attribute__((ext_vector_type(8)));
typedef __bf16 bf16x2 __attribute__((ext_vector_type(2)));
typedef short s16x4 __attribute__((ext_vector_type(4)));
typedef float f32x4 __attribute__((ext_vector_type(4)));
typedef float f32x2 __attribute__((ext_vector_type(2)));
typedef u32 u32x4 __attribute__((ext_vector_type(4)));
typedef u32 u32x2 __attribute__((ext_vector_type(2)));
#define LAS __attribute__((address_space(3)))
#define GAS __attribute__((address_space(1)))
#define POLL_SLEEP 4
#define SB __builtin_amdgcn_sched_barrier(0)

constexpr int L = 2048, D = 576, NM = 128, F = 4608;

constexpr int FL_EPOCH = 0;
constexpr int FL_LN = 1;
constexpr int FL_A = FL_LN + 256;
constexpr int FL_H = FL_A + 144;
constexpr int FL_X = FL_H + 256;
constexpr size_t WS_XNF = 131072;
constexpr size_t WS_UT = WS_XNF + (size_t)L * D * 2;
constexpr size_t WS_A2 = WS_UT + (size_t)L * D * 2;
constexpr size_t WS_HF = WS_A2 + (size_t)L * D * 2;
constexpr size_t WS_SLAB = WS_HF + (size_t)L * F * 2;
constexpr int SLAB_B = 9216, HB = 73728;
constexpr size_t WS_END = WS_SLAB + (size_t)64 * 16 * SLAB_B;
#define LSTAMP(i)
#define RSTAMP(i) do { if ((i) == PAD_AT) { while (__builtin_amdgcn_s_memrealtime() - pad0 < PAD_US * 100ull) __builtin_amdgcn_s_sleep(1); } } while (0)

__device__ __forceinline__ float bl(u32 v) { return __uint_as_float(v << 16); }
__device__ __forceinline__ float bh(u32 v) { return __uint_as_float(v & 0xffff0000u); }
__device__ __forceinline__ float b2f(u16 v) { return __uint_as_float(((u32)v) << 16); }
__device__ __forceinline__ u32 pk(float lo, float hi) {
  bf16x2 r = {(__bf16)lo, (__bf16)hi};
  return __builtin_bit_cast(u32, r);
}
__device__ __forceinline__ float fadd(float a, float b) {
  float r;
  asm("v_add_f32 %0, %1, %2" : "=v"(r) : "v"(a), "v"(b));
  return r;
}
__device__ __forceinline__ float rb(float f) { return (float)(__bf16)f; }
__device__ __forceinline__ f32x4 mfma16(u32x4 a, u32x4 b, f32x4 c) {
  return __builtin_amdgcn_mfma_f32_16x16x32_bf16(__builtin_bit_cast(bf16x8, a), __builtin_bit_cast(bf16x8, b), c, 0, 0, 0);
}
__device__ __forceinline__ u32x2 tr_read(const char LAS* p) {
  return __builtin_bit_cast(u32x2, __builtin_amdgcn_ds_read_tr16_b64_v4i16((s16x4 LAS*)p));
}
__device__ __forceinline__ __amdgpu_buffer_rsrc_t rsrc(const void* p, int bytes) {
  return __builtin_amdgcn_make_buffer_rsrc((void*)p, 0, bytes, 0x00020000);
}
constexpr int SC1 = 16;
__device__ __forceinline__ void st128(u32x4 v, __amdgpu_buffer_rsrc_t r, int voff, int soff) {
  __builtin_amdgcn_raw_buffer_store_b128(v, r, voff, soff, SC1);
  asm volatile("s_nop 1" ::"v"(v) : "memory");
}
__device__ __forceinline__ u32x4 ld128(__amdgpu_buffer_rsrc_t r, int voff, int soff) {
  return __builtin_amdgcn_raw_buffer_load_b128(r, voff, soff, SC1);
}
__device__ __forceinline__ void glds16(const char* base, u32 voff, u32 dst) {
  asm volatile("s_mov_b32 m0, %2\n\ts_nop 0\n\tglobal_load_lds_dwordx4 %0, %1" ::"v"(voff), "s"(base), "s"(dst) : "memory");
}
template <int N> __device__ __forceinline__ void waitv() { asm volatile("s_waitcnt vmcnt(%0)" ::"i"(N) : "memory"); }
template <int N> __device__ __forceinline__ void waitl() { asm volatile("s_waitcnt lgkmcnt(%0)" ::"i"(N) : "memory"); }
__device__ __forceinline__ void bar() {
  asm volatile("" ::: "memory");
  __builtin_amdgcn_s_barrier();
  asm volatile("" ::: "memory");
}
template <int N> __device__ __forceinline__ void bars() {
#pragma unroll
  for (int i = 0; i < N; ++i) bar();
}
template <int I, int N, class Fn> __device__ __forceinline__ void static_for(Fn&& fn) {
  if constexpr (I < N) {
    fn(std::integral_constant<int, I>{});
    static_for<I + 1, N>(fn);
  }
}
__device__ __forceinline__ void flag_set(char* ws, int off, u64 tag) {
  __hip_atomic_store((GAS u64*)(ws + off * 128), tag, __ATOMIC_RELAXED, __HIP_MEMORY_SCOPE_AGENT);
}
__device__ __forceinline__ void flag_wait(char* ws, int off, int n, u64 tag, int lane) {
  GAS u64* p = (GAS u64*)(ws + (off + min(lane, n - 1)) * 128);
  for (u32 spin = 0; spin < (1u << 20); ++spin) {
    if (__all(__hip_atomic_load(p, __ATOMIC_RELAXED, __HIP_MEMORY_SCOPE_AGENT) == tag)) break;
    __builtin_amdgcn_s_sleep(POLL_SLEEP);
  }
  asm volatile("" ::: "memory");
}
__device__ __forceinline__ u32 lds_add(char LAS* p) {
  return __hip_atomic_fetch_add((u32 LAS*)p, 1u, __ATOMIC_RELAXED, __HIP_MEMORY_SCOPE_WORKGROUP);
}
__device__ __forceinline__ void lds_wait4(char LAS* p) {
  while (__builtin_amdgcn_readfirstlane(*(volatile u32 LAS*)p) < 4u) __builtin_amdgcn_s_sleep(1);
  asm volatile("" ::: "memory");
}

__device__ __forceinline__ float fmul(float a, float b) {
  float r;
  asm("v_mul_f32 %0, %1, %2" : "=v"(r) : "v"(a), "v"(b));
  return r;
}
__device__ __forceinline__ f32x2 cmul(f32x2 a, f32x2 b) {
  float re, im;
  asm("v_mul_f32 %0, %2, %4\n\tv_mul_f32 %1, %2, %5\n\tv_fma_f32 %0, -%3, %5, %0\n\tv_fma_f32 %1, %3, %4, %1"
      : "=&v"(re), "=&v"(im) : "v"(a[0]), "v"(a[1]), "v"(b[0]), "v"(b[1]));
  return f32x2{re, im};
}
template <int E> __device__ __forceinline__ f32x2 zpow2(float p2, float qh, float ql) {
  constexpr float s = (float)(1 << E);
  const float mag = __builtin_amdgcn_exp2f(p2 * s);
  const float ph = __builtin_fmaf(ql, s, __builtin_amdgcn_fractf(qh * s));
  float re, im;
  asm("s_nop 1\n\tv_mul_f32 %0, %2, %4\n\tv_mul_f32 %1, %3, %4"
      : "=&v"(re), "=&v"(im) : "v"(__builtin_amdgcn_cosf(ph)), "v"(__builtin_amdgcn_sinf(ph)), "v"(mag));
  return f32x2{re, im};
}
template <int CTRL> __device__ __forceinline__ float dpp_add(float v) {
  return v + __builtin_bit_cast(float, __builtin_amdgcn_update_dpp(0, __builtin_bit_cast(int, v), CTRL, 0xF, 0xF, true));
}
__device__ __forceinline__ float sum32(float v) {
  v = dpp_add<0x128>(v);
  v = dpp_add<0x124>(v);
  v = dpp_add<0x122>(v);
  v = dpp_add<0x121>(v);
  return v + __builtin_bit_cast(float, __builtin_amdgcn_ds_swizzle(__builtin_bit_cast(int, v), 0x401F));
}
__device__ __forceinline__ u32 gelu2(float a, float b) {
  constexpr float C0 = -2.f * 1.44269504088896340736f * 0.7978845608028654f, C1 = C0 * 0.044715f;
  const f32x2 v = {a, b};
  const f32x2 z = v * __builtin_elementwise_fma(v * v, f32x2{C1, C1}, f32x2{C0, C0});
  const f32x2 d = f32x2{__builtin_amdgcn_exp2f(z[0]), __builtin_amdgcn_exp2f(z[1])} + f32x2{1.f, 1.f};
  const f32x2 r = v * f32x2{__builtin_amdgcn_rcpf(d[0]), __builtin_amdgcn_rcpf(d[1])};
  return pk(r[0], r[1]);
}
__device__ __forceinline__ u32x4 gelu8(f32x4 a, f32x4 b) {
  return u32x4{gelu2(a[0], a[1]), gelu2(a[2], a[3]), gelu2(b[0], b[1]), gelu2(b[2], b[3])};
}

constexpr int SLOT = 9216;
constexpr int KIM_C1 = 4640, UIM_B = 5952, KG_S = 272, LN_S = 1168;
constexpr int LM_KIM = 10 * SLOT;
constexpr int LM_UIM = 14 * SLOT;
constexpr int LM_LN = LM_UIM + 4 * UIM_B;
constexpr int LM_CNT = LM_LN + 8 * LN_S;
constexpr int LM_END = LM_CNT + 32;
static_assert(LM_END <= 163840 && 4 * UIM_B >= 18432 && 21 * KG_S <= UIM_B && 32 * KG_S <= SLOT && UIM_B % 256 == 64, "LDS map");
constexpr int slot2(int j) { return j % 14 < 10 ? (j % 14 + 8) % 10 : j % 14; }

#define G2_XP 12
#define G2_X0 8
#define CODE_LINES 590
constexpr int G1_N0 = 4, G1_N1 = 8, G1_NB = 7;
constexpr int G1_POS[G1_NB + 1] = {4, 6, 8, 10, 12, 14, 16, 18};
constexpr int G1_UPTO[G1_NB] = {11, 14, 16, 18, 20, 22, 24};
constexpr bool g1_bar_before(int ks) {
  for (int i = 0; i < G1_NB; ++i)
    if (G1_POS[i] == ks) return true;
  return false;
}
constexpr int g2_upto(int st) { return st < 2 ? 6 : st + 6 < 17 ? st + 6 : 17; }

__global__ __launch_bounds__(512, 2) void k_all(const u16* __restrict__ x, const u16* __restrict__ Are, const u16* __restrict__ Aim,
                                                const u16* __restrict__ Bre, const u16* __restrict__ Bim, const u16* __restrict__ Cre,
                                                const u16* __restrict__ Cim, const u16* __restrict__ logdt, const u16* __restrict__ Dskip,
                                                const u16* __restrict__ w1, const u16* __restrict__ b1, const u16* __restrict__ w2,
                                                const u16* __restrict__ b2, char* ws, u16* __restrict__ out) {
  extern __shared__ __attribute__((aligned(16))) char smem[];
  char LAS* sm = (char LAS*)smem;
  const u32 smb = (u32)(uintptr_t)sm;
  const int tid = threadIdx.x, lane = tid & 63, wid = __builtin_amdgcn_readfirstlane(tid >> 6);
  const int b = blockIdx.x, xg = b & 7, idx = b >> 3, s = xg & 3;
  const int lb = 4 * (xg >> 2) + (idx >> 3), fb = 8 * s + (idx & 7);
  const int l2 = 8 * (xg >> 2) + (idx >> 2), dj = idx & 3, tile = l2 * 4 + dj;
  const int cl = xg * 4 + (idx >> 3);
  const int c = lane & 15, g = lane >> 4;
  const char* pc0;
  asm volatile("s_getpc_b64 %0" : "=s"(pc0));
  u64 tag;
  const u64 pad0 = __builtin_amdgcn_s_memrealtime();
  LSTAMP(63);
  u32x2 xv[2][2] = {}, av[2][2] = {}, bias[2] = {};
  auto load_base = [&]() {
    const __amdgpu_buffer_rsrc_t ra = rsrc(ws + WS_A2, L * D * 2);
#pragma unroll
    for (int k = 0; k < 2; ++k) {
      const int t = k ? 8 : wid;
      if (k == 0 || wid == 4) {
        const int d = 144 * dj + 16 * t + 4 * g;
        bias[k] = *(const u32x2*)(b2 + d);
#pragma unroll
        for (int i = 0; i < 2; ++i) {
          const int l = 128 * l2 + 32 * s + 16 * i + c;
          xv[k][i] = *(const u32x2*)(x + (size_t)l * D + d);
          av[k][i] = __builtin_amdgcn_raw_buffer_load_b64(ra, ((d >> 2) * L + l) * 8, 0, SC1);
        }
      }
    }
  };
  if (wid < 4) {
    __builtin_amdgcn_s_setprio(2);
    u32x2 bq[9];
    {
      const int r = tid >> 5, lam = tid & 31;
      const u32* src = (const u32*)(x + (size_t)(b * 8 + r) * D) + lam;
      u32 raw[9];
#pragma unroll
      for (int i = 0; i < 9; ++i) raw[i] = src[32 * i];
      const u64 ep = __hip_atomic_load((GAS u64*)(ws + FL_EPOCH), __ATOMIC_RELAXED, __HIP_MEMORY_SCOPE_AGENT);
#pragma unroll
      for (int t = 0; t < 9; ++t) bq[t] = *(const u32x2*)(b1 + fb * 144 + 16 * t + 4 * g);
      if (tid == 0) *(u32x4 LAS*)(sm + LM_CNT) = u32x4{0u, 0u, 0u, 0u};
      waitl<0>();
      bar();
      f32x2 v[9];
#pragma unroll
      for (int i = 0; i < 9; ++i) v[i] = f32x2{bl(raw[i]), bh(raw[i])};
      f32x2 s2 = v[0];
#pragma unroll
      for (int i = 1; i < 9; ++i) s2 += v[i];
      const float mean = sum32(s2[0] + s2[1]) * (1.f / D);
      LSTAMP(0);
      f32x2 q2 = {0.f, 0.f};
#pragma unroll
      for (int i = 0; i < 9; ++i) { f32x2 d = v[i] - f32x2{mean, mean}; q2 = __builtin_elementwise_fma(d, d, q2); }
      const float q = sum32(q2[0] + q2[1]);
      const float m = rb(mean);
      const float sd = rb(__builtin_sqrtf(rb(rb(q * (1.f / D)) + 1e-5f)));
      const float rs = __builtin_amdgcn_rcpf(sd);
#pragma unroll
      for (int i = 0; i < 9; ++i) {
        const f32x2 d = v[i] - f32x2{m, m};
        const u32 t = pk(d[0], d[1]);
        const f32x2 n = f32x2{bl(t), bh(t)} * f32x2{rs, rs};
        *(u32 LAS*)(sm + LM_LN + r * LN_S + (lam + 32 * i) * 4) = pk(n[0], n[1]);
      }
      const u64 t64 = ep * 0x9E3779B97F4A7C15ull + 0xD1B54A32D192ED03ull;
      tag = ((u64)__builtin_amdgcn_readfirstlane((u32)(t64 >> 32)) << 32) | __builtin_amdgcn_readfirstlane((u32)t64);
      if (tid == 0) *(u64 LAS*)(sm + LM_CNT + 16) = tag;
      waitl<0>();
      if (lane == 0) lds_add(sm + LM_CNT);
      lds_wait4(sm + LM_CNT);
      LSTAMP(1);
      const __amdgpu_buffer_rsrc_t xo = rsrc(ws + WS_XNF + (size_t)(b >> 1) * 18432 + (b & 1) * 128, 18432);
      const __amdgpu_buffer_rsrc_t uo = rsrc(ws + WS_UT + (size_t)b * 9216, 9216);
      const char LAS* xp = sm + LM_LN + (lane & 7) * LN_S + (lane >> 5) * 64 + ((lane >> 3) & 3) * 8;
      const char LAS* tp = sm + LM_LN + ((lane >> 2) & 3) * LN_S + (lane >> 4) * 32 + (lane & 3) * 8;
      const int xoff = (lane >> 5) * 1024 + ((lane >> 3) & 3) * 256 + (lane & 7) * 16;
#pragma unroll
      for (int kk = 0; kk < 3; ++kk) {
        const int k = wid + 4 * kk;
        if (k < 9) {
          const u32x2 a = *(const u32x2 LAS*)(xp + k * 128), bb = *(const u32x2 LAS*)(xp + k * 128 + 32);
          const u32x2 t0 = tr_read(tp + k * 128), t1 = tr_read(tp + k * 128 + 4 * LN_S);
          st128(u32x4{a[0], a[1], bb[0], bb[1]}, xo, xoff, k * 2048);
          st128(u32x4{t0[0], t0[1], t1[0], t1[1]}, uo, lane * 16, k * 1024);
        }
      }
    }
    {
      constexpr int XP = 3, WD = 3, WR = 4;
      const int lt0 = 16 * lb + 4 * wid;
      const __amdgpu_buffer_rsrc_t rx = rsrc(ws + WS_XNF + (size_t)lt0 * 18432, 4 * 18432);
      f32x4 acc[9][4];
#pragma unroll
      for (int t = 0; t < 9; ++t) {
#pragma unroll
        for (int i = 0; i < 4; ++i) {
          acc[t][i] = f32x4{bl(bq[t][0]), bh(bq[t][0]), bl(bq[t][1]), bh(bq[t][1])};
          asm volatile("" : "+v"(acc[t][i]));
        }
      }
      waitv<0>();
      LSTAMP(2);
      if (lane == 0 && lds_add(sm + LM_CNT + 4) == 3u) flag_set(ws, FL_LN + b, tag);
      flag_wait(ws, FL_LN + 32 * lb + 8 * wid, 8, tag, lane);
      LSTAMP(3);
      u32x4 xf[XP + 1][4];
#pragma unroll
      for (int k = 0; k < XP; ++k)
#pragma unroll
        for (int i = 0; i < 4; ++i) xf[k][i] = ld128(rx, lane * 16, i * 18432 + k * 1024);
      SB;
      const char LAS* wp = sm + (lane >> 2) * 288 + (lane & 3) * 8;
      u32x2 wa[WR], wb[WR];
      const char LAS* wph = wp + 6 * SLOT;
      asm volatile("" : "+v"(wph));
      auto wptr = [&](int n) {
        const int sl = (n / 9) % 10;
        return (sl < 6 ? wp + sl * SLOT : wph + (sl - 6) * SLOT) + (n % 9) * 32;
      };
      lds_wait4(sm + LM_CNT + 12);
      LSTAMP(4);
      RSTAMP(55);
#pragma unroll
      for (int n = 0; n < WD; ++n) { wa[n] = tr_read(wptr(n)); wb[n] = tr_read(wptr(n) + 16 * 288); }
#pragma unroll
      for (int ks = 0; ks < 18; ++ks) {
#pragma unroll
        for (int t = 0; t < 9; ++t) {
          const int n = 9 * ks + t;
          const u32x4 w = {wa[n % WR][0], wa[n % WR][1], wb[n % WR][0], wb[n % WR][1]};
          acc[t][0] = mfma16(w, xf[ks % (XP + 1)][0], acc[t][0]);
          SB;
          if (t == 9 - WD && g1_bar_before(ks + 1)) bar();
          if (n == 161) bar();
          if (n + WD < 162) wa[(n + WD) % WR] = tr_read(wptr(n + WD));
          SB;
          acc[t][1] = mfma16(w, xf[ks % (XP + 1)][1], acc[t][1]);
          SB;
          if (n + WD < 162) wb[(n + WD) % WR] = tr_read(wptr(n + WD) + 16 * 288);
          SB;
          acc[t][2] = mfma16(w, xf[ks % (XP + 1)][2], acc[t][2]);
          SB;
          if (t < 4 && ks + XP < 18) xf[(ks + XP) % (XP + 1)][t] = ld128(rx, lane * 16, t * 18432 + (ks + XP) * 1024);
          SB;
          acc[t][3] = mfma16(w, xf[ks % (XP + 1)][3], acc[t][3]);
          SB;
        }
      }
      asm volatile("s_nop 15");
      SB;
      LSTAMP(5);
      RSTAMP(56);
      const __amdgpu_buffer_rsrc_t rh = rsrc(ws + WS_HF + (size_t)(cl * 8 + (idx & 7)) * HB, HB);
#pragma unroll
      for (int i = 0; i < 4; ++i)
#pragma unroll
        for (int r = 0; r < 4; ++r) st128(gelu8(acc[2 * r][i], acc[2 * r + 1][i]), rh, lane * 16, (4 * wid + i) * 4096 + r * 1024);
#pragma unroll
      for (int i = 0; i < 2; ++i) st128(gelu8(acc[8][2 * i], acc[8][2 * i + 1]), rh, lane * 16, 65536 + (2 * wid + i) * 1024);
      LSTAMP(6);
    }
    bar();
    {
      constexpr int XP = G2_XP, X0 = G2_X0, WD = 5, WR = 6, TAIL = 4;
      const __amdgpu_buffer_rsrc_t rh = rsrc(ws + WS_HF + (size_t)cl * 8 * HB, 8 * HB);
      const int pr = 4 * ((idx >> 2) & 1) + wid;
      const __amdgpu_buffer_rsrc_t rs = rsrc(ws + WS_SLAB + (size_t)tile * 16 * SLAB_B, 16 * SLAB_B);
      f32x4 acc[9][2];
#pragma unroll
      for (int t = 0; t < 9; ++t)
#pragma unroll
        for (int i = 0; i < 2; ++i) {
          acc[t][i] = f32x4{0.f, 0.f, 0.f, 0.f};
          asm volatile("" : "+v"(acc[t][i]));
        }
      const char LAS* wp = sm + (lane >> 2) * 288 + (lane & 3) * 8;
      u32x2 wa[WR], wb[WR];
      constexpr auto ord_ks = [](int n) { return n < 9 * (36 - TAIL) ? n / 9 : 36 - TAIL + (n - 9 * (36 - TAIL)) % TAIL; };
      constexpr auto ord_t = [](int n) { return n < 9 * (36 - TAIL) ? n % 9 : (n - 9 * (36 - TAIL)) / TAIL; };
      const char LAS* wph = wp + 7 * SLOT;
      asm volatile("" : "+v"(wph));
      auto wptr = [&](int n) {
        const int sl = slot2(ord_ks(n));
        return (sl < 7 ? wp + sl * SLOT : wph + (sl - 7) * SLOT) + ord_t(n) * 32;
      };
#pragma unroll
      for (int n = 0; n < WD; ++n) { wa[n] = tr_read(wptr(n)); wb[n] = tr_read(wptr(n) + 16 * 288); }
      u32x4 xf[XP + 1][2];
      auto hload = [&](int k, int i) {
        const int r = k % 9, P = ((idx >> 1) + k / 9) & 3;
        const int so = r < 8 ? (2 * P + (((r >> 2) ^ idx) & 1)) * HB + pr * 8192 + i * 4096 : (2 * P + i) * HB + 65536 + pr * 1024;
        xf[k % (XP + 1)][i] = ld128(rh, lane * 16 + (r < 8 ? (r & 3) * 1024 : 0), so);
      };
      waitv<0>();
      LSTAMP(7);
      const int flh = FL_H + cl * 8;
      if (lane == 0 && lds_add(sm + LM_CNT + 8) == 3u) flag_set(ws, flh + (idx & 7), tag);
      lds_wait4(sm + LM_CNT + 8);
#pragma unroll
      for (int k = 0; k < 4; ++k) { hload(k, 0); hload(k, 1); }
      flag_wait(ws, flh, 8, tag, lane);
      LSTAMP(8);
      RSTAMP(57);
#pragma unroll
      for (int k = 4; k < X0; ++k) { hload(k, 0); hload(k, 1); }
      SB;
      auto flush = [&](auto OWN, int tt) {
        if (decltype(OWN)::value) {
          *(f32x4 LAS*)(sm + LM_UIM + (2 * tt) * 1024 + lane * 16) = acc[tt][0];
          *(f32x4 LAS*)(sm + LM_UIM + (2 * tt + 1) * 1024 + lane * 16) = acc[tt][1];
        } else {
          const u32x4 v = {pk(acc[tt][0][0], acc[tt][0][1]), pk(acc[tt][0][2], acc[tt][0][3]), pk(acc[tt][1][0], acc[tt][1][1]), pk(acc[tt][1][2], acc[tt][1][3])};
          st128(v, rs, lane * 16, (wid * 4 + s) * SLAB_B + tt * 1024);
        }
      };
      auto step = [&](auto N, auto OWN) {
        constexpr int n = decltype(N)::value;
        constexpr int ks = ord_ks(n), t = ord_t(n);
        if (ks % 9 == 8 && t == 0) {
          const u32x4 a = xf[ks % (XP + 1)][0], bb = xf[ks % (XP + 1)][1];
          xf[ks % (XP + 1)][0] = u32x4{a[0], a[1], bb[0], bb[1]};
          xf[ks % (XP + 1)][1] = u32x4{a[2], a[3], bb[2], bb[3]};
        }
        const u32x4 w = {wa[n % WR][0], wa[n % WR][1], wb[n % WR][0], wb[n % WR][1]};
        acc[t][0] = mfma16(w, xf[ks % (XP + 1)][0], acc[t][0]);
        SB;
        if ((n + WD) % 36 == 18 && n + WD >= 54 && n + WD <= 270) bar();
        if (n + WD < 324) wa[(n + WD) % WR] = tr_read(wptr(n + WD));
        SB;
        acc[t][1] = mfma16(w, xf[ks % (XP + 1)][1], acc[t][1]);
        SB;
        if (n + WD < 324) wb[(n + WD) % WR] = tr_read(wptr(n + WD) + 16 * 288);
        if (ks < XP - X0 && (t & 1) && t < 8) hload(X0 + 2 * ks + (t >> 2), (t >> 1) & 1);
        if (ks >= XP - X0 && (t == 2 || t == 6) && ks + XP < 36) hload(ks + XP, (t >> 2) & 1);
        if (ks == 30 && t == 2) load_base();
        if (ks == 37 - TAIL && t > 0 && n >= 9 * (36 - TAIL)) flush(OWN, t > 0 ? t - 1 : 0);
        SB;
      };
      static_for<0, 9 * (36 - TAIL) + TAIL>([&](auto N) { step(N, std::false_type{}); });
      if (wid == s) {
        static_for<9 * (36 - TAIL) + TAIL, 324>([&](auto N) { step(N, std::true_type{}); });
        flush(std::true_type{}, 8);
      } else {
        static_for<9 * (36 - TAIL) + TAIL, 324>([&](auto N) { step(N, std::false_type{}); });
        flush(std::false_type{}, 8);
      }
      LSTAMP(9);
      RSTAMP(58);
    }
  } else {
    const bool conv = idx < 18;
    const int Q = xg * 18 + idx, p = wid - 4, n0 = (p & 1) * 5;
    u32 par[2][6], vdt, vds;
    {
      const int ch = conv ? 4 * Q + p : 0;
      const u32 o0 = (ch * NM + lane) * 2, o1 = ch * 2;
      const u16* src[6] = {Are, Aim, Bre, Bim, Cre, Cim};
#pragma unroll
      for (int i = 0; i < 6; ++i)
        asm volatile("global_load_ushort %0, %2, %3\n\tglobal_load_ushort %1, %2, %3 offset:128" : "=&v"(par[0][i]), "=&v"(par[1][i]) : "v"(o0), "s"(src[i]));
      asm volatile("global_load_ushort %0, %2, %3\n\tglobal_load_ushort %1, %2, %4" : "=&v"(vdt), "=&v"(vds) : "v"(o1), "s"(logdt), "s"(Dskip));
    }
    bar();
    u32 cr[9], cc[9];
#pragma unroll
    for (int n = 0; n < 9; ++n) { const int ci = 64 * n + lane; cr[n] = ci / 18; cc[n] = (ci % 18) * 16; }
    u32 v1[5], v2[9];
#pragma unroll
    for (int n = 0; n < 5; ++n) v1[n] = n0 ? cr[n < 4 ? 5 + n : 8] * (F * 2) + cc[n < 4 ? 5 + n : 8] : cr[n] * (F * 2) + cc[n];
    const char* w1s = (const char*)(w1 + fb * 144);
    auto issue1 = [&](int k) {
      const char* sp = w1s + (size_t)k * 32 * F * 2;
      const u32 dst = smb + (k % 10) * SLOT + n0 * 1024;
#pragma unroll
      for (int n = 0; n < 4; ++n) glds16(sp, v1[n], dst + n * 1024);
      if (n0 == 0) glds16(sp, v1[4], dst + 4096);
    };
#pragma unroll
    for (int k = 0; k < G1_N0 / 2; ++k) issue1(2 * k + (p >> 1));
    u32 cpf;
    {
      const u32 co = min(idx + 32 * (16 * p + (lane & 15)), CODE_LINES - 1) * 128;
      asm volatile("global_load_dword %0, %1, %2" : "=v"(cpf) : "v"(co), "s"(pc0));
    }
    LSTAMP(0);
#pragma unroll
    for (int n = 0; n < 9; ++n) v2[n] = cr[n] * (D * 2) + cc[n];
    const u32 v24s = v2[4] + (lane >= 32 ? 128 * D * 2 : 0);
    const char* w2s = (const char*)(w2 + (size_t)1152 * s * D + 144 * dj);
    auto ksrc = [&](int k) {
      const int r = k % 9;
      return w2s + (size_t)(288 * (((idx >> 1) + k / 9) & 3) + (r < 8 ? 144 * (((r >> 2) ^ idx) & 1) + 32 * (r & 3) : 128)) * (D * 2);
    };
    auto issue2 = [&](int k) {
      const char* sp = ksrc(k);
      const u32 dst = smb + slot2(k) * SLOT;
      if (n0 == 0) {
#pragma unroll
        for (int n = 0; n < 4; ++n) glds16(sp, v2[n], dst + n * 1024);
        glds16(sp, k % 9 == 8 ? v24s : v2[4], dst + 4096);
      } else {
#pragma unroll
        for (int n = 5; n < 9; ++n) glds16(sp + (k % 9 == 8 ? 128 * D * 2 : 0), v2[n], dst + n * 1024);
      }
    };
    auto issue_own = [&]() {
      const char* sp = ksrc(10 + p);
#pragma unroll
      for (int n = 0; n < 9; ++n) glds16(sp, v2[n], smb + (10 + p) * SLOT + n * 1024);
    };
    auto batch2 = [&]() {
      waitv<0>();
      asm volatile("" ::"v"(cpf));
      if (lane == 0) lds_add(sm + LM_CNT + 12);
#pragma unroll
      for (int k = G1_N0 / 2; k < G1_N1 / 2; ++k) issue1(2 * k + (p >> 1));
    };
    char LAS* kim = sm + LM_KIM + p * SLOT;
    char LAS* uim = sm + LM_UIM + p * UIM_B;
    if (conv) {
      LSTAMP(3);
      waitv<4 * (G1_N0 / 2) + 1>();
#pragma unroll
      for (int i = 0; i < 6; ++i) asm volatile("" : "+v"(par[0][i]), "+v"(par[1][i]));
      asm volatile("" : "+v"(vdt), "+v"(vds));
      const float dt = rb(__expf(bl(vdt)));
      f32x4 ka[2][2];
#pragma unroll
      for (int i = 0; i < 4; ++i) ka[i >> 1][i & 1] = f32x4{0.f, 0.f, 0.f, 0.f};
#pragma unroll 1
      for (int h = 0; h < 2; ++h) {
        float pv[6];
#pragma unroll
        for (int i = 0; i < 6; ++i) pv[i] = bl(h ? par[1][i] : par[0][i]);
        const float p2 = dt * pv[0] * 1.44269504088896340736f;
        const float q = dt * pv[1];
        constexpr float CH = 0.15915494309189535f;
        constexpr float CL = (float)(0.15915494309189533577 - (double)CH);
        const float qh = q * CH;
        const float ql = __builtin_fmaf(q, CL, __builtin_fmaf(q, CH, -qh));
        asm volatile("" ::"v"(ql));
        LSTAMP(50 + h);
        {
          f32x2 Z[16];
          auto put = [&](int i, f32x2 v) { *(u32 LAS*)(kim + i * KG_S + lane * 4) = pk(v[0], v[1]); };
          Z[0] = f32x2{1.f, 0.f};
          Z[1] = zpow2<3>(p2, qh, ql);
          Z[4] = zpow2<5>(p2, qh, ql);
          const f32x2 a7 = zpow2<7>(p2, qh, ql);
          Z[2] = cmul(Z[1], Z[1]);
          Z[8] = cmul(Z[4], Z[4]);
          Z[3] = cmul(Z[2], Z[1]);
#pragma unroll
          for (int i = 1; i < 4; ++i) Z[4 + i] = cmul(Z[4], Z[i]);
#pragma unroll
          for (int i = 1; i < 8; ++i) Z[8 + i] = cmul(Z[8], Z[i]);
#pragma unroll
          for (int i = 0; i < 16; ++i) put(i, Z[i]);
          put(16, a7);
#pragma unroll
          for (int i = 1; i < 16; ++i) put(16 + i, cmul(a7, Z[i]));
        }
        {
          f32x2 H[16];
          auto put = [&](int i, f32x2 v) { *(u32 LAS*)(uim + i * KG_S + lane * 4) = pk(v[0], -v[1]); };
          H[0] = cmul(f32x2{pv[4], pv[5]}, f32x2{fmul(dt, pv[2]), fmul(dt, pv[3])});
          const f32x2 a0 = zpow2<0>(p2, qh, ql), a8 = zpow2<8>(p2, qh, ql), a10 = zpow2<10>(p2, qh, ql);
          const f32x2 a1 = cmul(a0, a0), a2 = cmul(a1, a1), a9 = cmul(a8, a8), a11 = cmul(a10, a10);
          H[1] = cmul(a0, H[0]);
          H[2] = cmul(a1, H[0]);
          H[3] = cmul(a1, H[1]);
#pragma unroll
          for (int i = 0; i < 4; ++i) H[4 + i] = cmul(a2, H[i]);
#pragma unroll
          for (int i = 0; i < 4; ++i) H[8 + i] = cmul(a8, H[2 * i]);
          H[12] = cmul(a9, H[0]);
          H[13] = cmul(a9, H[4]);
          H[14] = cmul(a9, H[8]);
          H[15] = cmul(a9, H[10]);
#pragma unroll
          for (int i = 0; i < 16; ++i) put(i, H[i]);
          put(16, cmul(a10, H[0]));
          put(17, cmul(a10, H[8]));
          put(18, cmul(a10, H[12]));
          put(19, cmul(a10, H[14]));
          put(20, cmul(a11, H[0]));
        }
        lds_wait4(sm + LM_CNT);
#pragma unroll
        for (int k = 0; k < 4; ++k) {
          u32x4 lo[2], hi[2];
#pragma unroll
          for (int i = 0; i < 2; ++i) {
            lo[i] = *(const u32x4 LAS*)(kim + (16 * i + c) * KG_S + k * 64 + g * 16);
            hi[i] = *(const u32x4 LAS*)(uim + min(16 * i + c, 20) * KG_S + k * 64 + g * 16);
          }
#pragma unroll
          for (int i = 0; i < 4; ++i) ka[i >> 1][i & 1] = mfma16(lo[i & 1], hi[i >> 1], ka[i >> 1][i & 1]);
        }
        if (h == 0) batch2();
      }
      LSTAMP(1);
#pragma unroll
      for (int i = 0; i < 4; ++i)
        *(u32x2 LAS*)(uim + (16 * (i >> 1) + c) * 64 + (i & 1) * 32 + g * 8) =
            u32x2{pk(ka[i >> 1][i & 1][0], ka[i >> 1][i & 1][1]), pk(ka[i >> 1][i & 1][2], ka[i >> 1][i & 1][3])};
      if (lane < 56) *(u32x4 LAS*)(kim + (lane & 1) * KIM_C1 + (lane >> 1) * 16) = u32x4{0u, 0u, 0u, 0u};
      char LAS* ko = kim + (28 + lane) * 16;
      auto put = [&](int mi, const u32(&kv)[9]) {
        *(u32x4 LAS*)(ko + mi * 1024) = u32x4{kv[1] | (kv[2] << 16), kv[3] | (kv[4] << 16), kv[5] | (kv[6] << 16), kv[7] | (kv[8] << 16)};
        *(u32x4 LAS*)(ko + mi * 1024 + KIM_C1) = u32x4{kv[0] | (kv[1] << 16), kv[2] | (kv[3] << 16), kv[4] | (kv[5] << 16), kv[6] | (kv[7] << 16)};
      };
      {
        u32 kv[9];
#pragma unroll
        for (int e = 0; e < 9; ++e) {
          constexpr int EX[9] = {448, 384, 320, 256, 192, 128, 64, 0, 446}, R2[9] = {450, 640, 576, 576, 576, 512, 448, 448, 448};
          int o = 2 * lane + (lane < (e == 8 ? 33 : 32) ? EX[e] : R2[e]);
          if (e == 0 && lane == 63) o = 768;
          kv[e] = *(const u16 LAS*)(uim + o);
        }
        if (lane == 0) kv[8] = 0u;
        put(0, kv);
      }
      {
        const int oa = 768 + 128 * (lane >> 5) + 2 * (lane & 31), oe = 768 + 128 * ((lane + 1) >> 5) + 2 * ((lane + 1) & 31);
        const u32 a = *(const u16 LAS*)(uim + oa), bb = *(const u16 LAS*)(uim + oa + 64), e = *(const u16 LAS*)(uim + oe);
        const u32 kv[9] = {e, e, bb, bb, bb, a, a, a, a};
        put(1, kv);
      }
#pragma unroll
      for (int mi = 2; mi < 4; ++mi) {
        const u32 a = *(const u16 LAS*)(uim + 768 + 128 * mi + 2 * lane), e = *(const u16 LAS*)(uim + 770 + 128 * mi + 2 * lane);
        const u32 tie = (lane & 1) ? e : a;
        const u32 kv[9] = {e, e, e, tie, a, a, a, a, a};
        put(mi, kv);
      }
      waitl<0>();
      if (lane < 4) *(u32x4 LAS*)(uim + lane * 16) = u32x4{0u, 0u, 0u, 0u};
      waitl<0>();
    } else {
      batch2();
#pragma unroll
      for (int i = 0; i < 6; ++i) asm volatile("" ::"v"(par[0][i]), "v"(par[1][i]));
      asm volatile("" ::"v"(vdt), "v"(vds));
    }
    LSTAMP(2);
    lds_wait4(sm + LM_CNT);
    tag = *(const u64 LAS*)(sm + LM_CNT + 16);
    u32x4 uv[4], yv[8];
    if (conv) {
      flag_wait(ws, FL_LN + p * 64, 64, tag, lane);
      const __amdgpu_buffer_rsrc_t ru = rsrc(ws + WS_UT, L * D * 2);
#pragma unroll
      for (int j = 0; j < 4; ++j) uv[j] = ld128(ru, (64 * p + (lane >> 2)) * 9216 + Q * 64 + (lane & 3) * 16, j * 16 * 9216);
    }
    waitv<0>();
    static_for<0, G1_NB>([&](auto K) {
      constexpr int i = decltype(K)::value;
      constexpr int prev = i ? G1_UPTO[i ? i - 1 : 0] : G1_N1 - 1;
      static_assert(G1_UPTO[i] - 10 < G1_POS[i] - 1 || G1_UPTO[i] < 10, "ring schedule");
      constexpr int need = G1_POS[i + 1] - 1;
      constexpr int y0 = prev / 2 - need / 2, y1 = (prev + 1) / 2 - (need + 1) / 2;
      if (need >= G1_N1) {
        if (p == 0) waitv<5 * y0>(); else if (p == 1) waitv<4 * y0>(); else if (p == 2) waitv<5 * y1>(); else waitv<4 * y1>();
      }
      LSTAMP(4 + 2 * i);
      bar();
      LSTAMP(5 + 2 * i);
      if (i == 0 && conv) {
#pragma unroll
        for (int j = 0; j < 4; ++j) *(u32x4 LAS*)(sm + LM_UIM + (lane & 3) * UIM_B + 64 + (64 * p + 16 * j + (lane >> 2)) * 16) = uv[j];
        waitl<0>();
      }
#pragma unroll
      for (int u = prev + 1; u <= G1_UPTO[i]; ++u)
        if ((u & 1) == (p >> 1)) { if (u < 18) issue1(u); else issue2(u - 18); }
    });
    bar();
    LSTAMP(22);
#pragma unroll
    for (int u = G1_UPTO[G1_NB - 1] + 1; u < 28; ++u)
      if ((u & 1) == (p >> 1)) issue2(u - 18);
    if (!conv) {
      waitv<0>();
      bar();
      LSTAMP(23);
      issue_own();
    } else {
      const float dsk = bl(vds);
      const char LAS* kp = kim + (c & 1) * KIM_C1 + (4 * (c >> 1) + g) * 16;
      char LAS* ub = uim + 64;
      const char LAS* up[4];
#pragma unroll
      for (int i = 0; i < 4; ++i) {
        const char LAS* q = ub + 2 * (8 * ((c >> 2) - g) + 2 * (c & 3) - 6) + i * 1024;
        asm volatile("" : "+v"(q));
        up[i] = q + 448;
      }
      char LAS* yp = ub + 2 * (8 * (g + 4 * (c >> 1)) + 4 * (c & 1));
      u32x2 sk[8];
#pragma unroll
      for (int T = 0; T < 8; ++T) {
        const u32x2 uu = *(const u32x2 LAS*)(yp + T * 512);
        sk[T] = u32x2{pk(fmul(bl(uu[0]), dsk), fmul(bh(uu[0]), dsk)), pk(fmul(bl(uu[1]), dsk), fmul(bh(uu[1]), dsk))};
        asm volatile("" : "+v"(sk[T]));
      }
      f32x4 acc[8];
#pragma unroll
      for (int T = 0; T < 8; ++T) { acc[T] = f32x4{0.f, 0.f, 0.f, 0.f}; asm volatile("" : "+v"(acc[T])); }
      u32x4 kf[2][8], uf[2][8];
      auto rdk = [&](int be, int a) { kf[be & 1][a] = *(const u32x4 LAS*)(kp + be * 64 + a * 512); };
      auto rdu = [&](int be, int a) {
        const u32 LAS* q = (const u32 LAS*)(up[a >> 1] - be * 64 + (a & 1) * 512);
        uf[be & 1][a] = u32x4{q[0], q[1], q[2], q[3]};
      };
#pragma unroll
      for (int a = 0; a < 8; ++a) { rdk(0, a); rdu(0, a); }
      waitv<0>(); waitl<0>();
      __builtin_amdgcn_s_setprio(3);
      SB;
      bar();
      SB;
      LSTAMP(23);
      static_for<0, 288>([&](auto N) {
        constexpr int n = decltype(N)::value, be = n / 36, m = n % 36;
        constexpr int a = m < 8 ? 0 : m < 15 ? 1 : m < 21 ? 2 : m < 26 ? 3 : m < 30 ? 4 : m < 33 ? 5 : m < 35 ? 6 : 7;
        constexpr int T = a + m - (8 * a - a * (a - 1) / 2);
        acc[T] = mfma16(uf[be & 1][T - a], kf[be & 1][a], acc[T]);
        SB;
        if (be < 7 && m < 16 && (m & 1) == 0) rdu(be + 1, m >> 1);
        if (be < 7 && m >= 16 && m < 24) rdk(be + 1, (m - 16) & 7);
        if (n == 251) { waitl<0>(); issue_own(); }
        SB;
      });
#pragma unroll
      for (int T = 0; T < 8; ++T) yv[T] = u32x4{pk(acc[T][0], acc[T][1]), pk(acc[T][2], acc[T][3]), sk[T][0], sk[T][1]};
      __builtin_amdgcn_s_setprio(0);
    }
    LSTAMP(24);
    u64 fa = 0;
    static_for<1, 8>([&](auto K) {
      constexpr int st = 2 * decltype(K)::value;
      constexpr int yng = st == 14 ? 0 : g2_upto(st - 2) - (st + 2);
      if (st == 14 && wid == 7 && !__all(fa == tag)) flag_wait(ws, FL_A + dj * 36, 36, tag, lane);
      if (st > 2) { if (n0 == 0) waitv<5 * yng>(); else waitv<4 * yng>(); }
      LSTAMP(24 + st);
      bar();
      LSTAMP(25 + st);
      if (conv && st == 6) {
        const __amdgpu_buffer_rsrc_t ao = rsrc(ws + WS_A2 + (size_t)Q * 16384 + 4096 * p, 4096);
        const char LAS* tp = sm + LM_UIM + (c >> 2) * UIM_B + 64 + 2 * (512 * p + 16 * g) + (c & 3) * 8;
#pragma unroll
        for (int i = 0; i < 8; ++i) __builtin_amdgcn_raw_buffer_store_b64(tr_read(tp + i * 128), ao, lane * 8, i * 512, SC1);
      }
      if (conv && st == 8 && tid == 256) flag_set(ws, FL_A + Q, tag);
#pragma unroll
      for (int j = g2_upto(st - 2) + 1; j <= g2_upto(st); ++j) {
        if (p < 2) issue2(2 * j); else issue2(2 * j + 1);
      }
      if (st == 12 && wid == 7) fa = __hip_atomic_load((GAS u64*)(ws + (FL_A + dj * 36 + min(lane, 35)) * 128), __ATOMIC_RELAXED, __HIP_MEMORY_SCOPE_AGENT);
      if (conv && st <= 4) {
        const bool odd = c & 1;
        char LAS* yp = uim + 64 + 2 * (8 * (g + 4 * (c >> 1)) + 4 * (c & 1));
#pragma unroll
        for (int T = 2 * st - 4; T < 2 * st; ++T) {
          const u32 own = odd ? yv[T][1] : yv[T][0];
          const u32 rcv = (u32)__builtin_amdgcn_mov_dpp((int)(odd ? yv[T][0] : yv[T][1]), 0xB1, 0xF, 0xF, true);
          const u32 ev = odd ? rcv : own, od = odd ? own : rcv;
          *(u32x2 LAS*)(yp + T * 512) = u32x2{pk(fadd(bl(ev), bl(yv[T][2])), fadd(bl(od), bh(yv[T][2]))),
                                              pk(fadd(bh(ev), bl(yv[T][3])), fadd(bh(od), bh(yv[T][3])))};
        }
        waitl<0>();
      }
          });
    load_base();
  }
  u32x2 base[2][2];
#pragma unroll
  for (int k = 0; k < 2; ++k)
#pragma unroll
    for (int i = 0; i < 2; ++i)
      base[k][i] = u32x2{pk(fadd(bl(xv[k][i][0]), bl(av[k][i][0])), fadd(bh(xv[k][i][0]), bh(av[k][i][0]))),
                         pk(fadd(bl(xv[k][i][1]), bl(av[k][i][1])), fadd(bh(xv[k][i][1]), bh(av[k][i][1])))};
  waitv<0>(); waitl<0>();
  bar();
  LSTAMP(52);
  if (tid == 0) flag_set(ws, FL_X + tile * 4 + s, tag);
  if (tid == 64 && b == 0) flag_set(ws, FL_EPOCH, tag);
  flag_wait(ws, FL_X + tile * 4, 4, tag, lane);
  LSTAMP(53);
  const __amdgpu_buffer_rsrc_t rs = rsrc(ws + WS_SLAB + (size_t)tile * 16 * SLAB_B, 16 * SLAB_B);
  u32x4 part[2][3];
#pragma unroll
  for (int k = 0; k < 2; ++k)
    if (k == 0 || wid == 4) {
#pragma unroll
      for (int e = 0; e < 3; ++e) part[k][e] = ld128(rs, lane * 16, (s * 4 + ((s + 1 + e) & 3)) * SLAB_B + (k ? 8 : wid) * 1024);
    }
#pragma unroll
  for (int k = 0; k < 2; ++k)
    if (k == 0 || wid == 4) {
      const int t = k ? 8 : wid;
#pragma unroll
      for (int i = 0; i < 2; ++i) {
        f32x4 v = *(const f32x4 LAS*)(sm + LM_UIM + (2 * t + i) * 1024 + lane * 16);
#pragma unroll
        for (int e = 0; e < 3; ++e)
          v += f32x4{bl(part[k][e][2 * i]), bh(part[k][e][2 * i]), bl(part[k][e][2 * i + 1]), bh(part[k][e][2 * i + 1])};
        const u32 r0 = pk(v[0], v[1]), r1 = pk(v[2], v[3]);
        const u32 q0 = pk(bl(r0) + bl(bias[k][0]), bh(r0) + bh(bias[k][0])), q1 = pk(bl(r1) + bl(bias[k][1]), bh(r1) + bh(bias[k][1]));
        const int l = 128 * l2 + 32 * s + 16 * i + c, d = 144 * dj + 16 * t + 4 * g;
        *(u32x2*)(out + (size_t)l * D + d) =
            u32x2{pk(bl(base[k][i][0]) + bl(q0), bh(base[k][i][0]) + bh(q0)), pk(bl(base[k][i][1]) + bl(q1), bh(base[k][i][1]) + bh(q1))};
      }
    }
  LSTAMP(54);
  RSTAMP(59);
}

extern "C" void kernel_launch(void* const* d_in, const int* in_sizes, int n_in, void* d_out, int out_size, void* d_ws,
                              size_t ws_size, hipStream_t stream) {
  const u16* const* in = (const u16* const*)d_in;
  (void)hipFuncSetAttribute((const void*)k_all, hipFuncAttributeMaxDynamicSharedMemorySize, LM_END);
  k_all<<<256, 512, LM_END, stream>>>(in[0], in[1], in[2], in[3], in[4], in[5], in[6], in[7], in[8], in[10], in[11], in[12], in[13],
                                      (char*)d_ws, (u16*)d_out);
}
```

```cpp
#define PAD_AT 3
#define PAD_US 20
#include <hip/hip_runtime.h>
#include <hip/hip_bf16.h>
#include <stdint.h>
#include <type_traits>

typedef unsigned short u16;
typedef unsigned int u32;
typedef unsigned long long u64;
typedef __bf16 bf16x8 __attribute__((ext_vector_type(8)));
typedef __bf16 bf16x2 __attribute__((ext_vector_type(2)));
typedef short s16x4 __attribute__((ext_vector_type(4)));
typedef float f32x4 __attribute__((ext_vector_type(4)));
typedef float f32x2 __attribute__((ext_vector_type(2)));
typedef u32 u32x4 __attribute__((ext_vector_type(4)));
typedef u32 u32x2 __attribute__((ext_vector_type(2)));
#define LAS __attribute__((address_space(3)))
#define GAS __attribute__((address_space(1)))
#define POLL_SLEEP 4
#define SB __builtin_amdgcn_sched_barrier(0)

constexpr int L = 2048, D = 576, NM = 128, F = 4608;

constexpr int FL_EPOCH = 0;
constexpr int FL_LN = 1;
constexpr int FL_A = FL_LN + 256;
constexpr int FL_H = FL_A + 144;
constexpr int FL_X = FL_H + 256;
constexpr size_t WS_XNF = 131072;
constexpr size_t WS_UT = WS_XNF + (size_t)L * D * 2;
constexpr size_t WS_A2 = WS_UT + (size_t)L * D * 2;
constexpr size_t WS_HF = WS_A2 + (size_t)L * D * 2;
constexpr size_t WS_SLAB = WS_HF + (size_t)L * F * 2;
constexpr int SLAB_B = 9216, HB = 73728;
constexpr size_t WS_END = WS_SLAB + (size_t)64 * 16 * SLAB_B;
#define LSTAMP(i) do { if ((i) == PAD_AT && wid < 4) { while (__builtin_amdgcn_s_memrealtime() - pad0 < PAD_US * 100ull) __builtin_amdgcn_s_sleep(8); } } while (0)
#define RSTAMP(i)

__device__ __forceinline__ float bl(u32 v) { return __uint_as_float(v << 16); }
__device__ __forceinline__ float bh(u32 v) { return __uint_as_float(v & 0xffff0000u); }
__device__ __forceinline__ float b2f(u16 v) { return __uint_as_float(((u32)v) << 16); }
__device__ __forceinline__ u32 pk(float lo, float hi) {
  bf16x2 r = {(__bf16)lo, (__bf16)hi};
  return __builtin_bit_cast(u32, r);
}
__device__ __forceinline__ float fadd(float a, float b) {
  float r;
  asm("v_add_f32 %0, %1, %2" : "=v"(r) : "v"(a), "v"(b));
  return r;
}
__device__ __forceinline__ float rb(float f) { return (float)(__bf16)f; }
__device__ __forceinline__ f32x4 mfma16(u32x4 a, u32x4 b, f32x4 c) {
  return __builtin_amdgcn_mfma_f32_16x16x32_bf16(__builtin_bit_cast(bf16x8, a), __builtin_bit_cast(bf16x8, b), c, 0, 0, 0);
}
__device__ __forceinline__ u32x2 tr_read(const char LAS* p) {
  return __builtin_bit_cast(u32x2, __builtin_amdgcn_ds_read_tr16_b64_v4i16((s16x4 LAS*)p));
}
__device__ __forceinline__ __amdgpu_buffer_rsrc_t rsrc(const void* p, int bytes) {
  return __builtin_amdgcn_make_buffer_rsrc((void*)p, 0, bytes, 0x00020000);
}
constexpr int SC1 = 16;
__device__ __forceinline__ void st128(u32x4 v, __amdgpu_buffer_rsrc_t r, int voff, int soff) {
  __builtin_amdgcn_raw_buffer_store_b128(v, r, voff, soff, SC1);
  asm volatile("s_nop 1" ::"v"(v) : "memory");
}
__device__ __forceinline__ u32x4 ld128(__amdgpu_buffer_rsrc_t r, int voff, int soff) {
  return __builtin_amdgcn_raw_buffer_load_b128(r, voff, soff, SC1);
}
__device__ __forceinline__ void glds16(const char* base, u32 voff, u32 dst) {
  asm volatile("s_mov_b32 m0, %2\n\ts_nop 0\n\tglobal_load_lds_dwordx4 %0, %1" ::"v"(voff), "s"(base), "s"(dst) : "memory");
}
template <int N> __device__ __forceinline__ void waitv() { asm volatile("s_waitcnt vmcnt(%0)" ::"i"(N) : "memory"); }
template <int N> __device__ __forceinline__ void waitl() { asm volatile("s_waitcnt lgkmcnt(%0)" ::"i"(N) : "memory"); }
__device__ __forceinline__ void bar() {
  asm volatile("" ::: "memory");
  __builtin_amdgcn_s_barrier();
  asm volatile("" ::: "memory");
}
template <int N> __device__ __forceinline__ void bars() {
#pragma unroll
  for (int i = 0; i < N; ++i) bar();
}
template <int I, int N, class Fn> __device__ __forceinline__ void static_for(Fn&& fn) {
  if constexpr (I < N) {
    fn(std::integral_constant<int, I>{});
    static_for<I + 1, N>(fn);
  }
}
__device__ __forceinline__ void flag_set(char* ws, int off, u64 tag) {
  __hip_atomic_store((GAS u64*)(ws + off * 128), tag, __ATOMIC_RELAXED, __HIP_MEMORY_SCOPE_AGENT);
}
__device__ __forceinline__ void flag_wait(char* ws, int off, int n, u64 tag, int lane) {
  GAS u64* p = (GAS u64*)(ws + (off + min(lane, n - 1)) * 128);
  for (u32 spin = 0; spin < (1u << 20); ++spin) {
    if (__all(__hip_atomic_load(p, __ATOMIC_RELAXED, __HIP_MEMORY_SCOPE_AGENT) == tag)) break;
    __builtin_amdgcn_s_sleep(POLL_SLEEP);
  }
  asm volatile("" ::: "memory");
}
__device__ __forceinline__ u32 lds_add(char LAS* p) {
  return __hip_atomic_fetch_add((u32 LAS*)p, 1u, __ATOMIC_RELAXED, __HIP_MEMORY_SCOPE_WORKGROUP);
}
__device__ __forceinline__ void lds_wait4(char LAS* p) {
  while (__builtin_amdgcn_readfirstlane(*(volatile u32 LAS*)p) < 4u) __builtin_amdgcn_s_sleep(1);
  asm volatile("" ::: "memory");
}

__device__ __forceinline__ float fmul(float a, float b) {
  float r;
  asm("v_mul_f32 %0, %1, %2" : "=v"(r) : "v"(a), "v"(b));
  return r;
}
__device__ __forceinline__ f32x2 cmul(f32x2 a, f32x2 b) {
  float re, im;
  asm("v_mul_f32 %0, %2, %4\n\tv_mul_f32 %1, %2, %5\n\tv_fma_f32 %0, -%3, %5, %0\n\tv_fma_f32 %1, %3, %4, %1"
      : "=&v"(re), "=&v"(im) : "v"(a[0]), "v"(a[1]), "v"(b[0]), "v"(b[1]));
  return f32x2{re, im};
}
template <int E> __device__ __forceinline__ f32x2 zpow2(float p2, float qh, float ql) {
  constexpr float s = (float)(1 << E);
  const float mag = __builtin_amdgcn_exp2f(p2 * s);
  const float ph = __builtin_fmaf(ql, s, __builtin_amdgcn_fractf(qh * s));
  float re, im;
  asm("s_nop 1\n\tv_mul_f32 %0, %2, %4\n\tv_mul_f32 %1, %3, %4"
      : "=&v"(re), "=&v"(im) : "v"(__builtin_amdgcn_cosf(ph)), "v"(__builtin_amdgcn_sinf(ph)), "v"(mag));
  return f32x2{re, im};
}
template <int CTRL> __device__ __forceinline__ float dpp_add(float v) {
  return v + __builtin_bit_cast(float, __builtin_amdgcn_update_dpp(0, __builtin_bit_cast(int, v), CTRL, 0xF, 0xF, true));
}
__device__ __forceinline__ float sum32(float v) {
  v = dpp_add<0x128>(v);
  v = dpp_add<0x124>(v);
  v = dpp_add<0x122>(v);
  v = dpp_add<0x121>(v);
  return v + __builtin_bit_cast(float, __builtin_amdgcn_ds_swizzle(__builtin_bit_cast(int, v), 0x401F));
}
__device__ __forceinline__ u32 gelu2(float a, float b) {
  constexpr float C0 = -2.f * 1.44269504088896340736f * 0.7978845608028654f, C1 = C0 * 0.044715f;
  const f32x2 v = {a, b};
  const f32x2 z = v * __builtin_elementwise_fma(v * v, f32x2{C1, C1}, f32x2{C0, C0});
  const f32x2 d = f32x2{__builtin_amdgcn_exp2f(z[0]), __builtin_amdgcn_exp2f(z[1])} + f32x2{1.f, 1.f};
  const f32x2 r = v * f32x2{__builtin_amdgcn_rcpf(d[0]), __builtin_amdgcn_rcpf(d[1])};
  return pk(r[0], r[1]);
}
__device__ __forceinline__ u32x4 gelu8(f32x4 a, f32x4 b) {
  return u32x4{gelu2(a[0], a[1]), gelu2(a[2], a[3]), gelu2(b[0], b[1]), gelu2(b[2], b[3])};
}

constexpr int SLOT = 9216;
constexpr int KIM_C1 = 4640, UIM_B = 5952, KG_S = 272, LN_S = 1168;
constexpr int LM_KIM = 10 * SLOT;
constexpr int LM_UIM = 14 * SLOT;
constexpr int LM_LN = LM_UIM + 4 * UIM_B;
constexpr int LM_CNT = LM_LN + 8 * LN_S;
constexpr int LM_END = LM_CNT + 32;
static_assert(LM_END <= 163840 && 4 * UIM_B >= 18432 && 21 * KG_S <= UIM_B && 32 * KG_S <= SLOT && UIM_B % 256 == 64, "LDS map");
constexpr int slot2(int j) { return j % 14 < 10 ? (j % 14 + 8) % 10 : j % 14; }

#define G2_XP 12
#define G2_X0 8
#define CODE_LINES 590
constexpr int G1_N0 = 4, G1_N1 = 8, G1_NB = 7;
constexpr int G1_POS[G1_NB + 1] = {4, 6, 8, 10, 12, 14, 16, 18};
constexpr int G1_UPTO[G1_NB] = {11, 14, 16, 18, 20, 22, 24};
constexpr bool g1_bar_before(int ks) {
  for (int i = 0; i < G1_NB; ++i)
    if (G1_POS[i] == ks) return true;
  return false;
}
constexpr int g2_upto(int st) { return st < 2 ? 6 : st + 6 < 17 ? st + 6 : 17; }

__global__ __launch_bounds__(512, 2) void k_all(const u16* __restrict__ x, const u16* __restrict__ Are, const u16* __restrict__ Aim,
                                                const u16* __restrict__ Bre, const u16* __restrict__ Bim, const u16* __restrict__ Cre,
                                                const u16* __restrict__ Cim, const u16* __restrict__ logdt, const u16* __restrict__ Dskip,
                                                const u16* __restrict__ w1, const u16* __restrict__ b1, const u16* __restrict__ w2,
                                                const u16* __restrict__ b2, char* ws, u16* __restrict__ out) {
  extern __shared__ __attribute__((aligned(16))) char smem[];
  char LAS* sm = (char LAS*)smem;
  const u32 smb = (u32)(uintptr_t)sm;
  const int tid = threadIdx.x, lane = tid & 63, wid = __builtin_amdgcn_readfirstlane(tid >> 6);
  const int b = blockIdx.x, xg = b & 7, idx = b >> 3, s = xg & 3;
  const int lb = 4 * (xg >> 2) + (idx >> 3), fb = 8 * s + (idx & 7);
  const int l2 = 8 * (xg >> 2) + (idx >> 2), dj = idx & 3, tile = l2 * 4 + dj;
  const int cl = xg * 4 + (idx >> 3);
  const int c = lane & 15, g = lane >> 4;
  const char* pc0;
  asm volatile("s_getpc_b64 %0" : "=s"(pc0));
  u64 tag;
  const u64 pad0 = __builtin_amdgcn_s_memrealtime();
  LSTAMP(63);
  u32x2 xv[2][2] = {}, av[2][2] = {}, bias[2] = {};
  auto load_base = [&]() {
    const __amdgpu_buffer_rsrc_t ra = rsrc(ws + WS_A2, L * D * 2);
#pragma unroll
    for (int k = 0; k < 2; ++k) {
      const int t = k ? 8 : wid;
      if (k == 0 || wid == 4) {
        const int d = 144 * dj + 16 * t + 4 * g;
        bias[k] = *(const u32x2*)(b2 + d);
#pragma unroll
        for (int i = 0; i < 2; ++i) {
          const int l = 128 * l2 + 32 * s + 16 * i + c;
          xv[k][i] = *(const u32x2*)(x + (size_t)l * D + d);
          av[k][i] = __builtin_amdgcn_raw_buffer_load_b64(ra, ((d >> 2) * L + l) * 8, 0, SC1);
        }
      }
    }
  };
  if (wid < 4) {
    __builtin_amdgcn_s_setprio(2);
    u32x2 bq[9];
    {
      const int r = tid >> 5, lam = tid & 31;
      const u32* src = (const u32*)(x + (size_t)(b * 8 + r) * D) + lam;
      u32 raw[9];
#pragma unroll
      for (int i = 0; i < 9; ++i) raw[i] = src[32 * i];
      const u64 ep = __hip_atomic_load((GAS u64*)(ws + FL_EPOCH), __ATOMIC_RELAXED, __HIP_MEMORY_SCOPE_AGENT);
#pragma unroll
      for (int t = 0; t < 9; ++t) bq[t] = *(const u32x2*)(b1 + fb * 144 + 16 * t + 4 * g);
      if (tid == 0) *(u32x4 LAS*)(sm + LM_CNT) = u32x4{0u, 0u, 0u, 0u};
      waitl<0>();
      bar();
      f32x2 v[9];
#pragma unroll
      for (int i = 0; i < 9; ++i) v[i] = f32x2{bl(raw[i]), bh(raw[i])};
      f32x2 s2 = v[0];
#pragma unroll
      for (int i = 1; i < 9; ++i) s2 += v[i];
      const float mean = sum32(s2[0] + s2[1]) * (1.f / D);
      asm volatile("" ::"v"(mean));
      LSTAMP(0);
      f32x2 q2 = {0.f, 0.f};
#pragma unroll
      for (int i = 0; i < 9; ++i) { f32x2 d = v[i] - f32x2{mean, mean}; q2 = __builtin_elementwise_fma(d, d, q2); }
      const float q = sum32(q2[0] + q2[1]);
      const float m = rb(mean);
      const float sd = rb(__builtin_sqrtf(rb(rb(q * (1.f / D)) + 1e-5f)));
      const float rs = __builtin_amdgcn_rcpf(sd);
#pragma unroll
      for (int i = 0; i < 9; ++i) {
        const f32x2 d = v[i] - f32x2{m, m};
        const u32 t = pk(d[0], d[1]);
        const f32x2 n = f32x2{bl(t), bh(t)} * f32x2{rs, rs};
        *(u32 LAS*)(sm + LM_LN + r * LN_S + (lam + 32 * i) * 4) = pk(n[0], n[1]);
      }
      const u64 t64 = ep * 0x9E3779B97F4A7C15ull + 0xD1B54A32D192ED03ull;
      tag = ((u64)__builtin_amdgcn_readfirstlane((u32)(t64 >> 32)) << 32) | __builtin_amdgcn_readfirstlane((u32)t64);
      if (tid == 0) *(u64 LAS*)(sm + LM_CNT + 16) = tag;
      waitl<0>();
      if (lane == 0) lds_add(sm + LM_CNT);
      lds_wait4(sm + LM_CNT);
      LSTAMP(1);
      const __amdgpu_buffer_rsrc_t xo = rsrc(ws + WS_XNF + (size_t)(b >> 1) * 18432 + (b & 1) * 128, 18432);
      const __amdgpu_buffer_rsrc_t uo = rsrc(ws + WS_UT + (size_t)b * 9216, 9216);
      const char LAS* xp = sm + LM_LN + (lane & 7) * LN_S + (lane >> 5) * 64 + ((lane >> 3) & 3) * 8;
      const char LAS* tp = sm + LM_LN + ((lane >> 2) & 3) * LN_S + (lane >> 4) * 32 + (lane & 3) * 8;
      const int xoff = (lane >> 5) * 1024 + ((lane >> 3) & 3) * 256 + (lane & 7) * 16;
#pragma unroll
      for (int kk = 0; kk < 3; ++kk) {
        const int k = wid + 4 * kk;
        if (k < 9) {
          const u32x2 a = *(const u32x2 LAS*)(xp + k * 128), bb = *(const u32x2 LAS*)(xp + k * 128 + 32);
          const u32x2 t0 = tr_read(tp + k * 128), t1 = tr_read(tp + k * 128 + 4 * LN_S);
          st128(u32x4{a[0], a[1], bb[0], bb[1]}, xo, xoff, k * 2048);
          st128(u32x4{t0[0], t0[1], t1[0], t1[1]}, uo, lane * 16, k * 1024);
        }
      }
    }
    {
      constexpr int XP = 3, WD = 3, WR = 4;
      const int lt0 = 16 * lb + 4 * wid;
      const __amdgpu_buffer_rsrc_t rx = rsrc(ws + WS_XNF + (size_t)lt0 * 18432, 4 * 18432);
      f32x4 acc[9][4];
#pragma unroll
      for (int t = 0; t < 9; ++t) {
#pragma unroll
        for (int i = 0; i < 4; ++i) {
          acc[t][i] = f32x4{bl(bq[t][0]), bh(bq[t][0]), bl(bq[t][1]), bh(bq[t][1])};
          asm volatile("" : "+v"(acc[t][i]));
        }
      }
      waitv<0>();
      LSTAMP(2);
      if (lane == 0 && lds_add(sm + LM_CNT + 4) == 3u) flag_set(ws, FL_LN + b, tag);
      flag_wait(ws, FL_LN + 32 * lb + 8 * wid, 8, tag, lane);
      LSTAMP(3);
      u32x4 xf[XP + 1][4];
#pragma unroll
      for (int k = 0; k < XP; ++k)
#pragma unroll
        for (int i = 0; i < 4; ++i) xf[k][i] = ld128(rx, lane * 16, i * 18432 + k * 1024);
      SB;
      const char LAS* wp = sm + (lane >> 2) * 288 + (lane & 3) * 8;
      u32x2 wa[WR], wb[WR];
      const char LAS* wph = wp + 6 * SLOT;
      asm volatile("" : "+v"(wph));
      auto wptr = [&](int n) {
        const int sl = (n / 9) % 10;
        return (sl < 6 ? wp + sl * SLOT : wph + (sl - 6) * SLOT) + (n % 9) * 32;
      };
      lds_wait4(sm + LM_CNT + 12);
      LSTAMP(4);
      RSTAMP(55);
#pragma unroll
      for (int n = 0; n < WD; ++n) { wa[n] = tr_read(wptr(n)); wb[n] = tr_read(wptr(n) + 16 * 288); }
#pragma unroll
      for (int ks = 0; ks < 18; ++ks) {
#pragma unroll
        for (int t = 0; t < 9; ++t) {
          const int n = 9 * ks + t;
          const u32x4 w = {wa[n % WR][0], wa[n % WR][1], wb[n % WR][0], wb[n % WR][1]};
          acc[t][0] = mfma16(w, xf[ks % (XP + 1)][0], acc[t][0]);
          SB;
          if (t == 9 - WD && g1_bar_before(ks + 1)) bar();
          if (n == 161) bar();
          if (n + WD < 162) wa[(n + WD) % WR] = tr_read(wptr(n + WD));
          SB;
          acc[t][1] = mfma16(w, xf[ks % (XP + 1)][1], acc[t][1]);
          SB;
          if (n + WD < 162) wb[(n + WD) % WR] = tr_read(wptr(n + WD) + 16 * 288);
          SB;
          acc[t][2] = mfma16(w, xf[ks % (XP + 1)][2], acc[t][2]);
          SB;
          if (t < 4 && ks + XP < 18) xf[(ks + XP) % (XP + 1)][t] = ld128(rx, lane * 16, t * 18432 + (ks + XP) * 1024);
          SB;
          acc[t][3] = mfma16(w, xf[ks % (XP + 1)][3], acc[t][3]);
          SB;
        }
      }
      asm volatile("s_nop 15");
      SB;
      LSTAMP(5);
      RSTAMP(56);
      const __amdgpu_buffer_rsrc_t rh = rsrc(ws + WS_HF + (size_t)(cl * 8 + (idx & 7)) * HB, HB);
#pragma unroll
      for (int i = 0; i < 4; ++i)
#pragma unroll
        for (int r = 0; r < 4; ++r) st128(gelu8(acc[2 * r][i], acc[2 * r + 1][i]), rh, lane * 16, (4 * wid + i) * 4096 + r * 1024);
#pragma unroll
      for (int i = 0; i < 2; ++i) st128(gelu8(acc[8][2 * i], acc[8][2 * i + 1]), rh, lane * 16, 65536 + (2 * wid + i) * 1024);
      LSTAMP(6);
    }
    bar();
    {
      constexpr int XP = G2_XP, X0 = G2_X0, WD = 5, WR = 6, TAIL = 4;
      const __amdgpu_buffer_rsrc_t rh = rsrc(ws + WS_HF + (size_t)cl * 8 * HB, 8 * HB);
      const int pr = 4 * ((idx >> 2) & 1) + wid;
      const __amdgpu_buffer_rsrc_t rs = rsrc(ws + WS_SLAB + (size_t)tile * 16 * SLAB_B, 16 * SLAB_B);
      f32x4 acc[9][2];
#pragma unroll
      for (int t = 0; t < 9; ++t)
#pragma unroll
        for (int i = 0; i < 2; ++i) {
          acc[t][i] = f32x4{0.f, 0.f, 0.f, 0.f};
          asm volatile("" : "+v"(acc[t][i]));
        }
      const char LAS* wp = sm + (lane >> 2) * 288 + (lane & 3) * 8;
      u32x2 wa[WR], wb[WR];
      constexpr auto ord_ks = [](int n) { return n < 9 * (36 - TAIL) ? n / 9 : 36 - TAIL + (n - 9 * (36 - TAIL)) % TAIL; };
      constexpr auto ord_t = [](int n) { return n < 9 * (36 - TAIL) ? n % 9 : (n - 9 * (36 - TAIL)) / TAIL; };
      const char LAS* wph = wp + 7 * SLOT;
      asm volatile("" : "+v"(wph));
      auto wptr = [&](int n) {
        const int sl = slot2(ord_ks(n));
        return (sl < 7 ? wp + sl * SLOT : wph + (sl - 7) * SLOT) + ord_t(n) * 32;
      };
#pragma unroll
      for (int n = 0; n < WD; ++n) { wa[n] = tr_read(wptr(n)); wb[n] = tr_read(wptr(n) + 16 * 288); }
      u32x4 xf[XP + 1][2];
      auto hload = [&](int k, int i) {
        const int r = k % 9, P = ((idx >> 1) + k / 9) & 3;
        const int so = r < 8 ? (2 * P + (((r >> 2) ^ idx) & 1)) * HB + pr * 8192 + i * 4096 : (2 * P + i) * HB + 65536 + pr * 1024;
        xf[k % (XP + 1)][i] = ld128(rh, lane * 16 + (r < 8 ? (r & 3) * 1024 : 0), so);
      };
      waitv<0>();
      LSTAMP(7);
      const int flh = FL_H + cl * 8;
      if (lane == 0 && lds_add(sm + LM_CNT + 8) == 3u) flag_set(ws, flh + (idx & 7), tag);
      lds_wait4(sm + LM_CNT + 8);
#pragma unroll
      for (int k = 0; k < 4; ++k) { hload(k, 0); hload(k, 1); }
      flag_wait(ws, flh, 8, tag, lane);
      LSTAMP(8);
      RSTAMP(57);
#pragma unroll
      for (int k = 4; k < X0; ++k) { hload(k, 0); hload(k, 1); }
      SB;
      auto flush = [&](auto OWN, int tt) {
        if (decltype(OWN)::value) {
          *(f32x4 LAS*)(sm + LM_UIM + (2 * tt) * 1024 + lane * 16) = acc[tt][0];
          *(f32x4 LAS*)(sm + LM_UIM + (2 * tt + 1) * 1024 + lane * 16) = acc[tt][1];
        } else {
          const u32x4 v = {pk(acc[tt][0][0], acc[tt][0][1]), pk(acc[tt][0][2], acc[tt][0][3]), pk(acc[tt][1][0], acc[tt][1][1]), pk(acc[tt][1][2], acc[tt][1][3])};
          st128(v, rs, lane * 16, (wid * 4 + s) * SLAB_B + tt * 1024);
        }
      };
      auto step = [&](auto N, auto OWN) {
        constexpr int n = decltype(N)::value;
        constexpr int ks = ord_ks(n), t = ord_t(n);
        if (ks % 9 == 8 && t == 0) {
          const u32x4 a = xf[ks % (XP + 1)][0], bb = xf[ks % (XP + 1)][1];
          xf[ks % (XP + 1)][0] = u32x4{a[0], a[1], bb[0], bb[1]};
          xf[ks % (XP + 1)][1] = u32x4{a[2], a[3], bb[2], bb[3]};
        }
        const u32x4 w = {wa[n % WR][0], wa[n % WR][1], wb[n % WR][0], wb[n % WR][1]};
        acc[t][0] = mfma16(w, xf[ks % (XP + 1)][0], acc[t][0]);
        SB;
        if ((n + WD) % 36 == 18 && n + WD >= 54 && n + WD <= 270) bar();
        if (n + WD < 324) wa[(n + WD) % WR] = tr_read(wptr(n + WD));
        SB;
        acc[t][1] = mfma16(w, xf[ks % (XP + 1)][1], acc[t][1]);
        SB;
        if (n + WD < 324) wb[(n + WD) % WR] = tr_read(wptr(n + WD) + 16 * 288);
        if (ks < XP - X0 && (t & 1) && t < 8) hload(X0 + 2 * ks + (t >> 2), (t >> 1) & 1);
        if (ks >= XP - X0 && (t == 2 || t == 6) && ks + XP < 36) hload(ks + XP, (t >> 2) & 1);
        if (ks == 30 && t == 2) load_base();
        if (ks == 37 - TAIL && t > 0 && n >= 9 * (36 - TAIL)) flush(OWN, t > 0 ? t - 1 : 0);
        SB;
      };
      static_for<0, 9 * (36 - TAIL) + TAIL>([&](auto N) { step(N, std::false_type{}); });
      if (wid == s) {
        static_for<9 * (36 - TAIL) + TAIL, 324>([&](auto N) { step(N, std::true_type{}); });
        flush(std::true_type{}, 8);
      } else {
        static_for<9 * (36 - TAIL) + TAIL, 324>([&](auto N) { step(N, std::false_type{}); });
        flush(std::false_type{}, 8);
      }
      LSTAMP(9);
      RSTAMP(58);
    }
  } else {
    const bool conv = idx < 18;
    const int Q = xg * 18 + idx, p = wid - 4, n0 = (p & 1) * 5;
    u32 par[2][6], vdt, vds;
    {
      const int ch = conv ? 4 * Q + p : 0;
      const u32 o0 = (ch * NM + lane) * 2, o1 = ch * 2;
      const u16* src[6] = {Are, Aim, Bre, Bim, Cre, Cim};
#pragma unroll
      for (int i = 0; i < 6; ++i)
        asm volatile("global_load_ushort %0, %2, %3\n\tglobal_load_ushort %1, %2, %3 offset:128" : "=&v"(par[0][i]), "=&v"(par[1][i]) : "v"(o0), "s"(src[i]));
      asm volatile("global_load_ushort %0, %2, %3\n\tglobal_load_ushort %1, %2, %4" : "=&v"(vdt), "=&v"(vds) : "v"(o1), "s"(logdt), "s"(Dskip));
    }
    bar();
    u32 cr[9], cc[9];
#pragma unroll
    for (int n = 0; n < 9; ++n) { const int ci = 64 * n + lane; cr[n] = ci / 18; cc[n] = (ci % 18) * 16; }
    u32 v1[5], v2[9];
#pragma unroll
    for (int n = 0; n < 5; ++n) v1[n] = n0 ? cr[n < 4 ? 5 + n : 8] * (F * 2) + cc[n < 4 ? 5 + n : 8] : cr[n] * (F * 2) + cc[n];
    const char* w1s = (const char*)(w1 + fb * 144);
    auto issue1 = [&](int k) {
      const char* sp = w1s + (size_t)k * 32 * F * 2;
      const u32 dst = smb + (k % 10) * SLOT + n0 * 1024;
#pragma unroll
      for (int n = 0; n < 4; ++n) glds16(sp, v1[n], dst + n * 1024);
      if (n0 == 0) glds16(sp, v1[4], dst + 4096);
    };
#pragma unroll
    for (int k = 0; k < G1_N0 / 2; ++k) issue1(2 * k + (p >> 1));
    u32 cpf;
    {
      const u32 co = min(idx + 32 * (16 * p + (lane & 15)), CODE_LINES - 1) * 128;
      asm volatile("global_load_dword %0, %1, %2" : "=v"(cpf) : "v"(co), "s"(pc0));
    }
    LSTAMP(0);
#pragma unroll
    for (int n = 0; n < 9; ++n) v2[n] = cr[n] * (D * 2) + cc[n];
    const u32 v24s = v2[4] + (lane >= 32 ? 128 * D * 2 : 0);
    const char* w2s = (const char*)(w2 + (size_t)1152 * s * D + 144 * dj);
    auto ksrc = [&](int k) {
      const int r = k % 9;
      return w2s + (size_t)(288 * (((idx >> 1) + k / 9) & 3) + (r < 8 ? 144 * (((r >> 2) ^ idx) & 1) + 32 * (r & 3) : 128)) * (D * 2);
    };
    auto issue2 = [&](int k) {
      const char* sp = ksrc(k);
      const u32 dst = smb + slot2(k) * SLOT;
      if (n0 == 0) {
#pragma unroll
        for (int n = 0; n < 4; ++n) glds16(sp, v2[n], dst + n * 1024);
        glds16(sp, k % 9 == 8 ? v24s : v2[4], dst + 4096);
      } else {
#pragma unroll
        for (int n = 5; n < 9; ++n) glds16(sp + (k % 9 == 8 ? 128 * D * 2 : 0), v2[n], dst + n * 1024);
      }
    };
    auto issue_own = [&]() {
      const char* sp = ksrc(10 + p);
#pragma unroll
      for (int n = 0; n < 9; ++n) glds16(sp, v2[n], smb + (10 + p) * SLOT + n * 1024);
    };
    auto batch2 = [&]() {
      waitv<0>();
      asm volatile("" ::"v"(cpf));
      if (lane == 0) lds_add(sm + LM_CNT + 12);
#pragma unroll
      for (int k = G1_N0 / 2; k < G1_N1 / 2; ++k) issue1(2 * k + (p >> 1));
    };
    char LAS* kim = sm + LM_KIM + p * SLOT;
    char LAS* uim = sm + LM_UIM + p * UIM_B;
    if (conv) {
      LSTAMP(3);
      waitv<4 * (G1_N0 / 2) + 1>();
#pragma unroll
      for (int i = 0; i < 6; ++i) asm volatile("" : "+v"(par[0][i]), "+v"(par[1][i]));
      asm volatile("" : "+v"(vdt), "+v"(vds));
      const float dt = rb(__expf(bl(vdt)));
      f32x4 ka[2][2];
#pragma unroll
      for (int i = 0; i < 4; ++i) ka[i >> 1][i & 1] = f32x4{0.f, 0.f, 0.f, 0.f};
#pragma unroll 1
      for (int h = 0; h < 2; ++h) {
        float pv[6];
#pragma unroll
        for (int i = 0; i < 6; ++i) pv[i] = bl(h ? par[1][i] : par[0][i]);
        const float p2 = dt * pv[0] * 1.44269504088896340736f;
        const float q = dt * pv[1];
        constexpr float CH = 0.15915494309189535f;
        constexpr float CL = (float)(0.15915494309189533577 - (double)CH);
        const float qh = q * CH;
        const float ql = __builtin_fmaf(q, CL, __builtin_fmaf(q, CH, -qh));
        asm volatile("" ::"v"(ql));
        LSTAMP(50 + h);
        {
          f32x2 Z[16];
          auto put = [&](int i, f32x2 v) { *(u32 LAS*)(kim + i * KG_S + lane * 4) = pk(v[0], v[1]); };
          Z[0] = f32x2{1.f, 0.f};
          Z[1] = zpow2<3>(p2, qh, ql);
          Z[4] = zpow2<5>(p2, qh, ql);
          const f32x2 a7 = zpow2<7>(p2, qh, ql);
          Z[2] = cmul(Z[1], Z[1]);
          Z[8] = cmul(Z[4], Z[4]);
          Z[3] = cmul(Z[2], Z[1]);
#pragma unroll
          for (int i = 1; i < 4; ++i) Z[4 + i] = cmul(Z[4], Z[i]);
#pragma unroll
          for (int i = 1; i < 8; ++i) Z[8 + i] = cmul(Z[8], Z[i]);
#pragma unroll
          for (int i = 0; i < 16; ++i) put(i, Z[i]);
          put(16, a7);
#pragma unroll
          for (int i = 1; i < 16; ++i) put(16 + i, cmul(a7, Z[i]));
        }
        {
          f32x2 H[16];
          auto put = [&](int i, f32x2 v) { *(u32 LAS*)(uim + i * KG_S + lane * 4) = pk(v[0], -v[1]); };
          H[0] = cmul(f32x2{pv[4], pv[5]}, f32x2{fmul(dt, pv[2]), fmul(dt, pv[3])});
          const f32x2 a0 = zpow2<0>(p2, qh, ql), a8 = zpow2<8>(p2, qh, ql), a10 = zpow2<10>(p2, qh, ql);
          const f32x2 a1 = cmul(a0, a0), a2 = cmul(a1, a1), a9 = cmul(a8, a8), a11 = cmul(a10, a10);
          H[1] = cmul(a0, H[0]);
          H[2] = cmul(a1, H[0]);
          H[3] = cmul(a1, H[1]);
#pragma unroll
          for (int i = 0; i < 4; ++i) H[4 + i] = cmul(a2, H[i]);
#pragma unroll
          for (int i = 0; i < 4; ++i) H[8 + i] = cmul(a8, H[2 * i]);
          H[12] = cmul(a9, H[0]);
          H[13] = cmul(a9, H[4]);
          H[14] = cmul(a9, H[8]);
          H[15] = cmul(a9, H[10]);
#pragma unroll
          for (int i = 0; i < 16; ++i) put(i, H[i]);
          put(16, cmul(a10, H[0]));
          put(17, cmul(a10, H[8]));
          put(18, cmul(a10, H[12]));
          put(19, cmul(a10, H[14]));
          put(20, cmul(a11, H[0]));
        }
        lds_wait4(sm + LM_CNT);
#pragma unroll
        for (int k = 0; k < 4; ++k) {
          u32x4 lo[2], hi[2];
#pragma unroll
          for (int i = 0; i < 2; ++i) {
            lo[i] = *(const u32x4 LAS*)(kim + (16 * i + c) * KG_S + k * 64 + g * 16);
            hi[i] = *(const u32x4 LAS*)(uim + min(16 * i + c, 20) * KG_S + k * 64 + g * 16);
          }
#pragma unroll
          for (int i = 0; i < 4; ++i) ka[i >> 1][i & 1] = mfma16(lo[i & 1], hi[i >> 1], ka[i >> 1][i & 1]);
        }
        if (h == 0) batch2();
      }
      LSTAMP(1);
#pragma unroll
      for (int i = 0; i < 4; ++i)
        *(u32x2 LAS*)(uim + (16 * (i >> 1) + c) * 64 + (i & 1) * 32 + g * 8) =
            u32x2{pk(ka[i >> 1][i & 1][0], ka[i >> 1][i & 1][1]), pk(ka[i >> 1][i & 1][2], ka[i >> 1][i & 1][3])};
      if (lane < 56) *(u32x4 LAS*)(kim + (lane & 1) * KIM_C1 + (lane >> 1) * 16) = u32x4{0u, 0u, 0u, 0u};
      char LAS* ko = kim + (28 + lane) * 16;
      auto put = [&](int mi, const u32(&kv)[9]) {
        *(u32x4 LAS*)(ko + mi * 1024) = u32x4{kv[1] | (kv[2] << 16), kv[3] | (kv[4] << 16), kv[5] | (kv[6] << 16), kv[7] | (kv[8] << 16)};
        *(u32x4 LAS*)(ko + mi * 1024 + KIM_C1) = u32x4{kv[0] | (kv[1] << 16), kv[2] | (kv[3] << 16), kv[4] | (kv[5] << 16), kv[6] | (kv[7] << 16)};
      };
      {
        u32 kv[9];
#pragma unroll
        for (int e = 0; e < 9; ++e) {
          constexpr int EX[9] = {448, 384, 320, 256, 192, 128, 64, 0, 446}, R2[9] = {450, 640, 576, 576, 576, 512, 448, 448, 448};
          int o = 2 * lane + (lane < (e == 8 ? 33 : 32) ? EX[e] : R2[e]);
          if (e == 0 && lane == 63) o = 768;
          kv[e] = *(const u16 LAS*)(uim + o);
        }
        if (lane == 0) kv[8] = 0u;
        put(0, kv);
      }
      {
        const int oa = 768 + 128 * (lane >> 5) + 2 * (lane & 31), oe = 768 + 128 * ((lane + 1) >> 5) + 2 * ((lane + 1) & 31);
        const u32 a = *(const u16 LAS*)(uim + oa), bb = *(const u16 LAS*)(uim + oa + 64), e = *(const u16 LAS*)(uim + oe);
        const u32 kv[9] = {e, e, bb, bb, bb, a, a, a, a};
        put(1, kv);
      }
#pragma unroll
      for (int mi = 2; mi < 4; ++mi) {
        const u32 a = *(const u16 LAS*)(uim + 768 + 128 * mi + 2 * lane), e = *(const u16 LAS*)(uim + 770 + 128 * mi + 2 * lane);
        const u32 tie = (lane & 1) ? e : a;
        const u32 kv[9] = {e, e, e, tie, a, a, a, a, a};
        put(mi, kv);
      }
      waitl<0>();
      if (lane < 4) *(u32x4 LAS*)(uim + lane * 16) = u32x4{0u, 0u, 0u, 0u};
      waitl<0>();
    } else {
      batch2();
#pragma unroll
      for (int i = 0; i < 6; ++i) asm volatile("" ::"v"(par[0][i]), "v"(par[1][i]));
      asm volatile("" ::"v"(vdt), "v"(vds));
    }
    LSTAMP(2);
    lds_wait4(sm + LM_CNT);
    tag = *(const u64 LAS*)(sm + LM_CNT + 16);
    u32x4 uv[4], yv[8];
    if (conv) {
      flag_wait(ws, FL_LN + p * 64, 64, tag, lane);
      const __amdgpu_buffer_rsrc_t ru = rsrc(ws + WS_UT, L * D * 2);
#pragma unroll
      for (int j = 0; j < 4; ++j) uv[j] = ld128(ru, (64 * p + (lane >> 2)) * 9216 + Q * 64 + (lane & 3) * 16, j * 16 * 9216);
    }
    waitv<0>();
    static_for<0, G1_NB>([&](auto K) {
      constexpr int i = decltype(K)::value;
      constexpr int prev = i ? G1_UPTO[i ? i - 1 : 0] : G1_N1 - 1;
      static_assert(G1_UPTO[i] - 10 < G1_POS[i] - 1 || G1_UPTO[i] < 10, "ring schedule");
      constexpr int need = G1_POS[i + 1] - 1;
      constexpr int y0 = prev / 2 - need / 2, y1 = (prev + 1) / 2 - (need + 1) / 2;
      if (need >= G1_N1) {
        if (p == 0) waitv<5 * y0>(); else if (p == 1) waitv<4 * y0>(); else if (p == 2) waitv<5 * y1>(); else waitv<4 * y1>();
      }
      LSTAMP(4 + 2 * i);
      bar();
      LSTAMP(5 + 2 * i);
      if (i == 0 && conv) {
#pragma unroll
        for (int j = 0; j < 4; ++j) *(u32x4 LAS*)(sm + LM_UIM + (lane & 3) * UIM_B + 64 + (64 * p + 16 * j + (lane >> 2)) * 16) = uv[j];
        waitl<0>();
      }
#pragma unroll
      for (int u = prev + 1; u <= G1_UPTO[i]; ++u)
        if ((u & 1) == (p >> 1)) { if (u < 18) issue1(u); else issue2(u - 18); }
    });
    bar();
    LSTAMP(22);
#pragma unroll
    for (int u = G1_UPTO[G1_NB - 1] + 1; u < 28; ++u)
      if ((u & 1) == (p >> 1)) issue2(u - 18);
    if (!conv) {
      waitv<0>();
      bar();
      LSTAMP(23);
      issue_own();
    } else {
      const float dsk = bl(vds);
      const char LAS* kp = kim + (c & 1) * KIM_C1 + (4 * (c >> 1) + g) * 16;
      char LAS* ub = uim + 64;
      const char LAS* up[4];
#pragma unroll
      for (int i = 0; i < 4; ++i) {
        const char LAS* q = ub + 2 * (8 * ((c >> 2) - g) + 2 * (c & 3) - 6) + i * 1024;
        asm volatile("" : "+v"(q));
        up[i] = q + 448;
      }
      char LAS* yp = ub + 2 * (8 * (g + 4 * (c >> 1)) + 4 * (c & 1));
      u32x2 sk[8];
#pragma unroll
      for (int T = 0; T < 8; ++T) {
        const u32x2 uu = *(const u32x2 LAS*)(yp + T * 512);
        sk[T] = u32x2{pk(fmul(bl(uu[0]), dsk), fmul(bh(uu[0]), dsk)), pk(fmul(bl(uu[1]), dsk), fmul(bh(uu[1]), dsk))};
        asm volatile("" : "+v"(sk[T]));
      }
      f32x4 acc[8];
#pragma unroll
      for (int T = 0; T < 8; ++T) { acc[T] = f32x4{0.f, 0.f, 0.f, 0.f}; asm volatile("" : "+v"(acc[T])); }
      u32x4 kf[2][8], uf[2][8];
      auto rdk = [&](int be, int a) { kf[be & 1][a] = *(const u32x4 LAS*)(kp + be * 64 + a * 512); };
      auto rdu = [&](int be, int a) {
        const u32 LAS* q = (const u32 LAS*)(up[a >> 1] - be * 64 + (a & 1) * 512);
        uf[be & 1][a] = u32x4{q[0], q[1], q[2], q[3]};
      };
#pragma unroll
      for (int a = 0; a < 8; ++a) { rdk(0, a); rdu(0, a); }
      waitv<0>(); waitl<0>();
      __builtin_amdgcn_s_setprio(3);
      SB;
      bar();
      SB;
      LSTAMP(23);
      static_for<0, 288>([&](auto N) {
        constexpr int n = decltype(N)::value, be = n / 36, m = n % 36;
        constexpr int a = m < 8 ? 0 : m < 15 ? 1 : m < 21 ? 2 : m < 26 ? 3 : m < 30 ? 4 : m < 33 ? 5 : m < 35 ? 6 : 7;
        constexpr int T = a + m - (8 * a - a * (a - 1) / 2);
        acc[T] = mfma16(uf[be & 1][T - a], kf[be & 1][a], acc[T]);
        SB;
        if (be < 7 && m < 16 && (m & 1) == 0) rdu(be + 1, m >> 1);
        if (be < 7 && m >= 16 && m < 24) rdk(be + 1, (m - 16) & 7);
        if (n == 251) { waitl<0>(); issue_own(); }
        SB;
      });
#pragma unroll
      for (int T = 0; T < 8; ++T) yv[T] = u32x4{pk(acc[T][0], acc[T][1]), pk(acc[T][2], acc[T][3]), sk[T][0], sk[T][1]};
      __builtin_amdgcn_s_setprio(0);
    }
    LSTAMP(24);
    u64 fa = 0;
    static_for<1, 8>([&](auto K) {
      constexpr int st = 2 * decltype(K)::value;
      constexpr int yng = st == 14 ? 0 : g2_upto(st - 2) - (st + 2);
      if (st == 14 && wid == 7 && !__all(fa == tag)) flag_wait(ws, FL_A + dj * 36, 36, tag, lane);
      if (st > 2) { if (n0 == 0) waitv<5 * yng>(); else waitv<4 * yng>(); }
      LSTAMP(24 + st);
      bar();
      LSTAMP(25 + st);
      if (conv && st == 6) {
        const __amdgpu_buffer_rsrc_t ao = rsrc(ws + WS_A2 + (size_t)Q * 16384 + 4096 * p, 4096);
        const char LAS* tp = sm + LM_UIM + (c >> 2) * UIM_B + 64 + 2 * (512 * p + 16 * g) + (c & 3) * 8;
#pragma unroll
        for (int i = 0; i < 8; ++i) __builtin_amdgcn_raw_buffer_store_b64(tr_read(tp + i * 128), ao, lane * 8, i * 512, SC1);
      }
      if (conv && st == 8 && tid == 256) flag_set(ws, FL_A + Q, tag);
#pragma unroll
      for (int j = g2_upto(st - 2) + 1; j <= g2_upto(st); ++j) {
        if (p < 2) issue2(2 * j); else issue2(2 * j + 1);
      }
      if (st == 12 && wid == 7) fa = __hip_atomic_load((GAS u64*)(ws + (FL_A + dj * 36 + min(lane, 35)) * 128), __ATOMIC_RELAXED, __HIP_MEMORY_SCOPE_AGENT);
      if (conv && st <= 4) {
        const bool odd = c & 1;
        char LAS* yp = uim + 64 + 2 * (8 * (g + 4 * (c >> 1)) + 4 * (c & 1));
#pragma unroll
        for (int T = 2 * st - 4; T < 2 * st; ++T) {
          const u32 own = odd ? yv[T][1] : yv[T][0];
          const u32 rcv = (u32)__builtin_amdgcn_mov_dpp((int)(odd ? yv[T][0] : yv[T][1]), 0xB1, 0xF, 0xF, true);
          const u32 ev = odd ? rcv : own, od = odd ? own : rcv;
          *(u32x2 LAS*)(yp + T * 512) = u32x2{pk(fadd(bl(ev), bl(yv[T][2])), fadd(bl(od), bh(yv[T][2]))),
                                              pk(fadd(bh(ev), bl(yv[T][3])), fadd(bh(od), bh(yv[T][3])))};
        }
        waitl<0>();
      }
          });
    load_base();
  }
  u32x2 base[2][2];
#pragma unroll
  for (int k = 0; k < 2; ++k)
#pragma unroll
    for (int i = 0; i < 2; ++i)
      base[k][i] = u32x2{pk(fadd(bl(xv[k][i][0]), bl(av[k][i][0])), fadd(bh(xv[k][i][0]), bh(av[k][i][0]))),
                         pk(fadd(bl(xv[k][i][1]), bl(av[k][i][1])), fadd(bh(xv[k][i][1]), bh(av[k][i][1])))};
  waitv<0>(); waitl<0>();
  bar();
  LSTAMP(52);
  if (tid == 0) flag_set(ws, FL_X + tile * 4 + s, tag);
  if (tid == 64 && b == 0) flag_set(ws, FL_EPOCH, tag);
  flag_wait(ws, FL_X + tile * 4, 4, tag, lane);
  LSTAMP(53);
  const __amdgpu_buffer_rsrc_t rs = rsrc(ws + WS_SLAB + (size_t)tile * 16 * SLAB_B, 16 * SLAB_B);
  u32x4 part[2][3];
#pragma unroll
  for (int k = 0; k < 2; ++k)
    if (k == 0 || wid == 4) {
#pragma unroll
      for (int e = 0; e < 3; ++e) part[k][e] = ld128(rs, lane * 16, (s * 4 + ((s + 1 + e) & 3)) * SLAB_B + (k ? 8 : wid) * 1024);
    }
#pragma unroll
  for (int k = 0; k < 2; ++k)
    if (k == 0 || wid == 4) {
      const int t = k ? 8 : wid;
#pragma unroll
      for (int i = 0; i < 2; ++i) {
        f32x4 v = *(const f32x4 LAS*)(sm + LM_UIM + (2 * t + i) * 1024 + lane * 16);
#pragma unroll
        for (int e = 0; e < 3; ++e)
          v += f32x4{bl(part[k][e][2 * i]), bh(part[k][e][2 * i]), bl(part[k][e][2 * i + 1]), bh(part[k][e][2 * i + 1])};
        const u32 r0 = pk(v[0], v[1]), r1 = pk(v[2], v[3]);
        const u32 q0 = pk(bl(r0) + bl(bias[k][0]), bh(r0) + bh(bias[k][0])), q1 = pk(bl(r1) + bl(bias[k][1]), bh(r1) + bh(bias[k][1]));
        const int l = 128 * l2 + 32 * s + 16 * i + c, d = 144 * dj + 16 * t + 4 * g;
        *(u32x2*)(out + (size_t)l * D + d) =
            u32x2{pk(bl(base[k][i][0]) + bl(q0), bh(base[k][i][0]) + bh(q0)), pk(bl(base[k][i][1]) + bl(q1), bh(base[k][i][1]) + bh(q1))};
      }
    }
  LSTAMP(54);
  RSTAMP(59);
}

extern "C" void kernel_launch(void* const* d_in, const int* in_sizes, int n_in, void* d_out, int out_size, void* d_ws,
                              size_t ws_size, hipStream_t stream) {
  const u16* const* in = (const u16* const*)d_in;
  (void)hipFuncSetAttribute((const void*)k_all, hipFuncAttributeMaxDynamicSharedMemorySize, LM_END);
  k_all<<<256, 512, LM_END, stream>>>(in[0], in[1], in[2], in[3], in[4], in[5], in[6], in[7], in[8], in[10], in[11], in[12], in[13],
                                      (char*)d_ws, (u16*)d_out);
}
```

```cpp
#include <hip/hip_runtime.h>
#include <hip/hip_bf16.h>
#include <stdint.h>
#include <type_traits>

typedef unsigned short u16;
typedef unsigned int u32;
typedef unsigned long long u64;
typedef __bf16 bf16x8 __attribute__((ext_vector_type(8)));
typedef __bf16 bf16x2 __attribute__((ext_vector_type(2)));
typedef short s16x4 __attribute__((ext_vector_type(4)));
typedef float f32x4 __attribute__((ext_vector_type(4)));
typedef float f32x2 __attribute__((ext_vector_type(2)));
typedef u32 u32x4 __attribute__((ext_vector_type(4)));
typedef u32 u32x2 __attribute__((ext_vector_type(2)));
#define LAS __attribute__((address_space(3)))
#define GAS __attribute__((address_space(1)))
#define POLL_SLEEP 4
#define SB __builtin_amdgcn_sched_barrier(0)

constexpr int L = 2048, D = 576, NM = 128, F = 4608;

constexpr int FL_EPOCH = 0;
constexpr int FL_LN = 1;
constexpr int FL_A = FL_LN + 256;
constexpr int FL_H = FL_A + 144;
constexpr int FL_X = FL_H + 256;
constexpr size_t WS_XNF = 131072;
constexpr size_t WS_UT = WS_XNF + (size_t)L * D * 2;
constexpr size_t WS_A2 = WS_UT + (size_t)L * D * 2;
constexpr size_t WS_HF = WS_A2 + (size_t)L * D * 2;
constexpr size_t WS_SLAB = WS_HF + (size_t)L * F * 2;
constexpr int SLAB_B = 9216, HB = 73728;
constexpr size_t WS_END = WS_SLAB + (size_t)64 * 16 * SLAB_B;
#define LSTAMP(i)
#define RSTAMP(i)

__device__ __forceinline__ float bl(u32 v) { return __uint_as_float(v << 16); }
__device__ __forceinline__ float bh(u32 v) { return __uint_as_float(v & 0xffff0000u); }
__device__ __forceinline__ float b2f(u16 v) { return __uint_as_float(((u32)v) << 16); }
__device__ __forceinline__ u32 pk(float lo, float hi) {
  bf16x2 r = {(__bf16)lo, (__bf16)hi};
  return __builtin_bit_cast(u32, r);
}
__device__ __forceinline__ float fadd(float a, float b) {
  float r;
  asm("v_add_f32 %0, %1, %2" : "=v"(r) : "v"(a), "v"(b));
  return r;
}
__device__ __forceinline__ float rb(float f) { return (float)(__bf16)f; }
__device__ __forceinline__ f32x4 mfma16(u32x4 a, u32x4 b, f32x4 c) {
  return __builtin_amdgcn_mfma_f32_16x16x32_bf16(__builtin_bit_cast(bf16x8, a), __builtin_bit_cast(bf16x8, b), c, 0, 0, 0);
}
__device__ __forceinline__ u32x2 tr_read(const char LAS* p) {
  return __builtin_bit_cast(u32x2, __builtin_amdgcn_ds_read_tr16_b64_v4i16((s16x4 LAS*)p));
}
__device__ __forceinline__ __amdgpu_buffer_rsrc_t rsrc(const void* p, int bytes) {
  return __builtin_amdgcn_make_buffer_rsrc((void*)p, 0, bytes, 0x00020000);
}
constexpr int SC1 = 16;
__device__ __forceinline__ void st128(u32x4 v, __amdgpu_buffer_rsrc_t r, int voff, int soff) {
  __builtin_amdgcn_raw_buffer_store_b128(v, r, voff, soff, SC1);
  asm volatile("s_nop 1" ::"v"(v) : "memory");
}
__device__ __forceinline__ u32x4 ld128(__amdgpu_buffer_rsrc_t r, int voff, int soff) {
  return __builtin_amdgcn_raw_buffer_load_b128(r, voff, soff, SC1);
}
__device__ __forceinline__ void glds16(const char* base, u32 voff, u32 dst) {
  asm volatile("s_mov_b32 m0, %2\n\ts_nop 0\n\tglobal_load_lds_dwordx4 %0, %1" ::"v"(voff), "s"(base), "s"(dst) : "memory");
}
template <int N> __device__ __forceinline__ void waitv() { asm volatile("s_waitcnt vmcnt(%0)" ::"i"(N) : "memory"); }
template <int N> __device__ __forceinline__ void waitl() { asm volatile("s_waitcnt lgkmcnt(%0)" ::"i"(N) : "memory"); }
__device__ __forceinline__ void bar() {
  asm volatile("" ::: "memory");
  __builtin_amdgcn_s_barrier();
  asm volatile("" ::: "memory");
}
template <int N> __device__ __forceinline__ void bars() {
#pragma unroll
  for (int i = 0; i < N; ++i) bar();
}
template <int I, int N, class Fn> __device__ __forceinline__ void static_for(Fn&& fn) {
  if constexpr (I < N) {
    fn(std::integral_constant<int, I>{});
    static_for<I + 1, N>(fn);
  }
}
__device__ __forceinline__ void flag_set(char* ws, int off, u64 tag) {
  __hip_atomic_store((GAS u64*)(ws + off * 128), tag, __ATOMIC_RELAXED, __HIP_MEMORY_SCOPE_AGENT);
}
__device__ __forceinline__ void flag_wait(char* ws, int off, int n, u64 tag, int lane) {
  GAS u64* p = (GAS u64*)(ws + (off + min(lane, n - 1)) * 128);
  for (u32 spin = 0; spin < (1u << 20); ++spin) {
    if (__all(__hip_atomic_load(p, __ATOMIC_RELAXED, __HIP_MEMORY_SCOPE_AGENT) == tag)) break;
    __builtin_amdgcn_s_sleep(POLL_SLEEP);
  }
  asm volatile("" ::: "memory");
}
__device__ __forceinline__ u32 lds_add(char LAS* p) {
  return __hip_atomic_fetch_add((u32 LAS*)p, 1u, __ATOMIC_RELAXED, __HIP_MEMORY_SCOPE_WORKGROUP);
}
__device__ __forceinline__ void lds_wait4(char LAS* p) {
  u32 v, sv;
  asm volatile(
      "1:\n\tds_read_b32 %0, %2\n\ts_waitcnt lgkmcnt(0)\n\tv_readfirstlane_b32 %1, %0\n\ts_cmp_lt_u32 %1, 4\n\ts_cbranch_scc0 2f\n\t"
      "s_sleep 1\n\ts_branch 1b\n2:"
      : "=&v"(v), "=&s"(sv) : "v"(p) : "memory", "scc");
}

__device__ __forceinline__ float fmul(float a, float b) {
  float r;
  asm("v_mul_f32 %0, %1, %2" : "=v"(r) : "v"(a), "v"(b));
  return r;
}
__device__ __forceinline__ f32x2 cmul(f32x2 a, f32x2 b) {
  float re, im;
  asm("v_mul_f32 %0, %2, %4\n\tv_mul_f32 %1, %2, %5\n\tv_fma_f32 %0, -%3, %5, %0\n\tv_fma_f32 %1, %3, %4, %1"
      : "=&v"(re), "=&v"(im) : "v"(a[0]), "v"(a[1]), "v"(b[0]), "v"(b[1]));
  return f32x2{re, im};
}
template <int E> __device__ __forceinline__ f32x2 zpow2(float p2, float qh, float ql) {
  constexpr float s = (float)(1 << E);
  const float mag = __builtin_amdgcn_exp2f(p2 * s);
  const float ph = __builtin_fmaf(ql, s, __builtin_amdgcn_fractf(qh * s));
  float re, im;
  asm("s_nop 1\n\tv_mul_f32 %0, %2, %4\n\tv_mul_f32 %1, %3, %4"
      : "=&v"(re), "=&v"(im) : "v"(__builtin_amdgcn_cosf(ph)), "v"(__builtin_amdgcn_sinf(ph)), "v"(mag));
  return f32x2{re, im};
}
template <int CTRL> __device__ __forceinline__ float dpp_add(float v) {
  return v + __builtin_bit_cast(float, __builtin_amdgcn_update_dpp(0, __builtin_bit_cast(int, v), CTRL, 0xF, 0xF, true));
}
__device__ __forceinline__ float sum32(float v) {
  v = dpp_add<0x128>(v);
  v = dpp_add<0x124>(v);
  v = dpp_add<0x122>(v);
  v = dpp_add<0x121>(v);
  return v + __builtin_bit_cast(float, __builtin_amdgcn_ds_swizzle(__builtin_bit_cast(int, v), 0x401F));
}
__device__ __forceinline__ u32 gelu2(float a, float b) {
  constexpr float C0 = -2.f * 1.44269504088896340736f * 0.7978845608028654f, C1 = C0 * 0.044715f;
  const f32x2 v = {a, b};
  const f32x2 z = v * __builtin_elementwise_fma(v * v, f32x2{C1, C1}, f32x2{C0, C0});
  const f32x2 d = f32x2{__builtin_amdgcn_exp2f(z[0]), __builtin_amdgcn_exp2f(z[1])} + f32x2{1.f, 1.f};
  const f32x2 r = v * f32x2{__builtin_amdgcn_rcpf(d[0]), __builtin_amdgcn_rcpf(d[1])};
  return pk(r[0], r[1]);
}
__device__ __forceinline__ u32x4 gelu8(f32x4 a, f32x4 b) {
  return u32x4{gelu2(a[0], a[1]), gelu2(a[2], a[3]), gelu2(b[0], b[1]), gelu2(b[2], b[3])};
}

constexpr int SLOT = 9216;
constexpr int KIM_C1 = 4640, UIM_B = 5952, KG_S = 272, LN_S = 1168;
constexpr int LM_KIM = 10 * SLOT;
constexpr int LM_UIM = 14 * SLOT;
constexpr int LM_LN = LM_UIM + 4 * UIM_B;
constexpr int LM_CNT = LM_LN + 8 * LN_S;
constexpr int LM_TAG = LM_CNT + 32;
constexpr int LM_END = LM_TAG + 16;
static_assert(LM_END <= 163840 && 4 * UIM_B >= 18432 && 21 * KG_S <= UIM_B && 32 * KG_S <= SLOT && UIM_B % 256 == 64, "LDS map");
constexpr int slot2(int j) { return j % 14 < 10 ? (j % 14 + 8) % 10 : j % 14; }

#define G2_XP 12
#define G2_X0 8
#define TOUCH 0
#define CODE_LINES 590
constexpr int G1_N0 = 4, G1_N1 = 10, G1_NB = 6;
constexpr int G1_POS[G1_NB + 1] = {6, 8, 10, 12, 14, 16, 18};
constexpr int G1_UPTO[G1_NB] = {13, 16, 18, 20, 22, 24};
constexpr bool g1_bar_before(int ks) {
  for (int i = 0; i < G1_NB; ++i)
    if (G1_POS[i] == ks) return true;
  return false;
}
constexpr int g2_upto(int st) { return st < 2 ? 6 : st + 6 < 17 ? st + 6 : 17; }

__global__ __launch_bounds__(512, 2) void k_all(const u16* __restrict__ x, const u16* __restrict__ Are, const u16* __restrict__ Aim,
                                                const u16* __restrict__ Bre, const u16* __restrict__ Bim, const u16* __restrict__ Cre,
                                                const u16* __restrict__ Cim, const u16* __restrict__ logdt, const u16* __restrict__ Dskip,
                                                const u16* __restrict__ w1, const u16* __restrict__ b1, const u16* __restrict__ w2,
                                                const u16* __restrict__ b2, char* ws, u16* __restrict__ out) {
  extern __shared__ __attribute__((aligned(16))) char smem[];
  char LAS* sm = (char LAS*)smem;
  const u32 smb = (u32)(uintptr_t)sm;
  const int tid = threadIdx.x, lane = tid & 63, wid = __builtin_amdgcn_readfirstlane(tid >> 6);
  const int b = blockIdx.x, xg = b & 7, idx = b >> 3, s = xg & 3;
  const int lb = 4 * (xg >> 2) + (idx >> 3), fb = 8 * s + (idx & 7);
  const int l2 = 8 * (xg >> 2) + (idx >> 2), dj = idx & 3, tile = l2 * 4 + dj;
  const int cl = xg * 4 + (idx >> 3);
  const int c = lane & 15, g = lane >> 4;
  const char* pc0;
  asm volatile("s_getpc_b64 %0" : "=s"(pc0));
  u64 tag;
  RSTAMP(62);
  LSTAMP(63);
  u32x2 xv[2][2] = {}, av[2][2] = {}, bias[2] = {};
  auto load_base = [&]() {
    const __amdgpu_buffer_rsrc_t ra = rsrc(ws + WS_A2, L * D * 2);
#pragma unroll
    for (int k = 0; k < 2; ++k) {
      const int t = k ? 8 : wid;
      if (k == 0 || wid == 4) {
        const int d = 144 * dj + 16 * t + 4 * g;
        bias[k] = *(const u32x2*)(b2 + d);
#pragma unroll
        for (int i = 0; i < 2; ++i) {
          const int l = 128 * l2 + 32 * s + 16 * i + c;
          xv[k][i] = *(const u32x2*)(x + (size_t)l * D + d);
          av[k][i] = __builtin_amdgcn_raw_buffer_load_b64(ra, ((d >> 2) * L + l) * 8, 0, SC1);
        }
      }
    }
  };
  if (__builtin_expect(wid < 4, 1)) {
    __builtin_amdgcn_s_setprio(2);
    u32x2 bq[9];
    {
      const int r = tid >> 5, lam = tid & 31;
      const u32* src = (const u32*)(x + (size_t)(b * 8 + r) * D) + lam;
      u32 raw[9];
#pragma unroll
      for (int i = 0; i < 9; ++i) raw[i] = src[32 * i];
      const u64 ep = __hip_atomic_load((GAS u64*)(ws + FL_EPOCH), __ATOMIC_RELAXED, __HIP_MEMORY_SCOPE_AGENT);
#pragma unroll
      for (int t = 0; t < 9; ++t) bq[t] = *(const u32x2*)(b1 + fb * 144 + 16 * t + 4 * g);
      if (tid < 2) *(u32x4 LAS*)(sm + LM_CNT + tid * 16) = u32x4{0u, 0u, 0u, 0u};
      waitl<0>();
      bar();
      f32x2 v[9];
#pragma unroll
      for (int i = 0; i < 9; ++i) v[i] = f32x2{bl(raw[i]), bh(raw[i])};
      f32x2 s2 = v[0];
#pragma unroll
      for (int i = 1; i < 9; ++i) s2 += v[i];
      const float mean = sum32(s2[0] + s2[1]) * (1.f / D);
      LSTAMP(0);
      f32x2 q2 = {0.f, 0.f};
#pragma unroll
      for (int i = 0; i < 9; ++i) { f32x2 d = v[i] - f32x2{mean, mean}; q2 = __builtin_elementwise_fma(d, d, q2); }
      const float q = sum32(q2[0] + q2[1]);
      const float m = rb(mean);
      const float sd = rb(__builtin_amdgcn_sqrtf(rb(rb(q * (1.f / D)) + 1e-5f)));
      const float rs = __builtin_amdgcn_rcpf(sd);
#pragma unroll
      for (int i = 0; i < 9; ++i) {
        const f32x2 d = v[i] - f32x2{m, m};
        const u32 t = pk(d[0], d[1]);
        const f32x2 n = f32x2{bl(t), bh(t)} * f32x2{rs, rs};
        *(u32 LAS*)(sm + LM_LN + r * LN_S + (lam + 32 * i) * 4) = pk(n[0], n[1]);
      }
      waitl<0>();
      if (lane == 0) lds_add(sm + LM_CNT);
      lds_wait4(sm + LM_CNT);
      LSTAMP(1);
      const __amdgpu_buffer_rsrc_t xo = rsrc(ws + WS_XNF + (size_t)(b >> 1) * 18432 + (b & 1) * 128, 18432);
      const __amdgpu_buffer_rsrc_t uo = rsrc(ws + WS_UT + (size_t)b * 9216, 9216);
      const char LAS* xp = sm + LM_LN + (lane & 7) * LN_S + (lane >> 5) * 64 + ((lane >> 3) & 3) * 8;
      const char LAS* tp = sm + LM_LN + ((lane >> 2) & 3) * LN_S + (lane >> 4) * 32 + (lane & 3) * 8;
      const int xoff = (lane >> 5) * 1024 + ((lane >> 3) & 3) * 256 + (lane & 7) * 16;
#pragma unroll
      for (int kk = 0; kk < 3; ++kk) {
        const int k = wid + 4 * kk;
        if (k < 9) {
          const u32x2 a = *(const u32x2 LAS*)(xp + k * 128), bb = *(const u32x2 LAS*)(xp + k * 128 + 32);
          const u32x2 t0 = tr_read(tp + k * 128), t1 = tr_read(tp + k * 128 + 4 * LN_S);
          st128(u32x4{a[0], a[1], bb[0], bb[1]}, xo, xoff, k * 2048);
          st128(u32x4{t0[0], t0[1], t1[0], t1[1]}, uo, lane * 16, k * 1024);
        }
      }
      tag = (((u64)__builtin_amdgcn_readfirstlane((u32)(ep >> 32)) << 32) | __builtin_amdgcn_readfirstlane((u32)ep)) * 0x9E3779B97F4A7C15ull +
            0xD1B54A32D192ED03ull;
      if (tid == 0) *(u64 LAS*)(sm + LM_TAG) = tag;
    }
    {
      constexpr int XP = 3, WD = 3, WR = 4;
      const int lt0 = 16 * lb + 4 * wid;
      const __amdgpu_buffer_rsrc_t rx = rsrc(ws + WS_XNF + (size_t)lt0 * 18432, 4 * 18432);
      f32x4 acc[9][4];
#pragma unroll
      for (int t = 0; t < 9; ++t) {
#pragma unroll
        for (int i = 0; i < 4; ++i) {
          acc[t][i] = f32x4{bl(bq[t][0]), bh(bq[t][0]), bl(bq[t][1]), bh(bq[t][1])};
          asm volatile("" : "+v"(acc[t][i]));
        }
      }
      waitv<0>(); waitl<0>();
      LSTAMP(2);
      if (lane == 0 && lds_add(sm + LM_CNT + 4) == 3u) flag_set(ws, FL_LN + b, tag);
      flag_wait(ws, FL_LN + 32 * lb + 8 * wid, 8, tag, lane);
      LSTAMP(3);
      u32x4 xf[XP + 1][4];
#pragma unroll
      for (int k = 0; k < XP; ++k)
#pragma unroll
        for (int i = 0; i < 4; ++i) xf[k][i] = ld128(rx, lane * 16, i * 18432 + k * 1024);
      SB;
      const char LAS* wp = sm + (lane >> 2) * 288 + (lane & 3) * 8;
      u32x2 wa[WR], wb[WR];
      const char LAS* wph = wp + 6 * SLOT;
      asm volatile("" : "+v"(wph));
      auto wptr = [&](int n) {
        const int sl = (n / 9) % 10;
        return (sl < 6 ? wp + sl * SLOT : wph + (sl - 6) * SLOT) + (n % 9) * 32;
      };
      lds_wait4(sm + LM_CNT + 12);
      LSTAMP(4);
      RSTAMP(55);
#pragma unroll
      for (int n = 0; n < WD; ++n) { wa[n] = tr_read(wptr(n)); wb[n] = tr_read(wptr(n) + 16 * 288); }
#pragma unroll
      for (int ks = 0; ks < 18; ++ks) {
#pragma unroll
        for (int t = 0; t < 9; ++t) {
          const int n = 9 * ks + t;
          const u32x4 w = {wa[n % WR][0], wa[n % WR][1], wb[n % WR][0], wb[n % WR][1]};
          acc[t][0] = mfma16(w, xf[ks % (XP + 1)][0], acc[t][0]);
          SB;
          if (t == 9 - WD && g1_bar_before(ks + 1)) bar();
          if (t == 9 - WD && ks + 1 == G1_N0) lds_wait4(sm + LM_CNT + 16);
          if (n == 161) bar();
          if (n + WD < 162) wa[(n + WD) % WR] = tr_read(wptr(n + WD));
          SB;
          acc[t][1] = mfma16(w, xf[ks % (XP + 1)][1], acc[t][1]);
          SB;
          if (n + WD < 162) wb[(n + WD) % WR] = tr_read(wptr(n + WD) + 16 * 288);
          SB;
          acc[t][2] = mfma16(w, xf[ks % (XP + 1)][2], acc[t][2]);
          SB;
          if (t < 4 && ks + XP < 18) xf[(ks + XP) % (XP + 1)][t] = ld128(rx, lane * 16, t * 18432 + (ks + XP) * 1024);
          SB;
          acc[t][3] = mfma16(w, xf[ks % (XP + 1)][3], acc[t][3]);
          SB;
        }
      }
      asm volatile("s_nop 15");
      SB;
      LSTAMP(5);
      RSTAMP(56);
      const __amdgpu_buffer_rsrc_t rh = rsrc(ws + WS_HF + (size_t)(cl * 8 + (idx & 7)) * HB, HB);
#pragma unroll
      for (int i = 0; i < 4; ++i)
#pragma unroll
        for (int r = 0; r < 4; ++r) st128(gelu8(acc[2 * r][i], acc[2 * r + 1][i]), rh, lane * 16, (4 * wid + i) * 4096 + r * 1024);
#pragma unroll
      for (int i = 0; i < 2; ++i) st128(gelu8(acc[8][2 * i], acc[8][2 * i + 1]), rh, lane * 16, 65536 + (2 * wid + i) * 1024);
      LSTAMP(6);
    }
    bar();
    {
      constexpr int XP = G2_XP, X0 = G2_X0, WD = 5, WR = 6, TAIL = 4;
      const __amdgpu_buffer_rsrc_t rh = rsrc(ws + WS_HF + (size_t)cl * 8 * HB, 8 * HB);
      const int pr = 4 * ((idx >> 2) & 1) + wid;
      const __amdgpu_buffer_rsrc_t rs = rsrc(ws + WS_SLAB + (size_t)tile * 16 * SLAB_B, 16 * SLAB_B);
      f32x4 acc[9][2];
#pragma unroll
      for (int t = 0; t < 9; ++t)
#pragma unroll
        for (int i = 0; i < 2; ++i) {
          acc[t][i] = f32x4{0.f, 0.f, 0.f, 0.f};
          asm volatile("" : "+v"(acc[t][i]));
        }
      const char LAS* wp = sm + (lane >> 2) * 288 + (lane & 3) * 8;
      u32x2 wa[WR], wb[WR];
      constexpr auto ord_ks = [](int n) { return n < 9 * (36 - TAIL) ? n / 9 : 36 - TAIL + (n - 9 * (36 - TAIL)) % TAIL; };
      constexpr auto ord_t = [](int n) { return n < 9 * (36 - TAIL) ? n % 9 : (n - 9 * (36 - TAIL)) / TAIL; };
      const char LAS* wph = wp + 7 * SLOT;
      asm volatile("" : "+v"(wph));
      auto wptr = [&](int n) {
        const int sl = slot2(ord_ks(n));
        return (sl < 7 ? wp + sl * SLOT : wph + (sl - 7) * SLOT) + ord_t(n) * 32;
      };
#pragma unroll
      for (int n = 0; n < WD; ++n) { wa[n] = tr_read(wptr(n)); wb[n] = tr_read(wptr(n) + 16 * 288); }
      u32x4 xf[XP + 1][2];
      auto hload = [&](int k, int i) {
        const int r = k % 9, P = ((idx >> 1) + k / 9) & 3;
        const int so = r < 8 ? (2 * P + (((r >> 2) ^ idx) & 1)) * HB + pr * 8192 + i * 4096 : (2 * P + i) * HB + 65536 + pr * 1024;
        xf[k % (XP + 1)][i] = ld128(rh, lane * 16 + (r < 8 ? (r & 3) * 1024 : 0), so);
      };
      waitv<0>();
      LSTAMP(7);
      const int flh = FL_H + cl * 8;
      if (lane == 0 && lds_add(sm + LM_CNT + 8) == 3u) flag_set(ws, flh + (idx & 7), tag);
      lds_wait4(sm + LM_CNT + 8);
#pragma unroll
      for (int k = 0; k < 4; ++k) { hload(k, 0); hload(k, 1); }
      flag_wait(ws, flh, 8, tag, lane);
      LSTAMP(8);
      RSTAMP(57);
#pragma unroll
      for (int k = 4; k < X0; ++k) { hload(k, 0); hload(k, 1); }
      SB;
      auto flush = [&](auto OWN, int tt) {
        if (decltype(OWN)::value) {
          *(f32x4 LAS*)(sm + LM_UIM + (2 * tt) * 1024 + lane * 16) = acc[tt][0];
          *(f32x4 LAS*)(sm + LM_UIM + (2 * tt + 1) * 1024 + lane * 16) = acc[tt][1];
        } else {
          const u32x4 v = {pk(acc[tt][0][0], acc[tt][0][1]), pk(acc[tt][0][2], acc[tt][0][3]), pk(acc[tt][1][0], acc[tt][1][1]), pk(acc[tt][1][2], acc[tt][1][3])};
          st128(v, rs, lane * 16, (wid * 4 + s) * SLAB_B + tt * 1024);
        }
      };
      auto step = [&](auto N, auto OWN) {
        constexpr int n = decltype(N)::value;
        constexpr int ks = ord_ks(n), t = ord_t(n);
        if (ks % 9 == 8 && t == 0) {
          const u32x4 a = xf[ks % (XP + 1)][0], bb = xf[ks % (XP + 1)][1];
          xf[ks % (XP + 1)][0] = u32x4{a[0], a[1], bb[0], bb[1]};
          xf[ks % (XP + 1)][1] = u32x4{a[2], a[3], bb[2], bb[3]};
        }
        const u32x4 w = {wa[n % WR][0], wa[n % WR][1], wb[n % WR][0], wb[n % WR][1]};
        acc[t][0] = mfma16(w, xf[ks % (XP + 1)][0], acc[t][0]);
        SB;
        if ((n + WD) % 36 == 18 && n + WD >= 54 && n + WD <= 270) bar();
        if (n + WD < 324) wa[(n + WD) % WR] = tr_read(wptr(n + WD));
        SB;
        acc[t][1] = mfma16(w, xf[ks % (XP + 1)][1], acc[t][1]);
        SB;
        if (n + WD < 324) wb[(n + WD) % WR] = tr_read(wptr(n + WD) + 16 * 288);
        if (ks < XP - X0 && (t & 1) && t < 8) hload(X0 + 2 * ks + (t >> 2), (t >> 1) & 1);
        if (ks >= XP - X0 && (t == 2 || t == 6) && ks + XP < 36) hload(ks + XP, (t >> 2) & 1);
        if (ks == 30 && t == 2) load_base();
        if (ks == 37 - TAIL && t > 0 && n >= 9 * (36 - TAIL)) flush(OWN, t > 0 ? t - 1 : 0);
        SB;
      };
      static_for<0, 9 * (36 - TAIL) + TAIL>([&](auto N) { step(N, std::false_type{}); });
      if (wid == s) {
        static_for<9 * (36 - TAIL) + TAIL, 324>([&](auto N) { step(N, std::true_type{}); });
        flush(std::true_type{}, 8);
      } else {
        static_for<9 * (36 - TAIL) + TAIL, 324>([&](auto N) { step(N, std::false_type{}); });
        flush(std::false_type{}, 8);
      }
      LSTAMP(9);
      RSTAMP(58);
    }
  } else {
    const bool conv = idx < 18;
    const int Q = xg * 18 + idx, p = wid - 4, n0 = (p & 1) * 5;
    u32 cpf, tpf;
    {
      const u32 co = min(idx + 32 * (16 * p + (lane & 15)), CODE_LINES - 1) * 128;
      asm volatile("global_load_dword %0, %1, %2" : "=v"(cpf) : "v"(co), "s"(pc0));
      const u32 to = p == 0   ? (u32)WS_XNF + lb * 294912 + min(lane, 8) * 32768
                     : p == 1 ? (u32)WS_HF + cl * 8 * HB + min(lane, 17) * 32768
                     : p == 2 ? (u32)WS_SLAB + tile * 16 * SLAB_B + min(lane, 4) * 32768
                              : (lane & 1 ? (u32)WS_XNF : (u32)WS_UT) + b * 9216;
      asm volatile("global_load_dword %0, %1, %2 sc1" : "=v"(tpf) : "v"(TOUCH ? to : co), "s"(TOUCH ? (const char*)ws : pc0));
    }
    u32 par[2][6], vdt, vds;
    {
      const int ch = conv ? 4 * Q + p : 0;
      const u32 o0 = (ch * NM + lane) * 2, o1 = ch * 2;
      const u16* src[6] = {Are, Aim, Bre, Bim, Cre, Cim};
#pragma unroll
      for (int i = 0; i < 6; ++i)
        asm volatile("global_load_ushort %0, %2, %3\n\tglobal_load_ushort %1, %2, %3 offset:128" : "=&v"(par[0][i]), "=&v"(par[1][i]) : "v"(o0), "s"(src[i]));
      asm volatile("global_load_ushort %0, %2, %3\n\tglobal_load_ushort %1, %2, %4" : "=&v"(vdt), "=&v"(vds) : "v"(o1), "s"(logdt), "s"(Dskip));
    }
    bar();
    u32 cr[9], cc[9];
#pragma unroll
    for (int n = 0; n < 9; ++n) { const int ci = 64 * n + lane; cr[n] = ci / 18; cc[n] = (ci % 18) * 16; }
    u32 v1[5], v2[9];
#pragma unroll
    for (int n = 0; n < 5; ++n) v1[n] = n0 ? cr[n < 4 ? 5 + n : 8] * (F * 2) + cc[n < 4 ? 5 + n : 8] : cr[n] * (F * 2) + cc[n];
    const char* w1s = (const char*)(w1 + fb * 144);
    auto issue1 = [&](int k) {
      const char* sp = w1s + (size_t)k * 32 * F * 2;
      const u32 dst = smb + (k % 10) * SLOT + n0 * 1024;
#pragma unroll
      for (int n = 0; n < 4; ++n) glds16(sp, v1[n], dst + n * 1024);
      if (n0 == 0) glds16(sp, v1[4], dst + 4096);
    };
#pragma unroll
    for (int k = 0; k < G1_N0 / 2; ++k) issue1(2 * k + (p >> 1));
    LSTAMP(0);
#pragma unroll
    for (int n = 0; n < 9; ++n) v2[n] = cr[n] * (D * 2) + cc[n];
    const u32 v24s = v2[4] + (lane >= 32 ? 128 * D * 2 : 0);
    const char* w2s = (const char*)(w2 + (size_t)1152 * s * D + 144 * dj);
    auto ksrc = [&](int k) {
      const int r = k % 9;
      return w2s + (size_t)(288 * (((idx >> 1) + k / 9) & 3) + (r < 8 ? 144 * (((r >> 2) ^ idx) & 1) + 32 * (r & 3) : 128)) * (D * 2);
    };
    auto issue2 = [&](int k) {
      const char* sp = ksrc(k);
      const u32 dst = smb + slot2(k) * SLOT;
      if (n0 == 0) {
#pragma unroll
        for (int n = 0; n < 4; ++n) glds16(sp, v2[n], dst + n * 1024);
        glds16(sp, k % 9 == 8 ? v24s : v2[4], dst + 4096);
      } else {
#pragma unroll
        for (int n = 5; n < 9; ++n) glds16(sp + (k % 9 == 8 ? 128 * D * 2 : 0), v2[n], dst + n * 1024);
      }
    };
    auto issue_own = [&]() {
      const char* sp = ksrc(10 + p);
#pragma unroll
      for (int n = 0; n < 9; ++n) glds16(sp, v2[n], smb + (10 + p) * SLOT + n * 1024);
    };
    auto batch2 = [&]() {
      waitv<0>();
      asm volatile("" ::"v"(cpf), "v"(tpf));
      if (lane == 0) lds_add(sm + LM_CNT + 12);
#pragma unroll
      for (int k = G1_N0 / 2; k < G1_N1 / 2; ++k) issue1(2 * k + (p >> 1));
    };
    auto landed2 = [&]() {
      waitv<0>();
      if (lane == 0) lds_add(sm + LM_CNT + 16);
    };
    char LAS* kim = sm + LM_KIM + p * SLOT;
    char LAS* uim = sm + LM_UIM + p * UIM_B;
    if (conv) {
      LSTAMP(3);
      waitv<4 * (G1_N0 / 2)>();
#pragma unroll
      for (int i = 0; i < 6; ++i) asm volatile("" : "+v"(par[0][i]), "+v"(par[1][i]));
      asm volatile("" : "+v"(vdt), "+v"(vds));
      const float dt = rb(__expf(bl(vdt)));
      f32x4 ka[2][2];
#pragma unroll
      for (int i = 0; i < 4; ++i) ka[i >> 1][i & 1] = f32x4{0.f, 0.f, 0.f, 0.f};
#pragma unroll 1
      for (int h = 0; h < 2; ++h) {
        float pv[6];
#pragma unroll
        for (int i = 0; i < 6; ++i) pv[i] = bl(h ? par[1][i] : par[0][i]);
        const float p2 = dt * pv[0] * 1.44269504088896340736f;
        const float q = dt * pv[1];
        constexpr float CH = 0.15915494309189535f;
        constexpr float CL = (float)(0.15915494309189533577 - (double)CH);
        const float qh = q * CH;
        const float ql = __builtin_fmaf(q, CL, __builtin_fmaf(q, CH, -qh));
        asm volatile("" ::"v"(ql));
        LSTAMP(50 + h);
        {
          f32x2 Z[16];
          auto put = [&](int i, f32x2 v) { *(u32 LAS*)(kim + i * KG_S + lane * 4) = pk(v[0], v[1]); };
          Z[0] = f32x2{1.f, 0.f};
          Z[1] = zpow2<3>(p2, qh, ql);
          Z[4] = zpow2<5>(p2, qh, ql);
          const f32x2 a7 = zpow2<7>(p2, qh, ql);
          Z[2] = cmul(Z[1], Z[1]);
          Z[8] = cmul(Z[4], Z[4]);
          Z[3] = cmul(Z[2], Z[1]);
#pragma unroll
          for (int i = 1; i < 4; ++i) Z[4 + i] = cmul(Z[4], Z[i]);
#pragma unroll
          for (int i = 1; i < 8; ++i) Z[8 + i] = cmul(Z[8], Z[i]);
#pragma unroll
          for (int i = 0; i < 16; ++i) put(i, Z[i]);
          put(16, a7);
#pragma unroll
          for (int i = 1; i < 16; ++i) put(16 + i, cmul(a7, Z[i]));
        }
        if (h == 1) landed2();
        {
          f32x2 H[16];
          auto put = [&](int i, f32x2 v) { *(u32 LAS*)(uim + i * KG_S + lane * 4) = pk(v[0], -v[1]); };
          H[0] = cmul(f32x2{pv[4], pv[5]}, f32x2{fmul(dt, pv[2]), fmul(dt, pv[3])});
          const f32x2 a0 = zpow2<0>(p2, qh, ql), a8 = zpow2<8>(p2, qh, ql), a10 = zpow2<10>(p2, qh, ql);
          const f32x2 a1 = cmul(a0, a0), a2 = cmul(a1, a1), a9 = cmul(a8, a8), a11 = cmul(a10, a10);
          H[1] = cmul(a0, H[0]);
          H[2] = cmul(a1, H[0]);
          H[3] = cmul(a1, H[1]);
#pragma unroll
          for (int i = 0; i < 4; ++i) H[4 + i] = cmul(a2, H[i]);
#pragma unroll
          for (int i = 0; i < 4; ++i) H[8 + i] = cmul(a8, H[2 * i]);
          H[12] = cmul(a9, H[0]);
          H[13] = cmul(a9, H[4]);
          H[14] = cmul(a9, H[8]);
          H[15] = cmul(a9, H[10]);
#pragma unroll
          for (int i = 0; i < 16; ++i) put(i, H[i]);
          put(16, cmul(a10, H[0]));
          put(17, cmul(a10, H[8]));
          put(18, cmul(a10, H[12]));
          put(19, cmul(a10, H[14]));
          put(20, cmul(a11, H[0]));
        }
        lds_wait4(sm + LM_CNT);
#pragma unroll
        for (int k = 0; k < 4; ++k) {
          u32x4 lo[2], hi[2];
#pragma unroll
          for (int i = 0; i < 2; ++i) {
            lo[i] = *(const u32x4 LAS*)(kim + (16 * i + c) * KG_S + k * 64 + g * 16);
            hi[i] = *(const u32x4 LAS*)(uim + min(16 * i + c, 20) * KG_S + k * 64 + g * 16);
          }
#pragma unroll
          for (int i = 0; i < 4; ++i) ka[i >> 1][i & 1] = mfma16(lo[i & 1], hi[i >> 1], ka[i >> 1][i & 1]);
        }
        if (h == 0) batch2();
      }
      LSTAMP(1);
#pragma unroll
      for (int i = 0; i < 4; ++i)
        *(u32x2 LAS*)(uim + (16 * (i >> 1) + c) * 64 + (i & 1) * 32 + g * 8) =
            u32x2{pk(ka[i >> 1][i & 1][0], ka[i >> 1][i & 1][1]), pk(ka[i >> 1][i & 1][2], ka[i >> 1][i & 1][3])};
    } else {
      batch2();
      landed2();
#pragma unroll
      for (int i = 0; i < 6; ++i) asm volatile("" ::"v"(par[0][i]), "v"(par[1][i]));
      asm volatile("" ::"v"(vdt), "v"(vds));
    }
    LSTAMP(2);
    u32x4 uv[4];
    u64 fl = 0;
    static_for<0, G1_NB>([&](auto K) {
      constexpr int i = decltype(K)::value;
      constexpr int prev = i ? G1_UPTO[i ? i - 1 : 0] : G1_N1 - 1;
      static_assert(G1_UPTO[i] - 10 < G1_POS[i] - 1 || G1_UPTO[i] < 10, "ring schedule");
      constexpr int need = G1_POS[i + 1] - 1;
      constexpr int y0 = prev / 2 - need / 2, y1 = (prev + 1) / 2 - (need + 1) / 2;
      if (need >= G1_N1) {
        if (p == 0) waitv<5 * y0>(); else if (p == 1) waitv<4 * y0>(); else if (p == 2) waitv<5 * y1>(); else waitv<4 * y1>();
      }
      LSTAMP(4 + 2 * i);
      bar();
      LSTAMP(5 + 2 * i);
      if (i == 0) tag = *(const u64 LAS*)(sm + LM_TAG);
      if (i == 0 && conv) fl = __hip_atomic_load((GAS u64*)(ws + (FL_LN + p * 64 + lane) * 128), __ATOMIC_RELAXED, __HIP_MEMORY_SCOPE_AGENT);
      if (i == 1 && conv) {
        if (!__all(fl == tag)) flag_wait(ws, FL_LN + p * 64, 64, tag, lane);
        asm volatile("" ::: "memory");
        const __amdgpu_buffer_rsrc_t ru = rsrc(ws + WS_UT, L * D * 2);
#pragma unroll
        for (int j = 0; j < 4; ++j) uv[j] = ld128(ru, (64 * p + (lane >> 2)) * 9216 + Q * 64 + (lane & 3) * 16, j * 16 * 9216);
      }
      if (i == 3 && conv) {
#pragma unroll
        for (int j = 0; j < 4; ++j) *(u32x4 LAS*)(sm + LM_UIM + (lane & 3) * UIM_B + 64 + (64 * p + 16 * j + (lane >> 2)) * 16) = uv[j];
        if (lane < 4) *(u32x4 LAS*)(uim + lane * 16) = u32x4{0u, 0u, 0u, 0u};
        waitl<0>();
      }
#pragma unroll
      for (int u = prev + 1; u <= G1_UPTO[i]; ++u)
        if ((u & 1) == (p >> 1)) { if (u < 18) issue1(u); else issue2(u - 18); }
      if (i < 2 && conv) {
        char LAS* ko = kim + (28 + lane) * 16;
        auto put = [&](int mi, const u32(&kv)[9]) {
          *(u32x4 LAS*)(ko + mi * 1024) = u32x4{kv[1] | (kv[2] << 16), kv[3] | (kv[4] << 16), kv[5] | (kv[6] << 16), kv[7] | (kv[8] << 16)};
          *(u32x4 LAS*)(ko + mi * 1024 + KIM_C1) = u32x4{kv[0] | (kv[1] << 16), kv[2] | (kv[3] << 16), kv[4] | (kv[5] << 16), kv[6] | (kv[7] << 16)};
        };
        if (i == 0) {
          {
            u32 kv[9];
#pragma unroll
            for (int e = 0; e < 9; ++e) {
              constexpr int EX[9] = {448, 384, 320, 256, 192, 128, 64, 0, 446}, R2[9] = {450, 640, 576, 576, 576, 512, 448, 448, 448};
              int o = 2 * lane + (lane < (e == 8 ? 33 : 32) ? EX[e] : R2[e]);
              if (e == 0 && lane == 63) o = 768;
              kv[e] = *(const u16 LAS*)(uim + o);
            }
            if (lane == 0) kv[8] = 0u;
            put(0, kv);
          }
        } else {
          if (lane < 56) *(u32x4 LAS*)(kim + (lane & 1) * KIM_C1 + (lane >> 1) * 16) = u32x4{0u, 0u, 0u, 0u};
          {
            const int oa = 768 + 128 * (lane >> 5) + 2 * (lane & 31), oe = 768 + 128 * ((lane + 1) >> 5) + 2 * ((lane + 1) & 31);
            const u32 a = *(const u16 LAS*)(uim + oa), bb = *(const u16 LAS*)(uim + oa + 64), e = *(const u16 LAS*)(uim + oe);
            const u32 kv[9] = {e, e, bb, bb, bb, a, a, a, a};
            put(1, kv);
          }
#pragma unroll
          for (int mi = 2; mi < 4; ++mi) {
            const u32 a = *(const u16 LAS*)(uim + 768 + 128 * mi + 2 * lane), e = *(const u16 LAS*)(uim + 770 + 128 * mi + 2 * lane);
            const u32 tie = (lane & 1) ? e : a;
            const u32 kv[9] = {e, e, e, tie, a, a, a, a, a};
            put(mi, kv);
          }
        }
        waitl<0>();
      }
    });
    bar();
    LSTAMP(22);
#pragma unroll
    for (int u = G1_UPTO[G1_NB - 1] + 1; u < 28; ++u)
      if ((u & 1) == (p >> 1)) issue2(u - 18);
    if (!conv) {
      waitv<0>();
      bar();
      LSTAMP(23);
      issue_own();
    } else {
      const float dsk = bl(vds);
      const char LAS* kp = kim + (c & 1) * KIM_C1 + (4 * (c >> 1) + g) * 16;
      char LAS* ub = uim + 64;
      const char LAS* up[4];
#pragma unroll
      for (int i = 0; i < 4; ++i) {
        const char LAS* q = ub + 2 * (8 * ((c >> 2) - g) + 2 * (c & 3) - 6) + i * 1024;
        asm volatile("" : "+v"(q));
        up[i] = q + 448;
      }
      char LAS* yp = ub + 2 * (8 * (g + 4 * (c >> 1)) + 4 * (c & 1));
      u32x2 sk[8];
#pragma unroll
      for (int T = 0; T < 8; ++T) {
        const u32x2 uu = *(const u32x2 LAS*)(yp + T * 512);
        sk[T] = u32x2{pk(fmul(bl(uu[0]), dsk), fmul(bh(uu[0]), dsk)), pk(fmul(bl(uu[1]), dsk), fmul(bh(uu[1]), dsk))};
        asm volatile("" : "+v"(sk[T]));
      }
      f32x4 acc[8];
#pragma unroll
      for (int T = 0; T < 8; ++T) { acc[T] = f32x4{0.f, 0.f, 0.f, 0.f}; asm volatile("" : "+v"(acc[T])); }
      u32x4 kf[2][8], uf[2][8];
      auto rdk = [&](int be, int a) { kf[be & 1][a] = *(const u32x4 LAS*)(kp + be * 64 + a * 512); };
      auto rdu = [&](int be, int a) {
        const u32 LAS* q = (const u32 LAS*)(up[a >> 1] - be * 64 + (a & 1) * 512);
        uf[be & 1][a] = u32x4{q[0], q[1], q[2], q[3]};
      };
#pragma unroll
      for (int a = 0; a < 8; ++a) { rdk(0, a); rdu(0, a); }
      waitv<0>(); waitl<0>();
      __builtin_amdgcn_s_setprio(3);
      SB;
      bar();
      SB;
      LSTAMP(23);
      static_for<0, 288>([&](auto N) {
        constexpr int n = decltype(N)::value, be = n / 36, m = n % 36;
        constexpr int a = m < 8 ? 0 : m < 15 ? 1 : m < 21 ? 2 : m < 26 ? 3 : m < 30 ? 4 : m < 33 ? 5 : m < 35 ? 6 : 7;
        constexpr int T = a + m - (8 * a - a * (a - 1) / 2);
        acc[T] = mfma16(uf[be & 1][T - a], kf[be & 1][a], acc[T]);
        SB;
        if (be < 7 && m < 16 && (m & 1) == 0) rdu(be + 1, m >> 1);
        if (be < 7 && m >= 16 && m < 24) rdk(be + 1, (m - 16) & 7);
        if (n == 251) { waitl<0>(); issue_own(); }
        SB;
      });
      const bool odd = c & 1;
#pragma unroll
      for (int T = 0; T < 8; ++T) {
        const u32 p01 = pk(acc[T][0], acc[T][1]), p23 = pk(acc[T][2], acc[T][3]);
        const u32 own = odd ? p23 : p01;
        const u32 rcv = (u32)__builtin_amdgcn_mov_dpp((int)(odd ? p01 : p23), 0xB1, 0xF, 0xF, true);
        const u32 ev = odd ? rcv : own, od = odd ? own : rcv;
        *(u32x2 LAS*)(yp + T * 512) = u32x2{pk(fadd(bl(ev), bl(sk[T][0])), fadd(bl(od), bh(sk[T][0]))),
                                            pk(fadd(bh(ev), bl(sk[T][1])), fadd(bh(od), bh(sk[T][1])))};
      }
      waitl<0>();
      __builtin_amdgcn_s_setprio(0);
    }
    LSTAMP(24);
    u64 fa = 0;
    static_for<1, 8>([&](auto K) {
      constexpr int st = 2 * decltype(K)::value;
      constexpr int yng = st == 14 ? 0 : g2_upto(st - 2) - (st + 2);
      if (st == 14 && wid == 7 && !__all(fa == tag)) flag_wait(ws, FL_A + dj * 36, 36, tag, lane);
      if (st > 2) { if (n0 == 0) waitv<5 * yng>(); else waitv<4 * yng>(); }
      LSTAMP(24 + st);
      bar();
      LSTAMP(25 + st);
      if (conv && st == 4) {
        const __amdgpu_buffer_rsrc_t ao = rsrc(ws + WS_A2 + (size_t)Q * 16384 + 4096 * p, 4096);
        const char LAS* tp = sm + LM_UIM + (c >> 2) * UIM_B + 64 + 2 * (512 * p + 16 * g) + (c & 3) * 8;
#pragma unroll
        for (int i = 0; i < 8; ++i) __builtin_amdgcn_raw_buffer_store_b64(tr_read(tp + i * 128), ao, lane * 8, i * 512, SC1);
      }
      if (conv && st == 6 && tid == 256) flag_set(ws, FL_A + Q, tag);
#pragma unroll
      for (int j = g2_upto(st - 2) + 1; j <= g2_upto(st); ++j) {
        if (p < 2) issue2(2 * j); else issue2(2 * j + 1);
      }
      if (st == 12 && wid == 7) fa = __hip_atomic_load((GAS u64*)(ws + (FL_A + dj * 36 + min(lane, 35)) * 128), __ATOMIC_RELAXED, __HIP_MEMORY_SCOPE_AGENT);
    });
    load_base();
  }
  u32x2 base[2][2];
#pragma unroll
  for (int k = 0; k < 2; ++k)
#pragma unroll
    for (int i = 0; i < 2; ++i)
      base[k][i] = u32x2{pk(fadd(bl(xv[k][i][0]), bl(av[k][i][0])), fadd(bh(xv[k][i][0]), bh(av[k][i][0]))),
                         pk(fadd(bl(xv[k][i][1]), bl(av[k][i][1])), fadd(bh(xv[k][i][1]), bh(av[k][i][1])))};
  waitv<0>(); waitl<0>();
  bar();
  LSTAMP(52);
  if (tid == 0) flag_set(ws, FL_X + tile * 4 + s, tag);
  if (tid == 64 && b == 0) flag_set(ws, FL_EPOCH, tag);
  flag_wait(ws, FL_X + tile * 4, 4, tag, lane);
  LSTAMP(53);
  const __amdgpu_buffer_rsrc_t rs = rsrc(ws + WS_SLAB + (size_t)tile * 16 * SLAB_B, 16 * SLAB_B);
  u32x4 part[2][3];
#pragma unroll
  for (int k = 0; k < 2; ++k)
    if (k == 0 || wid == 4) {
#pragma unroll
      for (int e = 0; e < 3; ++e) part[k][e] = ld128(rs, lane * 16, (s * 4 + ((s + 1 + e) & 3)) * SLAB_B + (k ? 8 : wid) * 1024);
    }
#pragma unroll
  for (int k = 0; k < 2; ++k)
    if (k == 0 || wid == 4) {
      const int t = k ? 8 : wid;
#pragma unroll
      for (int i = 0; i < 2; ++i) {
        f32x4 v = *(const f32x4 LAS*)(sm + LM_UIM + (2 * t + i) * 1024 + lane * 16);
#pragma unroll
        for (int e = 0; e < 3; ++e)
          v += f32x4{bl(part[k][e][2 * i]), bh(part[k][e][2 * i]), bl(part[k][e][2 * i + 1]), bh(part[k][e][2 * i + 1])};
        const u32 r0 = pk(v[0], v[1]), r1 = pk(v[2], v[3]);
        const u32 q0 = pk(bl(r0) + bl(bias[k][0]), bh(r0) + bh(bias[k][0])), q1 = pk(bl(r1) + bl(bias[k][1]), bh(r1) + bh(bias[k][1]));
        const int l = 128 * l2 + 32 * s + 16 * i + c, d = 144 * dj + 16 * t + 4 * g;
        *(u32x2*)(out + (size_t)l * D + d) =
            u32x2{pk(bl(base[k][i][0]) + bl(q0), bh(base[k][i][0]) + bh(q0)), pk(bl(base[k][i][1]) + bl(q1), bh(base[k][i][1]) + bh(q1))};
      }
    }
  LSTAMP(54);
  RSTAMP(59);
}

extern "C" void kernel_launch(void* const* d_in, const int* in_sizes, int n_in, void* d_out, int out_size, void* d_ws,
                              size_t ws_size, hipStream_t stream) {
  const u16* const* in = (const u16* const*)d_in;
  (void)hipFuncSetAttribute((const void*)k_all, hipFuncAttributeMaxDynamicSharedMemorySize, LM_END);
  k_all<<<256, 512, LM_END, stream>>>(in[0], in[1], in[2], in[3], in[4], in[5], in[6], in[7], in[8], in[10], in[11], in[12], in[13],
                                      (char*)d_ws, (u16*)d_out);
}
```

```cpp
#include <hip/hip_runtime.h>
#include <hip/hip_bf16.h>
#include <stdint.h>
#include <type_traits>

typedef unsigned short u16;
typedef unsigned int u32;
typedef unsigned long long u64;
typedef __bf16 bf16x8 __attribute__((ext_vector_type(8)));
typedef __bf16 bf16x2 __attribute__((ext_vector_type(2)));
typedef short s16x4 __attribute__((ext_vector_type(4)));
typedef float f32x4 __attribute__((ext_vector_type(4)));
typedef float f32x2 __attribute__((ext_vector_type(2)));
typedef u32 u32x4 __attribute__((ext_vector_type(4)));
typedef u32 u32x2 __attribute__((ext_vector_type(2)));
#define LAS __attribute__((address_space(3)))
#define GAS __attribute__((address_space(1)))
#define POLL_SLEEP 4
#define SB __builtin_amdgcn_sched_barrier(0)

constexpr int L = 2048, D = 576, NM = 128, F = 4608;

constexpr int FL_EPOCH = 0;
constexpr int FL_LN = 1;
constexpr int FL_A = FL_LN + 256;
constexpr int FL_H = FL_A + 144;
constexpr int FL_X = FL_H + 256;
constexpr int FL_LN2 = FL_X + 256;
constexpr size_t WS_XNF = 131072;
constexpr size_t WS_UT = WS_XNF + (size_t)L * D * 2;
constexpr size_t WS_A2 = WS_UT + (size_t)L * D * 2;
constexpr size_t WS_HF = WS_A2 + (size_t)L * D * 2;
constexpr size_t WS_SLAB = WS_HF + (size_t)L * F * 2;
constexpr int SLAB_B = 9216, HB = 73728;
constexpr size_t WS_END = WS_SLAB + (size_t)64 * 16 * SLAB_B;
#define LSTAMP(i)
#define RSTAMP(i)

__device__ __forceinline__ float bl(u32 v) { return __uint_as_float(v << 16); }
__device__ __forceinline__ float bh(u32 v) { return __uint_as_float(v & 0xffff0000u); }
__device__ __forceinline__ float b2f(u16 v) { return __uint_as_float(((u32)v) << 16); }
__device__ __forceinline__ u32 pk(float lo, float hi) {
  bf16x2 r = {(__bf16)lo, (__bf16)hi};
  return __builtin_bit_cast(u32, r);
}
__device__ __forceinline__ float fadd(float a, float b) {
  float r;
  asm("v_add_f32 %0, %1, %2" : "=v"(r) : "v"(a), "v"(b));
  return r;
}
__device__ __forceinline__ u32 add2(u32 a, u32 b) {
  u32 r, t0, t1;
  asm("v_lshlrev_b32 %0, 16, %3\n\tv_lshlrev_b32 %1, 16, %4\n\tv_add_f32 %0, %0, %1\n\tv_and_b32 %1, 0xffff0000, %3\n\t"
      "v_and_b32 %2, 0xffff0000, %4\n\tv_add_f32 %1, %1, %2\n\tv_cvt_pk_bf16_f32 %0, %0, %1"
      : "=&v"(r), "=&v"(t0), "=&v"(t1) : "v"(a), "v"(b));
  return r;
}
__device__ __forceinline__ u32 sum4(float lo, float hi, u32 a, u32 b, u32 c) {
  u32 r, t0, t1;
  asm("v_lshlrev_b32 %0, 16, %5\n\tv_and_b32 %1, 0xffff0000, %5\n\tv_add_f32 %0, %3, %0\n\tv_add_f32 %1, %4, %1\n\t"
      "v_lshlrev_b32 %2, 16, %6\n\tv_add_f32 %0, %0, %2\n\tv_and_b32 %2, 0xffff0000, %6\n\tv_add_f32 %1, %1, %2\n\t"
      "v_lshlrev_b32 %2, 16, %7\n\tv_add_f32 %0, %0, %2\n\tv_and_b32 %2, 0xffff0000, %7\n\tv_add_f32 %1, %1, %2\n\t"
      "v_cvt_pk_bf16_f32 %0, %0, %1"
      : "=&v"(r), "=&v"(t0), "=&v"(t1) : "v"(lo), "v"(hi), "v"(a), "v"(b), "v"(c));
  return r;
}
__device__ __forceinline__ float rb(float f) { return (float)(__bf16)f; }
__device__ __forceinline__ f32x4 mfma16(u32x4 a, u32x4 b, f32x4 c) {
  return __builtin_amdgcn_mfma_f32_16x16x32_bf16(__builtin_bit_cast(bf16x8, a), __builtin_bit_cast(bf16x8, b), c, 0, 0, 0);
}
__device__ __forceinline__ u32x2 tr_read(const char LAS* p) {
  return __builtin_bit_cast(u32x2, __builtin_amdgcn_ds_read_tr16_b64_v4i16((s16x4 LAS*)p));
}
__device__ __forceinline__ __amdgpu_buffer_rsrc_t rsrc(const void* p, int bytes) {
  return __builtin_amdgcn_make_buffer_rsrc((void*)p, 0, bytes, 0x00020000);
}
constexpr int SC1 = 16;
__device__ __forceinline__ u32 umin(u32 a, u32 b) { return a < b ? a : b; }

__device__ __forceinline__ void st128(u32x4 v, __amdgpu_buffer_rsrc_t r, int voff, int soff) {
  __builtin_amdgcn_raw_buffer_store_b128(v, r, voff, soff, SC1);
  asm volatile("s_nop 1" ::"v"(v) : "memory");
}
__device__ __forceinline__ u32x4 ld128(__amdgpu_buffer_rsrc_t r, int voff, int soff) {
  return __builtin_amdgcn_raw_buffer_load_b128(r, voff, soff, SC1);
}
__device__ __forceinline__ void glds16(const char* base, u32 voff, u32 dst) {
  asm volatile("s_mov_b32 m0, %2\n\ts_nop 0\n\tglobal_load_lds_dwordx4 %0, %1" ::"v"(voff), "s"(base), "s"(dst) : "memory");
}
__device__ __forceinline__ void glds4(const void* base, u32 voff, u32 dst) {
  asm volatile("s_mov_b32 m0, %2\n\ts_nop 0\n\tglobal_load_lds_dword %0, %1" ::"v"(voff), "s"(base), "s"(dst) : "memory");
}
template <int N> __device__ __forceinline__ void waitv() { asm volatile("s_waitcnt vmcnt(%0)" ::"i"(N) : "memory"); }
template <int N> __device__ __forceinline__ void waitl() { asm volatile("s_waitcnt lgkmcnt(%0)" ::"i"(N) : "memory"); }
__device__ __forceinline__ void bar() {
  asm volatile("" ::: "memory");
  __builtin_amdgcn_s_barrier();
  asm volatile("" ::: "memory");
}
template <int N> __device__ __forceinline__ void bars() {
#pragma unroll
  for (int i = 0; i < N; ++i) bar();
}
template <int I, int N, class Fn> __device__ __forceinline__ void static_for(Fn&& fn) {
  if constexpr (I < N) {
    fn(std::integral_constant<int, I>{});
    static_for<I + 1, N>(fn);
  }
}
__device__ __forceinline__ void flag_set(char* ws, int off, u64 tag) {
  __hip_atomic_store((GAS u64*)(ws + off * 128), tag, __ATOMIC_RELAXED, __HIP_MEMORY_SCOPE_AGENT);
}
__device__ __forceinline__ void flag_wait(char* ws, int off, int n, u64 tag, int lane) {
  GAS u64* p = (GAS u64*)(ws + (off + min(lane, n - 1)) * 128);
  u32 zero = 0;
  asm volatile("" : "+v"(zero));
#pragma unroll 1
  for (u32 spin = 0; spin < (1u << 20); ++spin) {
    if (__all(__hip_atomic_load(p, __ATOMIC_RELAXED, __HIP_MEMORY_SCOPE_AGENT) == tag)) break;
    __builtin_amdgcn_s_sleep(POLL_SLEEP);
  }
  asm volatile("" ::: "memory");
}
__device__ __forceinline__ u32 lds_add(char LAS* p) {
  return __hip_atomic_fetch_add((u32 LAS*)p, 1u, __ATOMIC_RELAXED, __HIP_MEMORY_SCOPE_WORKGROUP);
}
__device__ __forceinline__ void lds_wait4(char LAS* p) {
  u32 v, sv;
  asm volatile(
      "1:\n\tds_read_b32 %0, %2\n\ts_waitcnt lgkmcnt(0)\n\tv_readfirstlane_b32 %1, %0\n\ts_cmp_lt_u32 %1, 4\n\ts_cbranch_scc0 2f\n\t"
      "s_sleep 1\n\ts_branch 1b\n2:"
      : "=&v"(v), "=&s"(sv) : "v"(p) : "memory", "scc");
}

__device__ __forceinline__ float fmul(float a, float b) {
  float r;
  asm("v_mul_f32 %0, %1, %2" : "=v"(r) : "v"(a), "v"(b));
  return r;
}
__device__ __forceinline__ f32x2 cmul(f32x2 a, f32x2 b) {
  float re, im;
  asm("v_mul_f32 %0, %2, %4\n\tv_mul_f32 %1, %2, %5\n\tv_fma_f32 %0, -%3, %5, %0\n\tv_fma_f32 %1, %3, %4, %1"
      : "=&v"(re), "=&v"(im) : "v"(a[0]), "v"(a[1]), "v"(b[0]), "v"(b[1]));
  return f32x2{re, im};
}
template <int E> __device__ __forceinline__ f32x2 zpow2(float p2, float qh, float ql) {
  constexpr float s = (float)(1 << E);
  const float mag = __builtin_amdgcn_exp2f(p2 * s);
  const float ph = __builtin_fmaf(ql, s, __builtin_amdgcn_fractf(qh * s));
  float re, im;
  asm("s_nop 1\n\tv_mul_f32 %0, %2, %4\n\tv_mul_f32 %1, %3, %4"
      : "=&v"(re), "=&v"(im) : "v"(__builtin_amdgcn_cosf(ph)), "v"(__builtin_amdgcn_sinf(ph)), "v"(mag));
  return f32x2{re, im};
}
template <int CTRL> __device__ __forceinline__ float dpp_add(float v) {
  return v + __builtin_bit_cast(float, __builtin_amdgcn_update_dpp(0, __builtin_bit_cast(int, v), CTRL, 0xF, 0xF, true));
}
__device__ __forceinline__ float sum32(float v) {
  v = dpp_add<0x128>(v);
  v = dpp_add<0x124>(v);
  v = dpp_add<0x122>(v);
  v = dpp_add<0x121>(v);
  return v + __builtin_bit_cast(float, __builtin_amdgcn_ds_swizzle(__builtin_bit_cast(int, v), 0x401F));
}
__device__ __forceinline__ u32 gelu2(float a, float b) {
  constexpr float C0 = -2.f * 1.44269504088896340736f * 0.7978845608028654f, C1 = C0 * 0.044715f;
  const f32x2 v = {a, b};
  const f32x2 z = v * __builtin_elementwise_fma(v * v, f32x2{C1, C1}, f32x2{C0, C0});
  const f32x2 d = f32x2{__builtin_amdgcn_exp2f(z[0]), __builtin_amdgcn_exp2f(z[1])} + f32x2{1.f, 1.f};
  const f32x2 r = v * f32x2{__builtin_amdgcn_rcpf(d[0]), __builtin_amdgcn_rcpf(d[1])};
  return pk(r[0], r[1]);
}
__device__ __forceinline__ u32x4 gelu8(f32x4 a, f32x4 b) {
  return u32x4{gelu2(a[0], a[1]), gelu2(a[2], a[3]), gelu2(b[0], b[1]), gelu2(b[2], b[3])};
}

constexpr int SLOT = 9216;
constexpr int KIM_C1 = 4640, UIM_B = 5952, KG_S = 272, LN_S = 1168;
constexpr int LM_KIM = 10 * SLOT;
constexpr int LM_UIM = 14 * SLOT;
constexpr int LM_LN = LM_UIM + 4 * UIM_B;
constexpr int LM_CNT = LM_LN + 8 * LN_S;
constexpr int LM_TAG = LM_CNT + 32;
constexpr int LM_SINK = LM_TAG + 16;
constexpr int LM_END = LM_SINK + 256;
static_assert(LM_END <= 163840 && 4 * UIM_B >= 18432 && 21 * KG_S <= UIM_B && 32 * KG_S <= SLOT && UIM_B % 256 == 64, "LDS map");
constexpr int slot2(int j) { return j % 14 < 10 ? (j % 14 + 8) % 10 : j % 14; }

#define G2_XP 15
#define G2_NUM 2
#define G2_DEN 3
#define G2_X0 8
#define L2PF 3
constexpr int G1_N0 = 4, G1_N1 = 10, G1_NB = 6;
constexpr int G1_POS[G1_NB + 1] = {6, 8, 10, 12, 14, 16, 18};
constexpr int G1_UPTO[G1_NB] = {13, 16, 18, 20, 22, 24};
constexpr bool g1_bar_before(int ks) {
  for (int i = 0; i < G1_NB; ++i)
    if (G1_POS[i] == ks) return true;
  return false;
}
constexpr int g2_upto(int st) { return st < 2 ? 6 : st + 6 < 17 ? st + 6 : 17; }

constexpr int h_upto(int ks) {
  const int kn = G2_XP - G2_X0 - 1;
  const int v = ks <= kn ? G2_X0 + 1 + 2 * ks : G2_X0 + 1 + 2 * kn + (ks - kn) * G2_NUM / G2_DEN;
  return v < 35 ? v : 35;
}

__global__ __launch_bounds__(512, 2) void k_all(const u16* __restrict__ x, const u16* __restrict__ Are, const u16* __restrict__ Aim,
                                                const u16* __restrict__ Bre, const u16* __restrict__ Bim, const u16* __restrict__ Cre,
                                                const u16* __restrict__ Cim, const u16* __restrict__ logdt, const u16* __restrict__ Dskip,
                                                const u16* __restrict__ w1, const u16* __restrict__ b1, const u16* __restrict__ w2,
                                                const u16* __restrict__ b2, char* ws, u16* __restrict__ out) {
  extern __shared__ __attribute__((aligned(16))) char smem[];
  char LAS* sm = (char LAS*)smem;
  const u32 smb = (u32)(uintptr_t)sm;
  const int tid = threadIdx.x, lane = tid & 63, wid = __builtin_amdgcn_readfirstlane(tid >> 6);
  const int b = blockIdx.x, xg = b & 7, idx = b >> 3, s = xg & 3;
  const int lb = 4 * (xg >> 2) + (idx >> 3), fb = 8 * s + (idx & 7);
  const int l2 = 8 * (xg >> 2) + (idx >> 2), dj = idx & 3, tile = l2 * 4 + dj;
  const int cl = xg * 4 + (idx >> 3);
  const int c = lane & 15, g = lane >> 4;
  const char* pc0;
  u32 code_len;
  asm volatile("s_getpc_b64 %0\n.Lk_all_head:\n\ts_mov_b32 %1, .Lk_all_tail-.Lk_all_head" : "=s"(pc0), "=s"(code_len));
  u64 tag;
  RSTAMP(62);
  LSTAMP(63);
  const __amdgpu_buffer_rsrc_t rs = rsrc(ws + WS_SLAB + (size_t)tile * 16 * SLAB_B, 16 * SLAB_B);
  struct Epi { u32x2 xv[2], bias; u32x4 av; };
  auto load_base = [&](Epi& e, int t) {
    const int d = 144 * dj + 16 * t + 4 * g, l = 128 * l2 + 32 * s + c;
    e.bias = *(const u32x2*)(b2 + d);
    e.av = ld128(rsrc(ws + WS_A2, L * D * 2), (d >> 2) * 16384 + (4 * l2 + s) * 256 + c * 16, 0);
    e.xv[0] = *(const u32x2*)(x + (size_t)l * D + d);
    e.xv[1] = *(const u32x2*)(x + (size_t)(l + 16) * D + d);
  };
  auto finish = [&](auto NT, const Epi* e, const int* t, const f32x4 (*own)[2]) {
    constexpr int nt = decltype(NT)::value;
    u32 base[nt][4];
#pragma unroll
    for (int j = 0; j < nt; ++j)
#pragma unroll
      for (int i = 0; i < 4; ++i) base[j][i] = add2(e[j].xv[i >> 1][i & 1], e[j].av[i]);
    flag_wait(ws, FL_X + tile * 4, 4, tag, lane);
    LSTAMP(53);
    u32x4 part[nt][3];
#pragma unroll
    for (int j = 0; j < nt; ++j)
#pragma unroll
      for (int k = 0; k < 3; ++k) part[j][k] = ld128(rs, lane * 16, (s * 4 + ((s + 1 + k) & 3)) * SLAB_B + t[j] * 1024);
#pragma unroll
    for (int j = 0; j < nt; ++j) {
      u16* dst = out + (size_t)(128 * l2 + 32 * s + c) * D + 144 * dj + 16 * t[j] + 4 * g;
#pragma unroll
      for (int i = 0; i < 2; ++i) {
        u32 o[2];
#pragma unroll
        for (int h = 0; h < 2; ++h) {
          const u32 r = sum4(own[j][i][2 * h], own[j][i][2 * h + 1], part[j][0][2 * i + h], part[j][1][2 * i + h], part[j][2][2 * i + h]);
          o[h] = add2(base[j][2 * i + h], add2(r, e[j].bias[h]));
        }
        *(u32x2*)(dst + i * 16 * D) = u32x2{o[0], o[1]};
      }
    }
  };
  auto finish_lds = [&](auto NT, const Epi* e, const int* t) {
    constexpr int nt = decltype(NT)::value;
    while (__builtin_amdgcn_readfirstlane(*(volatile u32 LAS*)(sm + LM_CNT + 24)) < 8u) __builtin_amdgcn_s_sleep(2);
    asm volatile("" ::: "memory");
    f32x4 own[nt][2];
#pragma unroll
    for (int j = 0; j < nt; ++j)
#pragma unroll
      for (int i = 0; i < 2; ++i) own[j][i] = *(const f32x4 LAS*)(sm + LM_UIM + (2 * t[j] + i) * 1024 + lane * 16);
    finish(NT, e, t, own);
  };
  if (__builtin_expect(wid < 4, 1)) {
    __builtin_amdgcn_s_setprio(2);
    u32x2 bq[9];
    {
      const int r = tid >> 5, lam = tid & 31;
      const u32* src = (const u32*)(x + (size_t)(b * 8 + r) * D) + lam;
      u32 raw[9];
#pragma unroll
      for (int i = 0; i < 9; ++i) raw[i] = src[32 * i];
      const u64 ep = __hip_atomic_load((GAS u64*)(ws + FL_EPOCH), __ATOMIC_RELAXED, __HIP_MEMORY_SCOPE_AGENT);
#pragma unroll
      for (int t = 0; t < 9; ++t) bq[t] = *(const u32x2*)(b1 + fb * 144 + 16 * t + 4 * g);
      if (tid < 2) *(u32x4 LAS*)(sm + LM_CNT + tid * 16) = u32x4{0u, 0u, 0u, 0u};
      waitl<0>();
      bar();
      f32x2 v[9];
#pragma unroll
      for (int i = 0; i < 9; ++i) v[i] = f32x2{bl(raw[i]), bh(raw[i])};
      f32x2 s2 = v[0];
#pragma unroll
      for (int i = 1; i < 9; ++i) s2 += v[i];
      const float mean = sum32(s2[0] + s2[1]) * (1.f / D);
      LSTAMP(0);
      f32x2 q2 = {0.f, 0.f};
#pragma unroll
      for (int i = 0; i < 9; ++i) { f32x2 d = v[i] - f32x2{mean, mean}; q2 = __builtin_elementwise_fma(d, d, q2); }
      const float q = sum32(q2[0] + q2[1]);
      const float m = rb(mean);
      const float sd = rb(__builtin_amdgcn_sqrtf(rb(rb(q * (1.f / D)) + 1e-5f)));
      const float rs = __builtin_amdgcn_rcpf(sd);
#pragma unroll
      for (int i = 0; i < 9; ++i) {
        const f32x2 d = v[i] - f32x2{m, m};
        const u32 t = pk(d[0], d[1]);
        const f32x2 n = f32x2{bl(t), bh(t)} * f32x2{rs, rs};
        *(u32 LAS*)(sm + LM_LN + r * LN_S + (lam + 32 * i) * 4) = pk(n[0], n[1]);
      }
      waitl<0>();
      if (lane == 0) lds_add(sm + LM_CNT);
      lds_wait4(sm + LM_CNT);
      LSTAMP(1);
      const __amdgpu_buffer_rsrc_t xo = rsrc(ws + WS_XNF + (size_t)(b >> 1) * 18432 + (b & 1) * 128, 18432);
      const __amdgpu_buffer_rsrc_t uo = rsrc(ws + WS_UT + (size_t)b * 9216, 9216);
      const char LAS* xp = sm + LM_LN + (lane & 7) * LN_S + (lane >> 5) * 64 + ((lane >> 3) & 3) * 8;
      const char LAS* tp = sm + LM_LN + ((lane >> 2) & 3) * LN_S + (lane >> 4) * 32 + (lane & 3) * 8;
      const int xoff = (lane >> 5) * 1024 + ((lane >> 3) & 3) * 256 + (lane & 7) * 16;
#pragma unroll
      for (int kk = 0; kk < 3; ++kk) {
        const int k = wid + 4 * kk;
        if (k < 9) {
          const u32x2 a = *(const u32x2 LAS*)(xp + k * 128), bb = *(const u32x2 LAS*)(xp + k * 128 + 32);
          const u32x2 t0 = tr_read(tp + k * 128), t1 = tr_read(tp + k * 128 + 4 * LN_S);
          st128(u32x4{a[0], a[1], bb[0], bb[1]}, xo, xoff, k * 2048);
          st128(u32x4{t0[0], t0[1], t1[0], t1[1]}, uo, lane * 16, k * 1024);
        }
      }
      tag = (((u64)__builtin_amdgcn_readfirstlane((u32)(ep >> 32)) << 32) | __builtin_amdgcn_readfirstlane((u32)ep)) * 0x9E3779B97F4A7C15ull +
            0xD1B54A32D192ED03ull;
      if (tid == 0) *(u64 LAS*)(sm + LM_TAG) = tag;
    }
    {
      constexpr int XP = 3, WD = 3, WR = 4;
      const int lt0 = 16 * lb + 4 * wid;
      const __amdgpu_buffer_rsrc_t rx = rsrc(ws + WS_XNF + (size_t)lt0 * 18432, 4 * 18432);
      f32x4 acc[9][4];
#pragma unroll
      for (int t = 0; t < 9; ++t) {
#pragma unroll
        for (int i = 0; i < 4; ++i) {
          acc[t][i] = f32x4{bl(bq[t][0]), bh(bq[t][0]), bl(bq[t][1]), bh(bq[t][1])};
          asm volatile("" : "+v"(acc[t][i]));
        }
      }
      waitv<0>(); waitl<0>();
      LSTAMP(2);
      if (lane == 0 && lds_add(sm + LM_CNT + 4) == 3u) {
        flag_set(ws, FL_LN + b, tag);
        flag_set(ws + b * 8, FL_LN2, tag);
      }
      flag_wait(ws, FL_LN + 32 * lb + 8 * wid, 8, tag, lane);
      LSTAMP(3);
      u32x4 xf[XP + 1][4];
#pragma unroll
      for (int k = 0; k < XP; ++k)
#pragma unroll
        for (int i = 0; i < 4; ++i) xf[k][i] = ld128(rx, lane * 16, i * 18432 + k * 1024);
      SB;
      const char LAS* wp = sm + (lane >> 2) * 288 + (lane & 3) * 8;
      u32x2 wa[WR], wb[WR];
      const char LAS* wph = wp + 6 * SLOT;
      asm volatile("" : "+v"(wph));
      auto wptr = [&](int n) {
        const int sl = (n / 9) % 10;
        return (sl < 6 ? wp + sl * SLOT : wph + (sl - 6) * SLOT) + (n % 9) * 32;
      };
      lds_wait4(sm + LM_CNT + 12);
      LSTAMP(4);
      RSTAMP(55);
#pragma unroll
      for (int n = 0; n < WD; ++n) { wa[n] = tr_read(wptr(n)); wb[n] = tr_read(wptr(n) + 16 * 288); }
#pragma unroll
      for (int ks = 0; ks < 18; ++ks) {
#pragma unroll
        for (int t = 0; t < 9; ++t) {
          const int n = 9 * ks + t;
          const u32x4 w = {wa[n % WR][0], wa[n % WR][1], wb[n % WR][0], wb[n % WR][1]};
          acc[t][0] = mfma16(w, xf[ks % (XP + 1)][0], acc[t][0]);
          SB;
          if (t == 9 - WD && g1_bar_before(ks + 1)) bar();
          if (t == 9 - WD && ks + 1 == G1_N0) lds_wait4(sm + LM_CNT + 16);
          if (n == 161) bar();
          if (n + WD < 162) wa[(n + WD) % WR] = tr_read(wptr(n + WD));
          SB;
          acc[t][1] = mfma16(w, xf[ks % (XP + 1)][1], acc[t][1]);
          SB;
          if (n + WD < 162) wb[(n + WD) % WR] = tr_read(wptr(n + WD) + 16 * 288);
          SB;
          acc[t][2] = mfma16(w, xf[ks % (XP + 1)][2], acc[t][2]);
          SB;
          if (t < 4 && ks + XP < 18) xf[(ks + XP) % (XP + 1)][t] = ld128(rx, lane * 16, t * 18432 + (ks + XP) * 1024);
          SB;
          acc[t][3] = mfma16(w, xf[ks % (XP + 1)][3], acc[t][3]);
          SB;
        }
      }
      asm volatile("s_nop 15");
      SB;
      LSTAMP(5);
      RSTAMP(56);
      const __amdgpu_buffer_rsrc_t rh = rsrc(ws + WS_HF + (size_t)(cl * 8 + (idx & 7)) * HB, HB);
#pragma unroll
      for (int i = 0; i < 4; ++i)
#pragma unroll
        for (int r = 0; r < 4; ++r) st128(gelu8(acc[2 * r][i], acc[2 * r + 1][i]), rh, lane * 16, (4 * wid + i) * 4096 + r * 1024);
#pragma unroll
      for (int i = 0; i < 2; ++i) st128(gelu8(acc[8][2 * i], acc[8][2 * i + 1]), rh, lane * 16, 65536 + (2 * wid + i) * 1024);
      LSTAMP(6);
    }
    bar();
    {
      constexpr int XP = G2_XP, X0 = G2_X0, WD = 5, WR = 6, TAIL = 4;
      const __amdgpu_buffer_rsrc_t rh = rsrc(ws + WS_HF + (size_t)cl * 8 * HB, 8 * HB);
      const int pr = 4 * ((idx >> 2) & 1) + wid;
      f32x4 acc[9][2];
#pragma unroll
      for (int t = 0; t < 9; ++t)
#pragma unroll
        for (int i = 0; i < 2; ++i) {
          acc[t][i] = f32x4{0.f, 0.f, 0.f, 0.f};
          asm volatile("" : "+v"(acc[t][i]));
        }
      const char LAS* wp = sm + (lane >> 2) * 288 + (lane & 3) * 8;
      u32x2 wa[WR], wb[WR];
      constexpr auto ord_ks = [](int n) { return n < 9 * (36 - TAIL) ? n / 9 : 36 - TAIL + (n - 9 * (36 - TAIL)) % TAIL; };
      constexpr auto ord_t = [](int n) { return n < 9 * (36 - TAIL) ? n % 9 : (n - 9 * (36 - TAIL)) / TAIL; };
      const char LAS* wph = wp + 7 * SLOT;
      asm volatile("" : "+v"(wph));
      auto wptr = [&](int n) {
        const int sl = slot2(ord_ks(n));
        return (sl < 7 ? wp + sl * SLOT : wph + (sl - 7) * SLOT) + ord_t(n) * 32;
      };
#pragma unroll
      for (int n = 0; n < WD; ++n) { wa[n] = tr_read(wptr(n)); wb[n] = tr_read(wptr(n) + 16 * 288); }
      u32x4 xf[XP + 1][2];
      auto hload = [&](int k, int i) {
        const int r = k % 9, P = ((idx >> 1) + k / 9) & 3;
        const int so = r < 8 ? (2 * P + (((r >> 2) ^ idx) & 1)) * HB + pr * 8192 + i * 4096 : (2 * P + i) * HB + 65536 + pr * 1024;
        xf[k % (XP + 1)][i] = ld128(rh, lane * 16 + (r < 8 ? (r & 3) * 1024 : 0), so);
      };
      waitv<0>();
      LSTAMP(7);
      const int flh = FL_H + cl * 8;
      if (lane == 0 && lds_add(sm + LM_CNT + 8) == 3u) flag_set(ws, flh + (idx & 7), tag);
      lds_wait4(sm + LM_CNT + 8);
#pragma unroll
      for (int k = 0; k < 4; ++k) { hload(k, 0); hload(k, 1); }
      flag_wait(ws, flh, 8, tag, lane);
      LSTAMP(8);
      RSTAMP(57);
#pragma unroll
      for (int k = 4; k < X0; ++k) { hload(k, 0); hload(k, 1); }
      SB;
      Epi epi;
      const int ft = wid == s ? 8 : 4 + ((wid - s - 1) & 3);
      auto flush = [&](auto OWN, int tt) {
        if (decltype(OWN)::value) {
          *(f32x4 LAS*)(sm + LM_UIM + (2 * tt) * 1024 + lane * 16) = acc[tt][0];
          *(f32x4 LAS*)(sm + LM_UIM + (2 * tt + 1) * 1024 + lane * 16) = acc[tt][1];
          asm volatile("" ::: "memory");
          if (tt == 7) *(volatile u32 LAS*)(sm + LM_CNT + 24) = 8u;
        } else {
          const u32x4 v = {pk(acc[tt][0][0], acc[tt][0][1]), pk(acc[tt][0][2], acc[tt][0][3]),
                           pk(acc[tt][1][0], acc[tt][1][1]), pk(acc[tt][1][2], acc[tt][1][3])};
          st128(v, rs, lane * 16, (wid * 4 + s) * SLAB_B + tt * 1024);
        }
      };
      auto step = [&](auto N, auto OWN) {
        constexpr int n = decltype(N)::value;
        constexpr int ks = ord_ks(n), t = ord_t(n);
        if (ks % 9 == 8 && t == 0) {
          const u32x4 a = xf[ks % (XP + 1)][0], bb = xf[ks % (XP + 1)][1];
          xf[ks % (XP + 1)][0] = u32x4{a[0], a[1], bb[0], bb[1]};
          xf[ks % (XP + 1)][1] = u32x4{a[2], a[3], bb[2], bb[3]};
        }
        const u32x4 w = {wa[n % WR][0], wa[n % WR][1], wb[n % WR][0], wb[n % WR][1]};
        acc[t][0] = mfma16(w, xf[ks % (XP + 1)][0], acc[t][0]);
        SB;
        if ((n + WD) % 36 == 18 && n + WD >= 54 && n + WD <= 270) bar();
        if (n + WD < 324) wa[(n + WD) % WR] = tr_read(wptr(n + WD));
        SB;
        acc[t][1] = mfma16(w, xf[ks % (XP + 1)][1], acc[t][1]);
        SB;
        if (n + WD < 324) wb[(n + WD) % WR] = tr_read(wptr(n + WD) + 16 * 288);
        if (n < 9 * (36 - TAIL) && (t & 1) && h_upto(ks - 1) + 1 + (t >> 2) <= h_upto(ks)) hload(h_upto(ks - 1) + 1 + (t >> 2), (t >> 1) & 1);
        if (ks == 30 && t == 2) load_base(epi, ft);
        if (ks == 37 - TAIL && t > 0 && n >= 9 * (36 - TAIL)) flush(OWN, t > 0 ? t - 1 : 0);
        SB;
      };
      static_for<0, 9 * (36 - TAIL) + TAIL>([&](auto N) { step(N, std::false_type{}); });
      if (wid == s) {
        static_for<9 * (36 - TAIL) + TAIL, 324>([&](auto N) { step(N, std::true_type{}); });
        LSTAMP(9);
        RSTAMP(58);
        asm volatile("s_nop 15");
        finish(std::integral_constant<int, 1>{}, &epi, &ft, &acc[8]);
      } else {
        static_for<9 * (36 - TAIL) + TAIL, 324>([&](auto N) { step(N, std::false_type{}); });
        flush(std::false_type{}, 8);
        LSTAMP(9);
        RSTAMP(58);
        waitv<0>();
        if (lane == 0 && lds_add(sm + LM_CNT + 28) == 2u) flag_set(ws, FL_X + tile * 4 + s, tag);
        LSTAMP(52);
        finish_lds(std::integral_constant<int, 1>{}, &epi, &ft);
      }
    }
  } else {
    const bool conv = idx < 18;
    const int Q = xg * 18 + idx, p = wid - 4, n0 = (p & 1) * 5;
    glds4(pc0, umin((idx + 32 * (8 * p + (lane & 7))) * 128, code_len), smb + LM_SINK);
    const int ch = conv ? 4 * Q + p : 0;
    if (conv) {
      const u16* src[6] = {Are, Aim, Bre, Bim, Cre, Cim};
#pragma unroll
      for (int i = 0; i < 6; ++i) glds4(src[i], ch * (NM * 2) + lane * 4, smb + LM_KIM + p * SLOT + i * 256);
    }
    const u32 vdt = ((const u32*)logdt)[ch >> 1] >> (16 * (ch & 1)), vds = ((const u32*)Dskip)[ch >> 1] >> (16 * (ch & 1));
    bar();
    u32 cr[9], cc[9];
#pragma unroll
    for (int n = 0; n < 9; ++n) { const int ci = 64 * n + lane; cr[n] = ci / 18; cc[n] = (ci % 18) * 16; }
    u32 v1[5], v2[9];
#pragma unroll
    for (int n = 0; n < 5; ++n) v1[n] = n0 ? cr[n < 4 ? 5 + n : 8] * (F * 2) + cc[n < 4 ? 5 + n : 8] : cr[n] * (F * 2) + cc[n];
    const char* w1s = (const char*)(w1 + fb * 144);
    auto issue1 = [&](int k) {
      const char* sp = w1s + (size_t)k * 32 * F * 2;
      const u32 dst = smb + (k % 10) * SLOT + n0 * 1024;
#pragma unroll
      for (int n = 0; n < 4; ++n) glds16(sp, v1[n], dst + n * 1024);
      if (n0 == 0) glds16(sp, v1[4], dst + 4096);
    };
#pragma unroll
    for (int k = 0; k < G1_N0 / 2; ++k) issue1(2 * k + (p >> 1));
    LSTAMP(0);
#pragma unroll
    for (int n = 0; n < 9; ++n) v2[n] = cr[n] * (D * 2) + cc[n];
    const u32 v24s = v2[4] + (lane >= 32 ? 128 * D * 2 : 0);
    const char* w2s = (const char*)(w2 + (size_t)1152 * s * D + 144 * dj);
    auto ksrc = [&](int k) {
      const int r = k % 9;
      return w2s + (size_t)(288 * (((idx >> 1) + k / 9) & 3) + (r < 8 ? 144 * (((r >> 2) ^ idx) & 1) + 32 * (r & 3) : 128)) * (D * 2);
    };
    auto issue2 = [&](int k) {
      const char* sp = ksrc(k);
      const u32 dst = smb + slot2(k) * SLOT;
      if (n0 == 0) {
#pragma unroll
        for (int n = 0; n < 4; ++n) glds16(sp, v2[n], dst + n * 1024);
        glds16(sp, k % 9 == 8 ? v24s : v2[4], dst + 4096);
      } else {
#pragma unroll
        for (int n = 5; n < 9; ++n) glds16(sp + (k % 9 == 8 ? 128 * D * 2 : 0), v2[n], dst + n * 1024);
      }
    };
    auto issue_own = [&]() {
      const char* sp = ksrc(10 + p);
#pragma unroll
      for (int n = 0; n < 9; ++n) glds16(sp, v2[n], smb + (10 + p) * SLOT + n * 1024);
    };
    auto batch2 = [&]() {
      waitv<0>();
      if (lane == 0) lds_add(sm + LM_CNT + 12);
#pragma unroll
      for (int k = G1_N0 / 2; k < G1_N1 / 2; ++k) issue1(2 * k + (p >> 1));
    };
    auto landed2 = [&]() {
      waitv<0>();
      if (lane == 0) lds_add(sm + LM_CNT + 16);
    };
    char LAS* kim = sm + LM_KIM + p * SLOT;
    char LAS* uim = sm + LM_UIM + p * UIM_B;
    if (conv) {
      LSTAMP(3);
      waitv<4 * (G1_N0 / 2)>();
      u32 par[2][6];
#pragma unroll
      for (int i = 0; i < 12; ++i) par[i & 1][i >> 1] = *(const u16 LAS*)(kim + i * 128 + lane * 2);
      const float dt = rb(__expf(bl(vdt)));
      f32x4 ka[2][2];
#pragma unroll
      for (int i = 0; i < 4; ++i) ka[i >> 1][i & 1] = f32x4{0.f, 0.f, 0.f, 0.f};
#pragma unroll 1
      for (int h = 0; h < 2; ++h) {
        float pv[6];
#pragma unroll
        for (int i = 0; i < 6; ++i) pv[i] = bl(h ? par[1][i] : par[0][i]);
        const float p2 = dt * pv[0] * 1.44269504088896340736f;
        const float q = dt * pv[1];
        constexpr float CH = 0.15915494309189535f;
        constexpr float CL = (float)(0.15915494309189533577 - (double)CH);
        const float qh = q * CH;
        const float ql = __builtin_fmaf(q, CL, __builtin_fmaf(q, CH, -qh));
        asm volatile("" ::"v"(ql));
        LSTAMP(50 + h);
        {
          f32x2 Z[16];
          auto put = [&](int i, f32x2 v) { *(u32 LAS*)(kim + i * KG_S + lane * 4) = pk(v[0], v[1]); };
          Z[0] = f32x2{1.f, 0.f};
          Z[1] = zpow2<3>(p2, qh, ql);
          Z[4] = zpow2<5>(p2, qh, ql);
          const f32x2 a7 = zpow2<7>(p2, qh, ql);
          Z[2] = cmul(Z[1], Z[1]);
          Z[8] = cmul(Z[4], Z[4]);
          Z[3] = cmul(Z[2], Z[1]);
#pragma unroll
          for (int i = 1; i < 4; ++i) Z[4 + i] = cmul(Z[4], Z[i]);
#pragma unroll
          for (int i = 1; i < 8; ++i) Z[8 + i] = cmul(Z[8], Z[i]);
#pragma unroll
          for (int i = 0; i < 16; ++i) put(i, Z[i]);
          put(16, a7);
#pragma unroll
          for (int i = 1; i < 16; ++i) put(16 + i, cmul(a7, Z[i]));
        }
        if (h == 1) landed2();
        {
          f32x2 H[16];
          auto put = [&](int i, f32x2 v) { *(u32 LAS*)(uim + i * KG_S + lane * 4) = pk(v[0], -v[1]); };
          H[0] = cmul(f32x2{pv[4], pv[5]}, f32x2{fmul(dt, pv[2]), fmul(dt, pv[3])});
          const f32x2 a0 = zpow2<0>(p2, qh, ql), a8 = zpow2<8>(p2, qh, ql), a10 = zpow2<10>(p2, qh, ql);
          const f32x2 a1 = cmul(a0, a0), a2 = cmul(a1, a1), a9 = cmul(a8, a8), a11 = cmul(a10, a10);
          H[1] = cmul(a0, H[0]);
          H[2] = cmul(a1, H[0]);
          H[3] = cmul(a1, H[1]);
#pragma unroll
          for (int i = 0; i < 4; ++i) H[4 + i] = cmul(a2, H[i]);
#pragma unroll
          for (int i = 0; i < 4; ++i) H[8 + i] = cmul(a8, H[2 * i]);
          H[12] = cmul(a9, H[0]);
          H[13] = cmul(a9, H[4]);
          H[14] = cmul(a9, H[8]);
          H[15] = cmul(a9, H[10]);
#pragma unroll
          for (int i = 0; i < 16; ++i) put(i, H[i]);
          put(16, cmul(a10, H[0]));
          put(17, cmul(a10, H[8]));
          put(18, cmul(a10, H[12]));
          put(19, cmul(a10, H[14]));
          put(20, cmul(a11, H[0]));
        }
        lds_wait4(sm + LM_CNT);
#pragma unroll
        for (int k = 0; k < 4; ++k) {
          u32x4 lo[2], hi[2];
#pragma unroll
          for (int i = 0; i < 2; ++i) {
            lo[i] = *(const u32x4 LAS*)(kim + (16 * i + c) * KG_S + k * 64 + g * 16);
            hi[i] = *(const u32x4 LAS*)(uim + min(16 * i + c, 20) * KG_S + k * 64 + g * 16);
          }
#pragma unroll
          for (int i = 0; i < 4; ++i) ka[i >> 1][i & 1] = mfma16(lo[i & 1], hi[i >> 1], ka[i >> 1][i & 1]);
        }
        if (h == 0) batch2();
      }
      LSTAMP(1);
#pragma unroll
      for (int i = 0; i < 4; ++i)
        *(u32x2 LAS*)(uim + (16 * (i >> 1) + c) * 64 + (i & 1) * 32 + g * 8) =
            u32x2{pk(ka[i >> 1][i & 1][0], ka[i >> 1][i & 1][1]), pk(ka[i >> 1][i & 1][2], ka[i >> 1][i & 1][3])};
      waitl<0>();
    } else {
      if (L2PF & 1) {
#pragma unroll
        for (int i = 0; i < 2; ++i) {
          const int ln = min(((idx - 18) * 8 + 2 * p + i) * 42 + min(lane, 41), 256 * 18 - 1);
          glds4(w1, ((320 + ln / 18) * F + 1152 * s + (ln % 18) * 64) * 2, smb + LM_SINK);
        }
      }
      batch2();
      landed2();
    }
    LSTAMP(2);
    if (p < 2) {
      static_for<0, G1_NB>([&](auto K) {
        constexpr int i = decltype(K)::value;
        constexpr int prev = i ? G1_UPTO[i ? i - 1 : 0] : G1_N1 - 1, need = G1_POS[i + 1] - 1;
        static_assert(G1_UPTO[i] - 10 < G1_POS[i] - 1 && prev >= need, "ring schedule");
        if (need >= G1_N1) { if (p == 0) waitv<5 * (prev - need)>(); else waitv<4 * (prev - need)>(); }
        LSTAMP(4 + 2 * i);
        bar();
        LSTAMP(5 + 2 * i);
#pragma unroll
        for (int u = prev + 1; u <= G1_UPTO[i]; ++u) { if (u < 18) issue1(u); else issue2(u - 18); }
      });
      tag = *(const u64 LAS*)(sm + LM_TAG);
    } else {
      const int q = p - 2;
      if (conv) {
        bar();
        tag = *(const u64 LAS*)(sm + LM_TAG);
        u64 fl[2];
#pragma unroll
        for (int j = 0; j < 2; ++j)
          fl[j] = __hip_atomic_load((GAS u64*)(ws + FL_LN2 * 128) + q * 128 + j * 64 + lane, __ATOMIC_RELAXED, __HIP_MEMORY_SCOPE_AGENT);
        u32x4 uv[8];
        auto uload = [&](int e) {
#pragma unroll
          for (int i = e; i < e + 2; ++i)
            uv[i] = ld128(rsrc(ws + WS_UT, L * D * 2), (128 * q + (lane >> 2)) * 9216 + Q * 64 + (lane & 3) * 16, i * 16 * 9216);
        };
#pragma unroll
        for (int j = 0; j < 2; ++j) {
          const char LAS* tb = sm + LM_UIM + (2 * q + j) * UIM_B;
          char LAS* ki = sm + LM_KIM + (2 * q + j) * SLOT;
          char LAS* ko = ki + (28 + lane) * 16;
          auto put = [&](int mi, const u32(&kv)[9]) {
            *(u32x4 LAS*)(ko + mi * 1024) = u32x4{kv[1] | (kv[2] << 16), kv[3] | (kv[4] << 16), kv[5] | (kv[6] << 16), kv[7] | (kv[8] << 16)};
            *(u32x4 LAS*)(ko + mi * 1024 + KIM_C1) = u32x4{kv[0] | (kv[1] << 16), kv[2] | (kv[3] << 16), kv[4] | (kv[5] << 16), kv[6] | (kv[7] << 16)};
          };
          {
            u32 kv[9];
#pragma unroll
            for (int e = 0; e < 9; ++e) {
              constexpr int EX[9] = {448, 384, 320, 256, 192, 128, 64, 0, 446}, R2[9] = {450, 640, 576, 576, 576, 512, 448, 448, 448};
              int o = 2 * lane + (lane < (e == 8 ? 33 : 32) ? EX[e] : R2[e]);
              if (e == 0 && lane == 63) o = 768;
              kv[e] = *(const u16 LAS*)(tb + o);
            }
            if (lane == 0) kv[8] = 0u;
            put(0, kv);
          }
          bar();
          if (j == 0) {
            if (!__all(fl[0] == tag && fl[1] == tag)) {
              flag_wait(ws, FL_LN + q * 128, 64, tag, lane);
              flag_wait(ws, FL_LN + q * 128 + 64, 64, tag, lane);
            }
            asm volatile("" ::: "memory");
          }
          uload(4 * j);
          if (lane < 56) *(u32x4 LAS*)(ki + (lane & 1) * KIM_C1 + (lane >> 1) * 16) = u32x4{0u, 0u, 0u, 0u};
          {
            const int oa = 768 + 128 * (lane >> 5) + 2 * (lane & 31), oe = 768 + 128 * ((lane + 1) >> 5) + 2 * ((lane + 1) & 31);
            const u32 a = *(const u16 LAS*)(tb + oa), bb = *(const u16 LAS*)(tb + oa + 64), e = *(const u16 LAS*)(tb + oe);
            const u32 kv[9] = {e, e, bb, bb, bb, a, a, a, a};
            put(1, kv);
          }
#pragma unroll
          for (int mi = 2; mi < 4; ++mi) {
            const u32 a = *(const u16 LAS*)(tb + 768 + 128 * mi + 2 * lane), e = *(const u16 LAS*)(tb + 770 + 128 * mi + 2 * lane);
            const u32 tie = (lane & 1) ? e : a;
            const u32 kv[9] = {e, e, e, tie, a, a, a, a, a};
            put(mi, kv);
          }
          waitl<0>();
          bar();
          if (j == 0) uload(2);
          if (j == 0) uload(6);
        }
#pragma unroll
        for (int e = 0; e < 8; ++e) *(u32x4 LAS*)(sm + LM_UIM + (lane & 3) * UIM_B + 64 + (128 * q + 16 * e + (lane >> 2)) * 16) = uv[e];
        if (lane < 16) *(u32x4 LAS*)(sm + LM_UIM + (lane >> 2) * UIM_B + (lane & 3) * 16) = u32x4{0u, 0u, 0u, 0u};
        waitl<0>();
        bar();
      } else {
        const int wv = (idx - 18) * 2 + q;
#pragma unroll
        for (int i = 0; i < 2 * G1_NB; ++i) {
          if (L2PF & 2) glds4(w2 + (size_t)1152 * s * D, min((wv * 12 + i) * 32 + (lane & 31), 1152 * 9 - 1) * 128, smb + LM_SINK);
          if (i & 1) bar(); else __builtin_amdgcn_s_sleep(9);
        }
        tag = *(const u64 LAS*)(sm + LM_TAG);
      }
    }
    bar();
    LSTAMP(22);
#pragma unroll
    for (int u = G1_UPTO[G1_NB - 1] + 1; u < 28; ++u)
      if ((u & 1) == (p >> 1)) issue2(u - 18);
    if (!conv) {
      waitv<0>();
      bar();
      LSTAMP(23);
      issue_own();
    } else {
      const float dsk = bl(vds);
      const char LAS* kp = kim + (c & 1) * KIM_C1 + (4 * (c >> 1) + g) * 16;
      char LAS* ub = uim + 64;
      const char LAS* up[4];
#pragma unroll
      for (int i = 0; i < 4; ++i) {
        const char LAS* q = ub + 2 * (8 * ((c >> 2) - g) + 2 * (c & 3) - 6) + i * 1024;
        asm volatile("" : "+v"(q));
        up[i] = q + 448;
      }
      char LAS* yp = ub + 2 * (8 * (g + 4 * (c >> 1)) + 4 * (c & 1));
      u32x2 sk[8];
#pragma unroll
      for (int T = 0; T < 8; ++T) {
        const u32x2 uu = *(const u32x2 LAS*)(yp + T * 512);
        sk[T] = u32x2{pk(fmul(bl(uu[0]), dsk), fmul(bh(uu[0]), dsk)), pk(fmul(bl(uu[1]), dsk), fmul(bh(uu[1]), dsk))};
        asm volatile("" : "+v"(sk[T]));
      }
      f32x4 acc[8];
#pragma unroll
      for (int T = 0; T < 8; ++T) { acc[T] = f32x4{0.f, 0.f, 0.f, 0.f}; asm volatile("" : "+v"(acc[T])); }
      u32x4 kf[2][8], uf[2][8];
      auto rdk = [&](int be, int a) { kf[be & 1][a] = *(const u32x4 LAS*)(kp + be * 64 + a * 512); };
      auto rdu = [&](int be, int a) {
        const u32 LAS* q = (const u32 LAS*)(up[a >> 1] - be * 64 + (a & 1) * 512);
        uf[be & 1][a] = u32x4{q[0], q[1], q[2], q[3]};
      };
#pragma unroll
      for (int a = 0; a < 8; ++a) { rdk(0, a); rdu(0, a); }
      waitv<0>(); waitl<0>();
      __builtin_amdgcn_s_setprio(3);
      SB;
      bar();
      SB;
      LSTAMP(23);
      static_for<0, 288>([&](auto N) {
        constexpr int n = decltype(N)::value, be = n / 36, m = n % 36;
        constexpr int a = m < 8 ? 0 : m < 15 ? 1 : m < 21 ? 2 : m < 26 ? 3 : m < 30 ? 4 : m < 33 ? 5 : m < 35 ? 6 : 7;
        constexpr int T = a + m - (8 * a - a * (a - 1) / 2);
        acc[T] = mfma16(uf[be & 1][T - a], kf[be & 1][a], acc[T]);
        SB;
        if (be < 7 && m < 16 && (m & 1) == 0) rdu(be + 1, m >> 1);
        if (be < 7 && m >= 16 && m < 24) rdk(be + 1, (m - 16) & 7);
        if (n == 251) { waitl<0>(); issue_own(); }
        SB;
      });
      const bool odd = c & 1;
#pragma unroll
      for (int T = 0; T < 8; ++T) {
        const u32 p01 = pk(acc[T][0], acc[T][1]), p23 = pk(acc[T][2], acc[T][3]);
        const u32 own = odd ? p23 : p01;
        const u32 rcv = (u32)__builtin_amdgcn_mov_dpp((int)(odd ? p01 : p23), 0xB1, 0xF, 0xF, true);
        const u32 ev = odd ? rcv : own, od = odd ? own : rcv;
        *(u32x2 LAS*)(yp + T * 512) = u32x2{pk(fadd(bl(ev), bl(sk[T][0])), fadd(bl(od), bh(sk[T][0]))),
                                            pk(fadd(bh(ev), bl(sk[T][1])), fadd(bh(od), bh(sk[T][1])))};
      }
      waitl<0>();
      __builtin_amdgcn_s_setprio(0);
    }
    LSTAMP(24);
    u64 fa = 0;
    static_for<1, 8>([&](auto K) {
      constexpr int st = 2 * decltype(K)::value;
      constexpr int yng = st == 14 ? 0 : g2_upto(st - 2) - (st + 2);
      if (st == 14 && wid == 7 && !__all(fa == tag)) flag_wait(ws, FL_A + dj * 36, 36, tag, lane);
      if (st == 6 && conv) waitv<0>();
      if (st > 2) { if (n0 == 0) waitv<5 * yng>(); else waitv<4 * yng>(); }
      LSTAMP(24 + st);
      bar();
      LSTAMP(25 + st);
      if (conv && st == 4) {
        const __amdgpu_buffer_rsrc_t ao = rsrc(ws + WS_A2 + (size_t)Q * 16384 + 4096 * p, 4096);
        const char LAS* tp = sm + LM_UIM + (c >> 2) * UIM_B + 64 + 2 * (512 * p + 32 * g) + (c & 3) * 8;
#pragma unroll
        for (int i = 0; i < 4; ++i) {
          const u32x2 a = tr_read(tp + i * 256), bb = tr_read(tp + i * 256 + 32);
          st128(u32x4{a[0], a[1], bb[0], bb[1]}, ao, lane * 16, i * 1024);
        }
      }
      if (conv && st == 6 && tid == 256) flag_set(ws, FL_A + Q, tag);
#pragma unroll
      for (int j = g2_upto(st - 2) + 1; j <= g2_upto(st); ++j) {
        if (p < 2) issue2(2 * j); else issue2(2 * j + 1);
      }
      if (st == 12 && wid == 7) fa = __hip_atomic_load((GAS u64*)(ws + (FL_A + dj * 36 + min(lane, 35)) * 128), __ATOMIC_RELAXED, __HIP_MEMORY_SCOPE_AGENT);
    });
    if (tid == 256 && b == 0) flag_set(ws, FL_EPOCH, tag);
    Epi epi[2];
    load_base(epi[0], p);
    if (p == 0) load_base(epi[1], 7);
    const int ft[2] = {p, 7};
    if (p == 0) finish_lds(std::integral_constant<int, 2>{}, epi, ft);
    asm volatile(".Lk_all_tail:");
    if (p != 0) finish_lds(std::integral_constant<int, 1>{}, epi, ft);
  }
  LSTAMP(54);
  RSTAMP(59);
}

extern "C" void kernel_launch(void* const* d_in, const int* in_sizes, int n_in, void* d_out, int out_size, void* d_ws,
                              size_t ws_size, hipStream_t stream) {
  const u16* const* in = (const u16* const*)d_in;
  (void)hipFuncSetAttribute((const void*)k_all, hipFuncAttributeMaxDynamicSharedMemorySize, LM_END);
  k_all<<<256, 512, LM_END, stream>>>(in[0], in[1], in[2], in[3], in[4], in[5], in[6], in[7], in[8], in[10], in[11], in[12], in[13],
                                      (char*)d_ws, (u16*)d_out);
}
```

```cpp
#define PADMIN 1
#include <hip/hip_runtime.h>
#include <hip/hip_bf16.h>
#include <stdint.h>
#include <type_traits>

typedef unsigned short u16;
typedef unsigned int u32;
typedef unsigned long long u64;
typedef __bf16 bf16x8 __attribute__((ext_vector_type(8)));
typedef __bf16 bf16x2 __attribute__((ext_vector_type(2)));
typedef short s16x4 __attribute__((ext_vector_type(4)));
typedef float f32x4 __attribute__((ext_vector_type(4)));
typedef float f32x2 __attribute__((ext_vector_type(2)));
typedef u32 u32x4 __attribute__((ext_vector_type(4)));
typedef u32 u32x2 __attribute__((ext_vector_type(2)));
#define LAS __attribute__((address_space(3)))
#define GAS __attribute__((address_space(1)))
#define POLL_SLEEP 4
#define SB __builtin_amdgcn_sched_barrier(0)

constexpr int L = 2048, D = 576, NM = 128, F = 4608;

constexpr int FL_EPOCH = 0;
constexpr int FL_LN = 1;
constexpr int FL_A = FL_LN + 256;
constexpr int FL_H = FL_A + 144;
constexpr int FL_X = FL_H + 256;
constexpr int FL_LN2 = FL_X + 256;
constexpr size_t WS_XNF = 131072;
constexpr size_t WS_UT = WS_XNF + (size_t)L * D * 2;
constexpr size_t WS_A2 = WS_UT + (size_t)L * D * 2;
constexpr size_t WS_HF = WS_A2 + (size_t)L * D * 2;
constexpr size_t WS_SLAB = WS_HF + (size_t)L * F * 2;
constexpr int SLAB_B = 9216, HB = 73728;
constexpr size_t WS_END = WS_SLAB + (size_t)64 * 16 * SLAB_B;
#define LSTAMP(i)
#define RSTAMP(i)

__device__ __forceinline__ float bl(u32 v) { return __uint_as_float(v << 16); }
__device__ __forceinline__ float bh(u32 v) { return __uint_as_float(v & 0xffff0000u); }
__device__ __forceinline__ float b2f(u16 v) { return __uint_as_float(((u32)v) << 16); }
__device__ __forceinline__ u32 pk(float lo, float hi) {
  bf16x2 r = {(__bf16)lo, (__bf16)hi};
  return __builtin_bit_cast(u32, r);
}
__device__ __forceinline__ float fadd(float a, float b) {
  float r;
  asm("v_add_f32 %0, %1, %2" : "=v"(r) : "v"(a), "v"(b));
  return r;
}
__device__ __forceinline__ u32 add2(u32 a, u32 b) {
  u32 r, t0, t1;
  asm("v_lshlrev_b32 %0, 16, %3\n\tv_lshlrev_b32 %1, 16, %4\n\tv_add_f32 %0, %0, %1\n\tv_and_b32 %1, 0xffff0000, %3\n\t"
      "v_and_b32 %2, 0xffff0000, %4\n\tv_add_f32 %1, %1, %2\n\tv_cvt_pk_bf16_f32 %0, %0, %1"
      : "=&v"(r), "=&v"(t0), "=&v"(t1) : "v"(a), "v"(b));
  return r;
}
__device__ __forceinline__ u32 sum4(float lo, float hi, u32 a, u32 b, u32 c) {
  u32 r, t0, t1;
  asm("v_lshlrev_b32 %0, 16, %5\n\tv_and_b32 %1, 0xffff0000, %5\n\tv_add_f32 %0, %3, %0\n\tv_add_f32 %1, %4, %1\n\t"
      "v_lshlrev_b32 %2, 16, %6\n\tv_add_f32 %0, %0, %2\n\tv_and_b32 %2, 0xffff0000, %6\n\tv_add_f32 %1, %1, %2\n\t"
      "v_lshlrev_b32 %2, 16, %7\n\tv_add_f32 %0, %0, %2\n\tv_and_b32 %2, 0xffff0000, %7\n\tv_add_f32 %1, %1, %2\n\t"
      "v_cvt_pk_bf16_f32 %0, %0, %1"
      : "=&v"(r), "=&v"(t0), "=&v"(t1) : "v"(lo), "v"(hi), "v"(a), "v"(b), "v"(c));
  return r;
}
__device__ __forceinline__ float rb(float f) { return (float)(__bf16)f; }
__device__ __forceinline__ f32x4 mfma16(u32x4 a, u32x4 b, f32x4 c) {
  return __builtin_amdgcn_mfma_f32_16x16x32_bf16(__builtin_bit_cast(bf16x8, a), __builtin_bit_cast(bf16x8, b), c, 0, 0, 0);
}
__device__ __forceinline__ u32x2 tr_read(const char LAS* p) {
  return __builtin_bit_cast(u32x2, __builtin_amdgcn_ds_read_tr16_b64_v4i16((s16x4 LAS*)p));
}
__device__ __forceinline__ __amdgpu_buffer_rsrc_t rsrc(const void* p, int bytes) {
  return __builtin_amdgcn_make_buffer_rsrc((void*)p, 0, bytes, 0x00020000);
}
constexpr int SC1 = 16;
__device__ __forceinline__ u32 umin(u32 a, u32 b) { return a < b ? a : b; }

__device__ __forceinline__ void st128(u32x4 v, __amdgpu_buffer_rsrc_t r, int voff, int soff) {
  __builtin_amdgcn_raw_buffer_store_b128(v, r, voff, soff, SC1);
  asm volatile("s_nop 1" ::"v"(v) : "memory");
}
__device__ __forceinline__ u32x4 ld128(__amdgpu_buffer_rsrc_t r, int voff, int soff) {
  return __builtin_amdgcn_raw_buffer_load_b128(r, voff, soff, SC1);
}
__device__ __forceinline__ void glds16(const char* base, u32 voff, u32 dst) {
  asm volatile("s_mov_b32 m0, %2\n\ts_nop 0\n\tglobal_load_lds_dwordx4 %0, %1" ::"v"(voff), "s"(base), "s"(dst) : "memory");
}
__device__ __forceinline__ void glds4(const void* base, u32 voff, u32 dst) {
  asm volatile("s_mov_b32 m0, %2\n\ts_nop 0\n\tglobal_load_lds_dword %0, %1" ::"v"(voff), "s"(base), "s"(dst) : "memory");
}
template <int N> __device__ __forceinline__ void waitv() { asm volatile("s_waitcnt vmcnt(%0)" ::"i"(N) : "memory"); }
template <int N> __device__ __forceinline__ void waitl() { asm volatile("s_waitcnt lgkmcnt(%0)" ::"i"(N) : "memory"); }
__device__ __forceinline__ void bar() {
  asm volatile("" ::: "memory");
  __builtin_amdgcn_s_barrier();
  asm volatile("" ::: "memory");
}
template <int N> __device__ __forceinline__ void bars() {
#pragma unroll
  for (int i = 0; i < N; ++i) bar();
}
template <int I, int N, class Fn> __device__ __forceinline__ void static_for(Fn&& fn) {
  if constexpr (I < N) {
    fn(std::integral_constant<int, I>{});
    static_for<I + 1, N>(fn);
  }
}
__device__ __forceinline__ void flag_set(char* ws, int off, u64 tag) {
  __hip_atomic_store((GAS u64*)(ws + off * 128), tag, __ATOMIC_RELAXED, __HIP_MEMORY_SCOPE_AGENT);
}
__device__ __forceinline__ void flag_wait(char* ws, int off, int n, u64 tag, int lane) {
  GAS u64* p = (GAS u64*)(ws + (off + min(lane, n - 1)) * 128);
  u32 zero = 0;
  asm volatile("" : "+v"(zero));
#pragma unroll 1
  for (u32 spin = 0; spin < (1u << 20); ++spin) {
    if (__all(__hip_atomic_load(p, __ATOMIC_RELAXED, __HIP_MEMORY_SCOPE_AGENT) == tag)) break;
    __builtin_amdgcn_s_sleep(POLL_SLEEP);
  }
  asm volatile("" ::: "memory");
}
__device__ __forceinline__ u32 lds_add(char LAS* p) {
  return __hip_atomic_fetch_add((u32 LAS*)p, 1u, __ATOMIC_RELAXED, __HIP_MEMORY_SCOPE_WORKGROUP);
}
__device__ __forceinline__ void lds_wait4(char LAS* p) {
  u32 v, sv;
  asm volatile(
      "1:\n\tds_read_b32 %0, %2\n\ts_waitcnt lgkmcnt(0)\n\tv_readfirstlane_b32 %1, %0\n\ts_cmp_lt_u32 %1, 4\n\ts_cbranch_scc0 2f\n\t"
      "s_sleep 1\n\ts_branch 1b\n2:"
      : "=&v"(v), "=&s"(sv) : "v"(p) : "memory", "scc");
}

__device__ __forceinline__ float fmul(float a, float b) {
  float r;
  asm("v_mul_f32 %0, %1, %2" : "=v"(r) : "v"(a), "v"(b));
  return r;
}
__device__ __forceinline__ f32x2 cmul(f32x2 a, f32x2 b) {
  float re, im;
  asm("v_mul_f32 %0, %2, %4\n\tv_mul_f32 %1, %2, %5\n\tv_fma_f32 %0, -%3, %5, %0\n\tv_fma_f32 %1, %3, %4, %1"
      : "=&v"(re), "=&v"(im) : "v"(a[0]), "v"(a[1]), "v"(b[0]), "v"(b[1]));
  return f32x2{re, im};
}
template <int E> __device__ __forceinline__ f32x2 zpow2(float p2, float qh, float ql) {
  constexpr float s = (float)(1 << E);
  const float mag = __builtin_amdgcn_exp2f(p2 * s);
  const float ph = __builtin_fmaf(ql, s, __builtin_amdgcn_fractf(qh * s));
  float re, im;
  asm("s_nop 1\n\tv_mul_f32 %0, %2, %4\n\tv_mul_f32 %1, %3, %4"
      : "=&v"(re), "=&v"(im) : "v"(__builtin_amdgcn_cosf(ph)), "v"(__builtin_amdgcn_sinf(ph)), "v"(mag));
  return f32x2{re, im};
}
template <int CTRL> __device__ __forceinline__ float dpp_add(float v) {
  return v + __builtin_bit_cast(float, __builtin_amdgcn_update_dpp(0, __builtin_bit_cast(int, v), CTRL, 0xF, 0xF, true));
}
__device__ __forceinline__ float sum32(float v) {
  v = dpp_add<0x128>(v);
  v = dpp_add<0x124>(v);
  v = dpp_add<0x122>(v);
  v = dpp_add<0x121>(v);
  return v + __builtin_bit_cast(float, __builtin_amdgcn_ds_swizzle(__builtin_bit_cast(int, v), 0x401F));
}
__device__ __forceinline__ u32 gelu2(float a, float b) {
  constexpr float C0 = -2.f * 1.44269504088896340736f * 0.7978845608028654f, C1 = C0 * 0.044715f;
  const f32x2 v = {a, b};
  const f32x2 z = v * __builtin_elementwise_fma(v * v, f32x2{C1, C1}, f32x2{C0, C0});
  const f32x2 d = f32x2{__builtin_amdgcn_exp2f(z[0]), __builtin_amdgcn_exp2f(z[1])} + f32x2{1.f, 1.f};
  const f32x2 r = v * f32x2{__builtin_amdgcn_rcpf(d[0]), __builtin_amdgcn_rcpf(d[1])};
  return pk(r[0], r[1]);
}
__device__ __forceinline__ u32x4 gelu8(f32x4 a, f32x4 b) {
  return u32x4{gelu2(a[0], a[1]), gelu2(a[2], a[3]), gelu2(b[0], b[1]), gelu2(b[2], b[3])};
}

constexpr int SLOT = 9216;
constexpr int KIM_C1 = 4640, UIM_B = 5952, KG_S = 272, LN_S = 1168;
constexpr int LM_KIM = 10 * SLOT;
constexpr int LM_UIM = 14 * SLOT;
constexpr int LM_LN = LM_UIM + 4 * UIM_B;
constexpr int LM_CNT = LM_LN + 8 * LN_S;
constexpr int LM_TAG = LM_CNT + 32;
constexpr int LM_SINK = LM_TAG + 16;
constexpr int LM_END = LM_SINK + 256;
static_assert(LM_END <= 163840 && 4 * UIM_B >= 18432 && 21 * KG_S <= UIM_B && 32 * KG_S <= SLOT && UIM_B % 256 == 64, "LDS map");
constexpr int slot2(int j) { return j % 14 < 10 ? (j % 14 + 8) % 10 : j % 14; }

#define G2_XP 15
#define G2_NUM 2
#define G2_DEN 3
#define G2_X0 8
#define L2PF 3
constexpr int G1_N0 = 4, G1_N1 = 10, G1_NB = 6;
constexpr int G1_POS[G1_NB + 1] = {6, 8, 10, 12, 14, 16, 18};
constexpr int G1_UPTO[G1_NB] = {13, 16, 18, 20, 22, 24};
constexpr bool g1_bar_before(int ks) {
  for (int i = 0; i < G1_NB; ++i)
    if (G1_POS[i] == ks) return true;
  return false;
}
constexpr int g2_upto(int st) { return st < 2 ? 6 : st + 6 < 17 ? st + 6 : 17; }

constexpr int h_upto(int ks) {
  const int kn = G2_XP - G2_X0 - 1;
  const int v = ks <= kn ? G2_X0 + 1 + 2 * ks : G2_X0 + 1 + 2 * kn + (ks - kn) * G2_NUM / G2_DEN;
  return v < 35 ? v : 35;
}

__global__ __launch_bounds__(512, 2) void k_all(const u16* __restrict__ x, const u16* __restrict__ Are, const u16* __restrict__ Aim,
                                                const u16* __restrict__ Bre, const u16* __restrict__ Bim, const u16* __restrict__ Cre,
                                                const u16* __restrict__ Cim, const u16* __restrict__ logdt, const u16* __restrict__ Dskip,
                                                const u16* __restrict__ w1, const u16* __restrict__ b1, const u16* __restrict__ w2,
                                                const u16* __restrict__ b2, char* ws, u16* __restrict__ out) {
  extern __shared__ __attribute__((aligned(16))) char smem[];
  char LAS* sm = (char LAS*)smem;
  const u32 smb = (u32)(uintptr_t)sm;
  const int tid = threadIdx.x, lane = tid & 63, wid = __builtin_amdgcn_readfirstlane(tid >> 6);
  const int b = blockIdx.x, xg = b & 7, idx = b >> 3, s = xg & 3;
  const int lb = 4 * (xg >> 2) + (idx >> 3), fb = 8 * s + (idx & 7);
  const int l2 = 8 * (xg >> 2) + (idx >> 2), dj = idx & 3, tile = l2 * 4 + dj;
  const int cl = xg * 4 + (idx >> 3);
  const int c = lane & 15, g = lane >> 4;
  const char* pc0;
  u32 code_len;
  asm volatile("s_getpc_b64 %0\n.Lk_all_head:\n\ts_mov_b32 %1, .Lk_all_tail-.Lk_all_head" : "=s"(pc0), "=s"(code_len));
  u64 tag;
  const u64 pad0 = __builtin_amdgcn_s_memrealtime();
  if (tid == 0) __hip_atomic_store((GAS u64*)(ws + WS_END) + b, pad0, __ATOMIC_RELAXED, __HIP_MEMORY_SCOPE_AGENT);
  LSTAMP(63);
  const __amdgpu_buffer_rsrc_t rs = rsrc(ws + WS_SLAB + (size_t)tile * 16 * SLAB_B, 16 * SLAB_B);
  struct Epi { u32x2 xv[2], bias; u32x4 av; };
  auto load_base = [&](Epi& e, int t) {
    const int d = 144 * dj + 16 * t + 4 * g, l = 128 * l2 + 32 * s + c;
    e.bias = *(const u32x2*)(b2 + d);
    e.av = ld128(rsrc(ws + WS_A2, L * D * 2), (d >> 2) * 16384 + (4 * l2 + s) * 256 + c * 16, 0);
    e.xv[0] = *(const u32x2*)(x + (size_t)l * D + d);
    e.xv[1] = *(const u32x2*)(x + (size_t)(l + 16) * D + d);
  };
  auto finish = [&](auto NT, const Epi* e, const int* t, const f32x4 (*own)[2]) {
    constexpr int nt = decltype(NT)::value;
    u32 base[nt][4];
#pragma unroll
    for (int j = 0; j < nt; ++j)
#pragma unroll
      for (int i = 0; i < 4; ++i) base[j][i] = add2(e[j].xv[i >> 1][i & 1], e[j].av[i]);
    flag_wait(ws, FL_X + tile * 4, 4, tag, lane);
    LSTAMP(53);
    u32x4 part[nt][3];
#pragma unroll
    for (int j = 0; j < nt; ++j)
#pragma unroll
      for (int k = 0; k < 3; ++k) part[j][k] = ld128(rs, lane * 16, (s * 4 + ((s + 1 + k) & 3)) * SLAB_B + t[j] * 1024);
#pragma unroll
    for (int j = 0; j < nt; ++j) {
      u16* dst = out + (size_t)(128 * l2 + 32 * s + c) * D + 144 * dj + 16 * t[j] + 4 * g;
#pragma unroll
      for (int i = 0; i < 2; ++i) {
        u32 o[2];
#pragma unroll
        for (int h = 0; h < 2; ++h) {
          const u32 r = sum4(own[j][i][2 * h], own[j][i][2 * h + 1], part[j][0][2 * i + h], part[j][1][2 * i + h], part[j][2][2 * i + h]);
          o[h] = add2(base[j][2 * i + h], add2(r, e[j].bias[h]));
        }
        *(u32x2*)(dst + i * 16 * D) = u32x2{o[0], o[1]};
      }
    }
  };
  auto finish_lds = [&](auto NT, const Epi* e, const int* t) {
    constexpr int nt = decltype(NT)::value;
    while (__builtin_amdgcn_readfirstlane(*(volatile u32 LAS*)(sm + LM_CNT + 24)) < 8u) __builtin_amdgcn_s_sleep(2);
    asm volatile("" ::: "memory");
    f32x4 own[nt][2];
#pragma unroll
    for (int j = 0; j < nt; ++j)
#pragma unroll
      for (int i = 0; i < 2; ++i) own[j][i] = *(const f32x4 LAS*)(sm + LM_UIM + (2 * t[j] + i) * 1024 + lane * 16);
    finish(NT, e, t, own);
  };
  if (__builtin_expect(wid < 4, 1)) {
    __builtin_amdgcn_s_setprio(2);
    u32x2 bq[9];
    {
      const int r = tid >> 5, lam = tid & 31;
      const u32* src = (const u32*)(x + (size_t)(b * 8 + r) * D) + lam;
      u32 raw[9];
#pragma unroll
      for (int i = 0; i < 9; ++i) raw[i] = src[32 * i];
      const u64 ep = __hip_atomic_load((GAS u64*)(ws + FL_EPOCH), __ATOMIC_RELAXED, __HIP_MEMORY_SCOPE_AGENT);
#pragma unroll
      for (int t = 0; t < 9; ++t) bq[t] = *(const u32x2*)(b1 + fb * 144 + 16 * t + 4 * g);
      if (tid < 2) *(u32x4 LAS*)(sm + LM_CNT + tid * 16) = u32x4{0u, 0u, 0u, 0u};
      waitl<0>();
      bar();
      f32x2 v[9];
#pragma unroll
      for (int i = 0; i < 9; ++i) v[i] = f32x2{bl(raw[i]), bh(raw[i])};
      f32x2 s2 = v[0];
#pragma unroll
      for (int i = 1; i < 9; ++i) s2 += v[i];
      const float mean = sum32(s2[0] + s2[1]) * (1.f / D);
      LSTAMP(0);
      f32x2 q2 = {0.f, 0.f};
#pragma unroll
      for (int i = 0; i < 9; ++i) { f32x2 d = v[i] - f32x2{mean, mean}; q2 = __builtin_elementwise_fma(d, d, q2); }
      const float q = sum32(q2[0] + q2[1]);
      const float m = rb(mean);
      const float sd = rb(__builtin_amdgcn_sqrtf(rb(rb(q * (1.f / D)) + 1e-5f)));
      const float rs = __builtin_amdgcn_rcpf(sd);
#pragma unroll
      for (int i = 0; i < 9; ++i) {
        const f32x2 d = v[i] - f32x2{m, m};
        const u32 t = pk(d[0], d[1]);
        const f32x2 n = f32x2{bl(t), bh(t)} * f32x2{rs, rs};
        *(u32 LAS*)(sm + LM_LN + r * LN_S + (lam + 32 * i) * 4) = pk(n[0], n[1]);
      }
      waitl<0>();
      if (lane == 0) lds_add(sm + LM_CNT);
      lds_wait4(sm + LM_CNT);
      LSTAMP(1);
      const __amdgpu_buffer_rsrc_t xo = rsrc(ws + WS_XNF + (size_t)(b >> 1) * 18432 + (b & 1) * 128, 18432);
      const __amdgpu_buffer_rsrc_t uo = rsrc(ws + WS_UT + (size_t)b * 9216, 9216);
      const char LAS* xp = sm + LM_LN + (lane & 7) * LN_S + (lane >> 5) * 64 + ((lane >> 3) & 3) * 8;
      const char LAS* tp = sm + LM_LN + ((lane >> 2) & 3) * LN_S + (lane >> 4) * 32 + (lane & 3) * 8;
      const int xoff = (lane >> 5) * 1024 + ((lane >> 3) & 3) * 256 + (lane & 7) * 16;
#pragma unroll
      for (int kk = 0; kk < 3; ++kk) {
        const int k = wid + 4 * kk;
        if (k < 9) {
          const u32x2 a = *(const u32x2 LAS*)(xp + k * 128), bb = *(const u32x2 LAS*)(xp + k * 128 + 32);
          const u32x2 t0 = tr_read(tp + k * 128), t1 = tr_read(tp + k * 128 + 4 * LN_S);
          st128(u32x4{a[0], a[1], bb[0], bb[1]}, xo, xoff, k * 2048);
          st128(u32x4{t0[0], t0[1], t1[0], t1[1]}, uo, lane * 16, k * 1024);
        }
      }
      tag = (((u64)__builtin_amdgcn_readfirstlane((u32)(ep >> 32)) << 32) | __builtin_amdgcn_readfirstlane((u32)ep)) * 0x9E3779B97F4A7C15ull +
            0xD1B54A32D192ED03ull;
      if (tid == 0) *(u64 LAS*)(sm + LM_TAG) = tag;
    }
    {
      constexpr int XP = 3, WD = 3, WR = 4;
      const int lt0 = 16 * lb + 4 * wid;
      const __amdgpu_buffer_rsrc_t rx = rsrc(ws + WS_XNF + (size_t)lt0 * 18432, 4 * 18432);
      f32x4 acc[9][4];
#pragma unroll
      for (int t = 0; t < 9; ++t) {
#pragma unroll
        for (int i = 0; i < 4; ++i) {
          acc[t][i] = f32x4{bl(bq[t][0]), bh(bq[t][0]), bl(bq[t][1]), bh(bq[t][1])};
          asm volatile("" : "+v"(acc[t][i]));
        }
      }
      waitv<0>(); waitl<0>();
      LSTAMP(2);
      if (lane == 0 && lds_add(sm + LM_CNT + 4) == 3u) {
        flag_set(ws, FL_LN + b, tag);
        flag_set(ws + b * 8, FL_LN2, tag);
      }
      flag_wait(ws, FL_LN + 32 * lb + 8 * wid, 8, tag, lane);
      LSTAMP(3);
      u32x4 xf[XP + 1][4];
#pragma unroll
      for (int k = 0; k < XP; ++k)
#pragma unroll
        for (int i = 0; i < 4; ++i) xf[k][i] = ld128(rx, lane * 16, i * 18432 + k * 1024);
      SB;
      const char LAS* wp = sm + (lane >> 2) * 288 + (lane & 3) * 8;
      u32x2 wa[WR], wb[WR];
      const char LAS* wph = wp + 6 * SLOT;
      asm volatile("" : "+v"(wph));
      auto wptr = [&](int n) {
        const int sl = (n / 9) % 10;
        return (sl < 6 ? wp + sl * SLOT : wph + (sl - 6) * SLOT) + (n % 9) * 32;
      };
      lds_wait4(sm + LM_CNT + 12);
      LSTAMP(4);
      RSTAMP(55);
#pragma unroll
      for (int n = 0; n < WD; ++n) { wa[n] = tr_read(wptr(n)); wb[n] = tr_read(wptr(n) + 16 * 288); }
#pragma unroll
      for (int ks = 0; ks < 18; ++ks) {
#pragma unroll
        for (int t = 0; t < 9; ++t) {
          const int n = 9 * ks + t;
          const u32x4 w = {wa[n % WR][0], wa[n % WR][1], wb[n % WR][0], wb[n % WR][1]};
          acc[t][0] = mfma16(w, xf[ks % (XP + 1)][0], acc[t][0]);
          SB;
          if (t == 9 - WD && g1_bar_before(ks + 1)) bar();
          if (t == 9 - WD && ks + 1 == G1_N0) lds_wait4(sm + LM_CNT + 16);
          if (n == 161) bar();
          if (n + WD < 162) wa[(n + WD) % WR] = tr_read(wptr(n + WD));
          SB;
          acc[t][1] = mfma16(w, xf[ks % (XP + 1)][1], acc[t][1]);
          SB;
          if (n + WD < 162) wb[(n + WD) % WR] = tr_read(wptr(n + WD) + 16 * 288);
          SB;
          acc[t][2] = mfma16(w, xf[ks % (XP + 1)][2], acc[t][2]);
          SB;
          if (t < 4 && ks + XP < 18) xf[(ks + XP) % (XP + 1)][t] = ld128(rx, lane * 16, t * 18432 + (ks + XP) * 1024);
          SB;
          acc[t][3] = mfma16(w, xf[ks % (XP + 1)][3], acc[t][3]);
          SB;
        }
      }
      asm volatile("s_nop 15");
      SB;
      LSTAMP(5);
      RSTAMP(56);
      const __amdgpu_buffer_rsrc_t rh = rsrc(ws + WS_HF + (size_t)(cl * 8 + (idx & 7)) * HB, HB);
#pragma unroll
      for (int i = 0; i < 4; ++i)
#pragma unroll
        for (int r = 0; r < 4; ++r) st128(gelu8(acc[2 * r][i], acc[2 * r + 1][i]), rh, lane * 16, (4 * wid + i) * 4096 + r * 1024);
#pragma unroll
      for (int i = 0; i < 2; ++i) st128(gelu8(acc[8][2 * i], acc[8][2 * i + 1]), rh, lane * 16, 65536 + (2 * wid + i) * 1024);
      LSTAMP(6);
    }
    bar();
    {
      constexpr int XP = G2_XP, X0 = G2_X0, WD = 5, WR = 6, TAIL = 4;
      const __amdgpu_buffer_rsrc_t rh = rsrc(ws + WS_HF + (size_t)cl * 8 * HB, 8 * HB);
      const int pr = 4 * ((idx >> 2) & 1) + wid;
      f32x4 acc[9][2];
#pragma unroll
      for (int t = 0; t < 9; ++t)
#pragma unroll
        for (int i = 0; i < 2; ++i) {
          acc[t][i] = f32x4{0.f, 0.f, 0.f, 0.f};
          asm volatile("" : "+v"(acc[t][i]));
        }
      const char LAS* wp = sm + (lane >> 2) * 288 + (lane & 3) * 8;
      u32x2 wa[WR], wb[WR];
      constexpr auto ord_ks = [](int n) { return n < 9 * (36 - TAIL) ? n / 9 : 36 - TAIL + (n - 9 * (36 - TAIL)) % TAIL; };
      constexpr auto ord_t = [](int n) { return n < 9 * (36 - TAIL) ? n % 9 : (n - 9 * (36 - TAIL)) / TAIL; };
      const char LAS* wph = wp + 7 * SLOT;
      asm volatile("" : "+v"(wph));
      auto wptr = [&](int n) {
        const int sl = slot2(ord_ks(n));
        return (sl < 7 ? wp + sl * SLOT : wph + (sl - 7) * SLOT) + ord_t(n) * 32;
      };
#pragma unroll
      for (int n = 0; n < WD; ++n) { wa[n] = tr_read(wptr(n)); wb[n] = tr_read(wptr(n) + 16 * 288); }
      u32x4 xf[XP + 1][2];
      auto hload = [&](int k, int i) {
        const int r = k % 9, P = ((idx >> 1) + k / 9) & 3;
        const int so = r < 8 ? (2 * P + (((r >> 2) ^ idx) & 1)) * HB + pr * 8192 + i * 4096 : (2 * P + i) * HB + 65536 + pr * 1024;
        xf[k % (XP + 1)][i] = ld128(rh, lane * 16 + (r < 8 ? (r & 3) * 1024 : 0), so);
      };
      waitv<0>();
      LSTAMP(7);
      const int flh = FL_H + cl * 8;
      if (lane == 0 && lds_add(sm + LM_CNT + 8) == 3u) flag_set(ws, flh + (idx & 7), tag);
      lds_wait4(sm + LM_CNT + 8);
#pragma unroll
      for (int k = 0; k < 4; ++k) { hload(k, 0); hload(k, 1); }
      flag_wait(ws, flh, 8, tag, lane);
      LSTAMP(8);
      RSTAMP(57);
#pragma unroll
      for (int k = 4; k < X0; ++k) { hload(k, 0); hload(k, 1); }
      SB;
      Epi epi;
      const int ft = wid == s ? 8 : 4 + ((wid - s - 1) & 3);
      auto flush = [&](auto OWN, int tt) {
        if (decltype(OWN)::value) {
          *(f32x4 LAS*)(sm + LM_UIM + (2 * tt) * 1024 + lane * 16) = acc[tt][0];
          *(f32x4 LAS*)(sm + LM_UIM + (2 * tt + 1) * 1024 + lane * 16) = acc[tt][1];
          asm volatile("" ::: "memory");
          if (tt == 7) *(volatile u32 LAS*)(sm + LM_CNT + 24) = 8u;
        } else {
          const u32x4 v = {pk(acc[tt][0][0], acc[tt][0][1]), pk(acc[tt][0][2], acc[tt][0][3]),
                           pk(acc[tt][1][0], acc[tt][1][1]), pk(acc[tt][1][2], acc[tt][1][3])};
          st128(v, rs, lane * 16, (wid * 4 + s) * SLAB_B + tt * 1024);
        }
      };
      auto step = [&](auto N, auto OWN) {
        constexpr int n = decltype(N)::value;
        constexpr int ks = ord_ks(n), t = ord_t(n);
        if (ks % 9 == 8 && t == 0) {
          const u32x4 a = xf[ks % (XP + 1)][0], bb = xf[ks % (XP + 1)][1];
          xf[ks % (XP + 1)][0] = u32x4{a[0], a[1], bb[0], bb[1]};
          xf[ks % (XP + 1)][1] = u32x4{a[2], a[3], bb[2], bb[3]};
        }
        const u32x4 w = {wa[n % WR][0], wa[n % WR][1], wb[n % WR][0], wb[n % WR][1]};
        acc[t][0] = mfma16(w, xf[ks % (XP + 1)][0], acc[t][0]);
        SB;
        if ((n + WD) % 36 == 18 && n + WD >= 54 && n + WD <= 270) bar();
        if (n + WD < 324) wa[(n + WD) % WR] = tr_read(wptr(n + WD));
        SB;
        acc[t][1] = mfma16(w, xf[ks % (XP + 1)][1], acc[t][1]);
        SB;
        if (n + WD < 324) wb[(n + WD) % WR] = tr_read(wptr(n + WD) + 16 * 288);
        if (n < 9 * (36 - TAIL) && (t & 1) && h_upto(ks - 1) + 1 + (t >> 2) <= h_upto(ks)) hload(h_upto(ks - 1) + 1 + (t >> 2), (t >> 1) & 1);
        if (ks == 30 && t == 2) load_base(epi, ft);
        if (ks == 37 - TAIL && t > 0 && n >= 9 * (36 - TAIL)) flush(OWN, t > 0 ? t - 1 : 0);
        SB;
      };
      static_for<0, 9 * (36 - TAIL) + TAIL>([&](auto N) { step(N, std::false_type{}); });
      if (wid == s) {
        static_for<9 * (36 - TAIL) + TAIL, 324>([&](auto N) { step(N, std::true_type{}); });
        LSTAMP(9);
        RSTAMP(58);
        asm volatile("s_nop 15");
        finish(std::integral_constant<int, 1>{}, &epi, &ft, &acc[8]);
      } else {
        static_for<9 * (36 - TAIL) + TAIL, 324>([&](auto N) { step(N, std::false_type{}); });
        flush(std::false_type{}, 8);
        LSTAMP(9);
        RSTAMP(58);
        waitv<0>();
        if (lane == 0 && lds_add(sm + LM_CNT + 28) == 2u) flag_set(ws, FL_X + tile * 4 + s, tag);
        LSTAMP(52);
        finish_lds(std::integral_constant<int, 1>{}, &epi, &ft);
      }
    }
  } else {
    const bool conv = idx < 18;
    const int Q = xg * 18 + idx, p = wid - 4, n0 = (p & 1) * 5;
    glds4(pc0, umin((idx + 32 * (8 * p + (lane & 7))) * 128, code_len), smb + LM_SINK);
    const int ch = conv ? 4 * Q + p : 0;
    if (conv) {
      const u16* src[6] = {Are, Aim, Bre, Bim, Cre, Cim};
#pragma unroll
      for (int i = 0; i < 6; ++i) glds4(src[i], ch * (NM * 2) + lane * 4, smb + LM_KIM + p * SLOT + i * 256);
    }
    const u32 vdt = ((const u32*)logdt)[ch >> 1] >> (16 * (ch & 1)), vds = ((const u32*)Dskip)[ch >> 1] >> (16 * (ch & 1));
    bar();
    u32 cr[9], cc[9];
#pragma unroll
    for (int n = 0; n < 9; ++n) { const int ci = 64 * n + lane; cr[n] = ci / 18; cc[n] = (ci % 18) * 16; }
    u32 v1[5], v2[9];
#pragma unroll
    for (int n = 0; n < 5; ++n) v1[n] = n0 ? cr[n < 4 ? 5 + n : 8] * (F * 2) + cc[n < 4 ? 5 + n : 8] : cr[n] * (F * 2) + cc[n];
    const char* w1s = (const char*)(w1 + fb * 144);
    auto issue1 = [&](int k) {
      const char* sp = w1s + (size_t)k * 32 * F * 2;
      const u32 dst = smb + (k % 10) * SLOT + n0 * 1024;
#pragma unroll
      for (int n = 0; n < 4; ++n) glds16(sp, v1[n], dst + n * 1024);
      if (n0 == 0) glds16(sp, v1[4], dst + 4096);
    };
#pragma unroll
    for (int k = 0; k < G1_N0 / 2; ++k) issue1(2 * k + (p >> 1));
    LSTAMP(0);
#pragma unroll
    for (int n = 0; n < 9; ++n) v2[n] = cr[n] * (D * 2) + cc[n];
    const u32 v24s = v2[4] + (lane >= 32 ? 128 * D * 2 : 0);
    const char* w2s = (const char*)(w2 + (size_t)1152 * s * D + 144 * dj);
    auto ksrc = [&](int k) {
      const int r = k % 9;
      return w2s + (size_t)(288 * (((idx >> 1) + k / 9) & 3) + (r < 8 ? 144 * (((r >> 2) ^ idx) & 1) + 32 * (r & 3) : 128)) * (D * 2);
    };
    auto issue2 = [&](int k) {
      const char* sp = ksrc(k);
      const u32 dst = smb + slot2(k) * SLOT;
      if (n0 == 0) {
#pragma unroll
        for (int n = 0; n < 4; ++n) glds16(sp, v2[n], dst + n * 1024);
        glds16(sp, k % 9 == 8 ? v24s : v2[4], dst + 4096);
      } else {
#pragma unroll
        for (int n = 5; n < 9; ++n) glds16(sp + (k % 9 == 8 ? 128 * D * 2 : 0), v2[n], dst + n * 1024);
      }
    };
    auto issue_own = [&]() {
      const char* sp = ksrc(10 + p);
#pragma unroll
      for (int n = 0; n < 9; ++n) glds16(sp, v2[n], smb + (10 + p) * SLOT + n * 1024);
    };
    auto batch2 = [&]() {
      waitv<0>();
      if (lane == 0) lds_add(sm + LM_CNT + 12);
#pragma unroll
      for (int k = G1_N0 / 2; k < G1_N1 / 2; ++k) issue1(2 * k + (p >> 1));
    };
    auto landed2 = [&]() {
      waitv<0>();
      if (lane == 0) lds_add(sm + LM_CNT + 16);
    };
    char LAS* kim = sm + LM_KIM + p * SLOT;
    char LAS* uim = sm + LM_UIM + p * UIM_B;
    if (conv) {
      LSTAMP(3);
      waitv<4 * (G1_N0 / 2)>();
      u32 par[2][6];
#pragma unroll
      for (int i = 0; i < 12; ++i) par[i & 1][i >> 1] = *(const u16 LAS*)(kim + i * 128 + lane * 2);
      const float dt = rb(__expf(bl(vdt)));
      f32x4 ka[2][2];
#pragma unroll
      for (int i = 0; i < 4; ++i) ka[i >> 1][i & 1] = f32x4{0.f, 0.f, 0.f, 0.f};
#pragma unroll 1
      for (int h = 0; h < 2; ++h) {
        float pv[6];
#pragma unroll
        for (int i = 0; i < 6; ++i) pv[i] = bl(h ? par[1][i] : par[0][i]);
        const float p2 = dt * pv[0] * 1.44269504088896340736f;
        const float q = dt * pv[1];
        constexpr float CH = 0.15915494309189535f;
        constexpr float CL = (float)(0.15915494309189533577 - (double)CH);
        const float qh = q * CH;
        const float ql = __builtin_fmaf(q, CL, __builtin_fmaf(q, CH, -qh));
        asm volatile("" ::"v"(ql));
        LSTAMP(50 + h);
        {
          f32x2 Z[16];
          auto put = [&](int i, f32x2 v) { *(u32 LAS*)(kim + i * KG_S + lane * 4) = pk(v[0], v[1]); };
          Z[0] = f32x2{1.f, 0.f};
          Z[1] = zpow2<3>(p2, qh, ql);
          Z[4] = zpow2<5>(p2, qh, ql);
          const f32x2 a7 = zpow2<7>(p2, qh, ql);
          Z[2] = cmul(Z[1], Z[1]);
          Z[8] = cmul(Z[4], Z[4]);
          Z[3] = cmul(Z[2], Z[1]);
#pragma unroll
          for (int i = 1; i < 4; ++i) Z[4 + i] = cmul(Z[4], Z[i]);
#pragma unroll
          for (int i = 1; i < 8; ++i) Z[8 + i] = cmul(Z[8], Z[i]);
#pragma unroll
          for (int i = 0; i < 16; ++i) put(i, Z[i]);
          put(16, a7);
#pragma unroll
          for (int i = 1; i < 16; ++i) put(16 + i, cmul(a7, Z[i]));
        }
        if (h == 1) landed2();
        {
          f32x2 H[16];
          auto put = [&](int i, f32x2 v) { *(u32 LAS*)(uim + i * KG_S + lane * 4) = pk(v[0], -v[1]); };
          H[0] = cmul(f32x2{pv[4], pv[5]}, f32x2{fmul(dt, pv[2]), fmul(dt, pv[3])});
          const f32x2 a0 = zpow2<0>(p2, qh, ql), a8 = zpow2<8>(p2, qh, ql), a10 = zpow2<10>(p2, qh, ql);
          const f32x2 a1 = cmul(a0, a0), a2 = cmul(a1, a1), a9 = cmul(a8, a8), a11 = cmul(a10, a10);
          H[1] = cmul(a0, H[0]);
          H[2] = cmul(a1, H[0]);
          H[3] = cmul(a1, H[1]);
#pragma unroll
          for (int i = 0; i < 4; ++i) H[4 + i] = cmul(a2, H[i]);
#pragma unroll
          for (int i = 0; i < 4; ++i) H[8 + i] = cmul(a8, H[2 * i]);
          H[12] = cmul(a9, H[0]);
          H[13] = cmul(a9, H[4]);
          H[14] = cmul(a9, H[8]);
          H[15] = cmul(a9, H[10]);
#pragma unroll
          for (int i = 0; i < 16; ++i) put(i, H[i]);
          put(16, cmul(a10, H[0]));
          put(17, cmul(a10, H[8]));
          put(18, cmul(a10, H[12]));
          put(19, cmul(a10, H[14]));
          put(20, cmul(a11, H[0]));
        }
        lds_wait4(sm + LM_CNT);
#pragma unroll
        for (int k = 0; k < 4; ++k) {
          u32x4 lo[2], hi[2];
#pragma unroll
          for (int i = 0; i < 2; ++i) {
            lo[i] = *(const u32x4 LAS*)(kim + (16 * i + c) * KG_S + k * 64 + g * 16);
            hi[i] = *(const u32x4 LAS*)(uim + min(16 * i + c, 20) * KG_S + k * 64 + g * 16);
          }
#pragma unroll
          for (int i = 0; i < 4; ++i) ka[i >> 1][i & 1] = mfma16(lo[i & 1], hi[i >> 1], ka[i >> 1][i & 1]);
        }
        if (h == 0) batch2();
      }
      LSTAMP(1);
#pragma unroll
      for (int i = 0; i < 4; ++i)
        *(u32x2 LAS*)(uim + (16 * (i >> 1) + c) * 64 + (i & 1) * 32 + g * 8) =
            u32x2{pk(ka[i >> 1][i & 1][0], ka[i >> 1][i & 1][1]), pk(ka[i >> 1][i & 1][2], ka[i >> 1][i & 1][3])};
      waitl<0>();
    } else {
      if (L2PF & 1) {
#pragma unroll
        for (int i = 0; i < 2; ++i) {
          const int ln = min(((idx - 18) * 8 + 2 * p + i) * 42 + min(lane, 41), 256 * 18 - 1);
          glds4(w1, ((320 + ln / 18) * F + 1152 * s + (ln % 18) * 64) * 2, smb + LM_SINK);
        }
      }
      batch2();
      landed2();
    }
    LSTAMP(2);
    if (p < 2) {
      static_for<0, G1_NB>([&](auto K) {
        constexpr int i = decltype(K)::value;
        constexpr int prev = i ? G1_UPTO[i ? i - 1 : 0] : G1_N1 - 1, need = G1_POS[i + 1] - 1;
        static_assert(G1_UPTO[i] - 10 < G1_POS[i] - 1 && prev >= need, "ring schedule");
        if (need >= G1_N1) { if (p == 0) waitv<5 * (prev - need)>(); else waitv<4 * (prev - need)>(); }
        LSTAMP(4 + 2 * i);
        bar();
        LSTAMP(5 + 2 * i);
#pragma unroll
        for (int u = prev + 1; u <= G1_UPTO[i]; ++u) { if (u < 18) issue1(u); else issue2(u - 18); }
      });
      tag = *(const u64 LAS*)(sm + LM_TAG);
    } else {
      const int q = p - 2;
      if (conv) {
        bar();
        tag = *(const u64 LAS*)(sm + LM_TAG);
        u64 fl[2];
#pragma unroll
        for (int j = 0; j < 2; ++j)
          fl[j] = __hip_atomic_load((GAS u64*)(ws + FL_LN2 * 128) + q * 128 + j * 64 + lane, __ATOMIC_RELAXED, __HIP_MEMORY_SCOPE_AGENT);
        u32x4 uv[8];
        auto uload = [&](int e) {
#pragma unroll
          for (int i = e; i < e + 2; ++i)
            uv[i] = ld128(rsrc(ws + WS_UT, L * D * 2), (128 * q + (lane >> 2)) * 9216 + Q * 64 + (lane & 3) * 16, i * 16 * 9216);
        };
#pragma unroll
        for (int j = 0; j < 2; ++j) {
          const char LAS* tb = sm + LM_UIM + (2 * q + j) * UIM_B;
          char LAS* ki = sm + LM_KIM + (2 * q + j) * SLOT;
          char LAS* ko = ki + (28 + lane) * 16;
          auto put = [&](int mi, const u32(&kv)[9]) {
            *(u32x4 LAS*)(ko + mi * 1024) = u32x4{kv[1] | (kv[2] << 16), kv[3] | (kv[4] << 16), kv[5] | (kv[6] << 16), kv[7] | (kv[8] << 16)};
            *(u32x4 LAS*)(ko + mi * 1024 + KIM_C1) = u32x4{kv[0] | (kv[1] << 16), kv[2] | (kv[3] << 16), kv[4] | (kv[5] << 16), kv[6] | (kv[7] << 16)};
          };
          {
            u32 kv[9];
#pragma unroll
            for (int e = 0; e < 9; ++e) {
              constexpr int EX[9] = {448, 384, 320, 256, 192, 128, 64, 0, 446}, R2[9] = {450, 640, 576, 576, 576, 512, 448, 448, 448};
              int o = 2 * lane + (lane < (e == 8 ? 33 : 32) ? EX[e] : R2[e]);
              if (e == 0 && lane == 63) o = 768;
              kv[e] = *(const u16 LAS*)(tb + o);
            }
            if (lane == 0) kv[8] = 0u;
            put(0, kv);
          }
          bar();
          if (j == 0) {
            if (!__all(fl[0] == tag && fl[1] == tag)) {
              flag_wait(ws, FL_LN + q * 128, 64, tag, lane);
              flag_wait(ws, FL_LN + q * 128 + 64, 64, tag, lane);
            }
            asm volatile("" ::: "memory");
          }
          uload(4 * j);
          if (lane < 56) *(u32x4 LAS*)(ki + (lane & 1) * KIM_C1 + (lane >> 1) * 16) = u32x4{0u, 0u, 0u, 0u};
          {
            const int oa = 768 + 128 * (lane >> 5) + 2 * (lane & 31), oe = 768 + 128 * ((lane + 1) >> 5) + 2 * ((lane + 1) & 31);
            const u32 a = *(const u16 LAS*)(tb + oa), bb = *(const u16 LAS*)(tb + oa + 64), e = *(const u16 LAS*)(tb + oe);
            const u32 kv[9] = {e, e, bb, bb, bb, a, a, a, a};
            put(1, kv);
          }
#pragma unroll
          for (int mi = 2; mi < 4; ++mi) {
            const u32 a = *(const u16 LAS*)(tb + 768 + 128 * mi + 2 * lane), e = *(const u16 LAS*)(tb + 770 + 128 * mi + 2 * lane);
            const u32 tie = (lane & 1) ? e : a;
            const u32 kv[9] = {e, e, e, tie, a, a, a, a, a};
            put(mi, kv);
          }
          waitl<0>();
          bar();
          if (j == 0) uload(2);
          if (j == 0) uload(6);
        }
#pragma unroll
        for (int e = 0; e < 8; ++e) *(u32x4 LAS*)(sm + LM_UIM + (lane & 3) * UIM_B + 64 + (128 * q + 16 * e + (lane >> 2)) * 16) = uv[e];
        if (lane < 16) *(u32x4 LAS*)(sm + LM_UIM + (lane >> 2) * UIM_B + (lane & 3) * 16) = u32x4{0u, 0u, 0u, 0u};
        waitl<0>();
        bar();
      } else {
        const int wv = (idx - 18) * 2 + q;
#pragma unroll
        for (int i = 0; i < 2 * G1_NB; ++i) {
          if (L2PF & 2) glds4(w2 + (size_t)1152 * s * D, min((wv * 12 + i) * 32 + (lane & 31), 1152 * 9 - 1) * 128, smb + LM_SINK);
          if (i & 1) bar(); else __builtin_amdgcn_s_sleep(9);
        }
        tag = *(const u64 LAS*)(sm + LM_TAG);
      }
    }
    bar();
    LSTAMP(22);
#pragma unroll
    for (int u = G1_UPTO[G1_NB - 1] + 1; u < 28; ++u)
      if ((u & 1) == (p >> 1)) issue2(u - 18);
    if (!conv) {
      waitv<0>();
      bar();
      LSTAMP(23);
      issue_own();
    } else {
      const float dsk = bl(vds);
      const char LAS* kp = kim + (c & 1) * KIM_C1 + (4 * (c >> 1) + g) * 16;
      char LAS* ub = uim + 64;
      const char LAS* up[4];
#pragma unroll
      for (int i = 0; i < 4; ++i) {
        const char LAS* q = ub + 2 * (8 * ((c >> 2) - g) + 2 * (c & 3) - 6) + i * 1024;
        asm volatile("" : "+v"(q));
        up[i] = q + 448;
      }
      char LAS* yp = ub + 2 * (8 * (g + 4 * (c >> 1)) + 4 * (c & 1));
      u32x2 sk[8];
#pragma unroll
      for (int T = 0; T < 8; ++T) {
        const u32x2 uu = *(const u32x2 LAS*)(yp + T * 512);
        sk[T] = u32x2{pk(fmul(bl(uu[0]), dsk), fmul(bh(uu[0]), dsk)), pk(fmul(bl(uu[1]), dsk), fmul(bh(uu[1]), dsk))};
        asm volatile("" : "+v"(sk[T]));
      }
      f32x4 acc[8];
#pragma unroll
      for (int T = 0; T < 8; ++T) { acc[T] = f32x4{0.f, 0.f, 0.f, 0.f}; asm volatile("" : "+v"(acc[T])); }
      u32x4 kf[2][8], uf[2][8];
      auto rdk = [&](int be, int a) { kf[be & 1][a] = *(const u32x4 LAS*)(kp + be * 64 + a * 512); };
      auto rdu = [&](int be, int a) {
        const u32 LAS* q = (const u32 LAS*)(up[a >> 1] - be * 64 + (a & 1) * 512);
        uf[be & 1][a] = u32x4{q[0], q[1], q[2], q[3]};
      };
#pragma unroll
      for (int a = 0; a < 8; ++a) { rdk(0, a); rdu(0, a); }
      waitv<0>(); waitl<0>();
      __builtin_amdgcn_s_setprio(3);
      SB;
      bar();
      SB;
      LSTAMP(23);
      static_for<0, 288>([&](auto N) {
        constexpr int n = decltype(N)::value, be = n / 36, m = n % 36;
        constexpr int a = m < 8 ? 0 : m < 15 ? 1 : m < 21 ? 2 : m < 26 ? 3 : m < 30 ? 4 : m < 33 ? 5 : m < 35 ? 6 : 7;
        constexpr int T = a + m - (8 * a - a * (a - 1) / 2);
        acc[T] = mfma16(uf[be & 1][T - a], kf[be & 1][a], acc[T]);
        SB;
        if (be < 7 && m < 16 && (m & 1) == 0) rdu(be + 1, m >> 1);
        if (be < 7 && m >= 16 && m < 24) rdk(be + 1, (m - 16) & 7);
        if (n == 251) { waitl<0>(); issue_own(); }
        SB;
      });
      const bool odd = c & 1;
#pragma unroll
      for (int T = 0; T < 8; ++T) {
        const u32 p01 = pk(acc[T][0], acc[T][1]), p23 = pk(acc[T][2], acc[T][3]);
        const u32 own = odd ? p23 : p01;
        const u32 rcv = (u32)__builtin_amdgcn_mov_dpp((int)(odd ? p01 : p23), 0xB1, 0xF, 0xF, true);
        const u32 ev = odd ? rcv : own, od = odd ? own : rcv;
        *(u32x2 LAS*)(yp + T * 512) = u32x2{pk(fadd(bl(ev), bl(sk[T][0])), fadd(bl(od), bh(sk[T][0]))),
                                            pk(fadd(bh(ev), bl(sk[T][1])), fadd(bh(od), bh(sk[T][1])))};
      }
      waitl<0>();
      __builtin_amdgcn_s_setprio(0);
    }
    LSTAMP(24);
    u64 fa = 0;
    static_for<1, 8>([&](auto K) {
      constexpr int st = 2 * decltype(K)::value;
      constexpr int yng = st == 14 ? 0 : g2_upto(st - 2) - (st + 2);
      if (st == 14 && wid == 7 && !__all(fa == tag)) flag_wait(ws, FL_A + dj * 36, 36, tag, lane);
      if (st == 6 && conv) waitv<0>();
      if (st > 2) { if (n0 == 0) waitv<5 * yng>(); else waitv<4 * yng>(); }
      LSTAMP(24 + st);
      bar();
      LSTAMP(25 + st);
      if (conv && st == 4) {
        const __amdgpu_buffer_rsrc_t ao = rsrc(ws + WS_A2 + (size_t)Q * 16384 + 4096 * p, 4096);
        const char LAS* tp = sm + LM_UIM + (c >> 2) * UIM_B + 64 + 2 * (512 * p + 32 * g) + (c & 3) * 8;
#pragma unroll
        for (int i = 0; i < 4; ++i) {
          const u32x2 a = tr_read(tp + i * 256), bb = tr_read(tp + i * 256 + 32);
          st128(u32x4{a[0], a[1], bb[0], bb[1]}, ao, lane * 16, i * 1024);
        }
      }
      if (conv && st == 6 && tid == 256) flag_set(ws, FL_A + Q, tag);
#pragma unroll
      for (int j = g2_upto(st - 2) + 1; j <= g2_upto(st); ++j) {
        if (p < 2) issue2(2 * j); else issue2(2 * j + 1);
      }
      if (st == 12 && wid == 7) fa = __hip_atomic_load((GAS u64*)(ws + (FL_A + dj * 36 + min(lane, 35)) * 128), __ATOMIC_RELAXED, __HIP_MEMORY_SCOPE_AGENT);
    });
    if (tid == 256 && b == 0) flag_set(ws, FL_EPOCH, tag);
    Epi epi[2];
    load_base(epi[0], p);
    if (p == 0) load_base(epi[1], 7);
    const int ft[2] = {p, 7};
    if (p == 0) finish_lds(std::integral_constant<int, 2>{}, epi, ft);
    asm volatile(".Lk_all_tail:");
    if (p != 0) finish_lds(std::integral_constant<int, 1>{}, epi, ft);
  }
  LSTAMP(54);
  if (wid == 4 && (b & 15) == 0) {
    u64 m = PADMIN ? ~0ull : 0ull;
    for (int i = 0; i < 256; ++i) {
      const u64 v = __hip_atomic_load((GAS u64*)(ws + WS_END) + i, __ATOMIC_RELAXED, __HIP_MEMORY_SCOPE_AGENT);
      m = PADMIN ? (v < m ? v : m) : (v > m ? v : m);
    }
    while (__builtin_amdgcn_s_memrealtime() - m < 20000ull) __builtin_amdgcn_s_sleep(2);
  }
}

extern "C" void kernel_launch(void* const* d_in, const int* in_sizes, int n_in, void* d_out, int out_size, void* d_ws,
                              size_t ws_size, hipStream_t stream) {
  const u16* const* in = (const u16* const*)d_in;
  (void)hipFuncSetAttribute((const void*)k_all, hipFuncAttributeMaxDynamicSharedMemorySize, LM_END);
  k_all<<<256, 512, LM_END, stream>>>(in[0], in[1], in[2], in[3], in[4], in[5], in[6], in[7], in[8], in[10], in[11], in[12], in[13],
                                      (char*)d_ws, (u16*)d_out);
}
```

```cpp
#include <hip/hip_runtime.h>
#include <hip/hip_bf16.h>
#include <stdint.h>
#include <type_traits>

typedef unsigned short u16;
typedef unsigned int u32;
typedef unsigned long long u64;
typedef __bf16 bf16x8 __attribute__((ext_vector_type(8)));
typedef __bf16 bf16x2 __attribute__((ext_vector_type(2)));
typedef short s16x4 __attribute__((ext_vector_type(4)));
typedef float f32x4 __attribute__((ext_vector_type(4)));
typedef float f32x2 __attribute__((ext_vector_type(2)));
typedef u32 u32x4 __attribute__((ext_vector_type(4)));
typedef u32 u32x2 __attribute__((ext_vector_type(2)));
#define LAS __attribute__((address_space(3)))
#define GAS __attribute__((address_space(1)))
#define POLL_SLEEP 4
#define SB __builtin_amdgcn_sched_barrier(0)

constexpr int L = 2048, D = 576, NM = 128, F = 4608;

constexpr int FL_EPOCH = 0;
constexpr int FL_LN = 1;
constexpr int FL_A = FL_LN + 128;
constexpr int FL_H = FL_A + 144;
constexpr int FL_X = FL_H + 256;
constexpr int FL_LN2 = FL_X + 256;
constexpr size_t WS_XNF = 131072;
constexpr size_t WS_UT = WS_XNF + (size_t)L * D * 2;
constexpr size_t WS_A2 = WS_UT + (size_t)L * D * 2;
constexpr size_t WS_HF = WS_A2 + (size_t)L * D * 2;
constexpr size_t WS_SLAB = WS_HF + (size_t)L * F * 2;
constexpr int SLAB_B = 9216, HB = 73728;
constexpr size_t WS_END = WS_SLAB + (size_t)64 * 16 * SLAB_B;
#define LSTAMP(i)
#define RSTAMP(i)

__device__ __forceinline__ float bl(u32 v) { return __uint_as_float(v << 16); }
__device__ __forceinline__ float bh(u32 v) { return __uint_as_float(v & 0xffff0000u); }
__device__ __forceinline__ float b2f(u16 v) { return __uint_as_float(((u32)v) << 16); }
__device__ __forceinline__ u32 pk(float lo, float hi) {
  bf16x2 r = {(__bf16)lo, (__bf16)hi};
  return __builtin_bit_cast(u32, r);
}
__device__ __forceinline__ float fadd(float a, float b) {
  float r;
  asm("v_add_f32 %0, %1, %2" : "=v"(r) : "v"(a), "v"(b));
  return r;
}
__device__ __forceinline__ u32 add2(u32 a, u32 b) {
  u32 r, t0, t1;
  asm("v_lshlrev_b32 %0, 16, %3\n\tv_lshlrev_b32 %1, 16, %4\n\tv_add_f32 %0, %0, %1\n\tv_and_b32 %1, 0xffff0000, %3\n\t"
      "v_and_b32 %2, 0xffff0000, %4\n\tv_add_f32 %1, %1, %2\n\tv_cvt_pk_bf16_f32 %0, %0, %1"
      : "=&v"(r), "=&v"(t0), "=&v"(t1) : "v"(a), "v"(b));
  return r;
}
__device__ __forceinline__ u32 sum4(float lo, float hi, u32 a, u32 b, u32 c) {
  u32 r, t0, t1;
  asm("v_lshlrev_b32 %0, 16, %5\n\tv_and_b32 %1, 0xffff0000, %5\n\tv_add_f32 %0, %3, %0\n\tv_add_f32 %1, %4, %1\n\t"
      "v_lshlrev_b32 %2, 16, %6\n\tv_add_f32 %0, %0, %2\n\tv_and_b32 %2, 0xffff0000, %6\n\tv_add_f32 %1, %1, %2\n\t"
      "v_lshlrev_b32 %2, 16, %7\n\tv_add_f32 %0, %0, %2\n\tv_and_b32 %2, 0xffff0000, %7\n\tv_add_f32 %1, %1, %2\n\t"
      "v_cvt_pk_bf16_f32 %0, %0, %1"
      : "=&v"(r), "=&v"(t0), "=&v"(t1) : "v"(lo), "v"(hi), "v"(a), "v"(b), "v"(c));
  return r;
}
__device__ __forceinline__ float rb(float f) { return (float)(__bf16)f; }
__device__ __forceinline__ f32x4 mfma16(u32x4 a, u32x4 b, f32x4 c) {
  return __builtin_amdgcn_mfma_f32_16x16x32_bf16(__builtin_bit_cast(bf16x8, a), __builtin_bit_cast(bf16x8, b), c, 0, 0, 0);
}
__device__ __forceinline__ u32x2 tr_read(const char LAS* p) {
  return __builtin_bit_cast(u32x2, __builtin_amdgcn_ds_read_tr16_b64_v4i16((s16x4 LAS*)p));
}
__device__ __forceinline__ __amdgpu_buffer_rsrc_t rsrc(const void* p, int bytes) {
  return __builtin_amdgcn_make_buffer_rsrc((void*)p, 0, bytes, 0x00020000);
}
constexpr int SC1 = 16;
__device__ __forceinline__ u32 umin(u32 a, u32 b) { return a < b ? a : b; }

__device__ __forceinline__ void st128(u32x4 v, __amdgpu_buffer_rsrc_t r, int voff, int soff) {
  __builtin_amdgcn_raw_buffer_store_b128(v, r, voff, soff, SC1);
  asm volatile("s_nop 1" ::"v"(v) : "memory");
}
__device__ __forceinline__ u32x4 ld128(__amdgpu_buffer_rsrc_t r, int voff, int soff) {
  return __builtin_amdgcn_raw_buffer_load_b128(r, voff, soff, SC1);
}
__device__ __forceinline__ void glds16(const char* base, u32 voff, u32 dst) {
  asm volatile("s_mov_b32 m0, %2\n\ts_nop 0\n\tglobal_load_lds_dwordx4 %0, %1" ::"v"(voff), "s"(base), "s"(dst) : "memory");
}
__device__ __forceinline__ void glds4(const void* base, u32 voff, u32 dst) {
  asm volatile("s_mov_b32 m0, %2\n\ts_nop 0\n\tglobal_load_lds_dword %0, %1" ::"v"(voff), "s"(base), "s"(dst) : "memory");
}
template <int N> __device__ __forceinline__ void waitv() { asm volatile("s_waitcnt vmcnt(%0)" ::"i"(N) : "memory"); }
template <int N> __device__ __forceinline__ void waitl() { asm volatile("s_waitcnt lgkmcnt(%0)" ::"i"(N) : "memory"); }
__device__ __forceinline__ void bar() {
  asm volatile("" ::: "memory");
  __builtin_amdgcn_s_barrier();
  asm volatile("" ::: "memory");
}
template <int N> __device__ __forceinline__ void bars() {
#pragma unroll
  for (int i = 0; i < N; ++i) bar();
}
template <int I, int N, class Fn> __device__ __forceinline__ void static_for(Fn&& fn) {
  if constexpr (I < N) {
    fn(std::integral_constant<int, I>{});
    static_for<I + 1, N>(fn);
  }
}
__device__ __forceinline__ void flag_set(char* ws, int off, u64 tag) {
  __hip_atomic_store((GAS u64*)(ws + off * 128), tag, __ATOMIC_RELAXED, __HIP_MEMORY_SCOPE_AGENT);
}
__device__ __forceinline__ void flag_wait(char* ws, int off, int n, u64 tag, int lane, int stride = 128) {
  GAS u64* p = (GAS u64*)(ws + off * 128 + min(lane, n - 1) * stride);
#pragma unroll 1
  for (u32 spin = 0; spin < (1u << 20); ++spin) {
    if (__all(__hip_atomic_load(p, __ATOMIC_RELAXED, __HIP_MEMORY_SCOPE_AGENT) == tag)) break;
    __builtin_amdgcn_s_sleep(POLL_SLEEP);
  }
  asm volatile("" ::: "memory");
}
__device__ __forceinline__ u32 lds_add(char LAS* p) {
  return __hip_atomic_fetch_add((u32 LAS*)p, 1u, __ATOMIC_RELAXED, __HIP_MEMORY_SCOPE_WORKGROUP);
}
__device__ __forceinline__ void lds_wait4(char LAS* p) {
  u32 v, sv;
  asm volatile(
      "1:\n\tds_read_b32 %0, %2\n\ts_waitcnt lgkmcnt(0)\n\tv_readfirstlane_b32 %1, %0\n\ts_cmp_lt_u32 %1, 4\n\ts_cbranch_scc0 2f\n\t"
      "s_sleep 1\n\ts_branch 1b\n2:"
      : "=&v"(v), "=&s"(sv) : "v"(p) : "memory", "scc");
}

__device__ __forceinline__ float fmul(float a, float b) {
  float r;
  asm("v_mul_f32 %0, %1, %2" : "=v"(r) : "v"(a), "v"(b));
  return r;
}
__device__ __forceinline__ f32x2 cmul(f32x2 a, f32x2 b) {
  float re, im;
  asm("v_mul_f32 %0, %2, %4\n\tv_mul_f32 %1, %2, %5\n\tv_fma_f32 %0, -%3, %5, %0\n\tv_fma_f32 %1, %3, %4, %1"
      : "=&v"(re), "=&v"(im) : "v"(a[0]), "v"(a[1]), "v"(b[0]), "v"(b[1]));
  return f32x2{re, im};
}
template <int E> __device__ __forceinline__ f32x2 zpow2(float p2, float qh, float ql) {
  constexpr float s = (float)(1 << E);
  const float mag = __builtin_amdgcn_exp2f(p2 * s);
  const float ph = __builtin_fmaf(ql, s, __builtin_amdgcn_fractf(qh * s));
  float re, im;
  asm("s_nop 1\n\tv_mul_f32 %0, %2, %4\n\tv_mul_f32 %1, %3, %4"
      : "=&v"(re), "=&v"(im) : "v"(__builtin_amdgcn_cosf(ph)), "v"(__builtin_amdgcn_sinf(ph)), "v"(mag));
  return f32x2{re, im};
}
template <int CTRL> __device__ __forceinline__ float dpp_add(float v) {
  return v + __builtin_bit_cast(float, __builtin_amdgcn_update_dpp(0, __builtin_bit_cast(int, v), CTRL, 0xF, 0xF, true));
}
__device__ __forceinline__ float sum16(float v) { return dpp_add<0x121>(dpp_add<0x122>(dpp_add<0x124>(dpp_add<0x128>(v)))); }
__device__ __forceinline__ u32 gelu2(float a, float b) {
  constexpr float C0 = -2.f * 1.44269504088896340736f * 0.7978845608028654f, C1 = C0 * 0.044715f;
  const f32x2 v = {a, b};
  const f32x2 z = v * __builtin_elementwise_fma(v * v, f32x2{C1, C1}, f32x2{C0, C0});
  const f32x2 d = f32x2{__builtin_amdgcn_exp2f(z[0]), __builtin_amdgcn_exp2f(z[1])} + f32x2{1.f, 1.f};
  const f32x2 r = v * f32x2{__builtin_amdgcn_rcpf(d[0]), __builtin_amdgcn_rcpf(d[1])};
  return pk(r[0], r[1]);
}
__device__ __forceinline__ u32x4 gelu8(f32x4 a, f32x4 b) {
  return u32x4{gelu2(a[0], a[1]), gelu2(a[2], a[3]), gelu2(b[0], b[1]), gelu2(b[2], b[3])};
}

constexpr int SLOT = 9216;
constexpr int KIM_C1 = 4640, UIM_B = 5952, KG_S = 272, LN_S = 1168;
constexpr int LM_KIM = 10 * SLOT;
constexpr int LM_UIM = 14 * SLOT;
constexpr int LM_LN = LM_KIM;
constexpr int LM_CNT = LM_UIM + 4 * UIM_B;
constexpr int LM_TAG = LM_CNT + 32;
constexpr int LM_SINK = LM_TAG + 16;
constexpr int LM_END = LM_SINK + 256;
static_assert(LM_END <= 163840 && 4 * UIM_B >= 18432 && 21 * KG_S <= UIM_B && 32 * KG_S <= SLOT && UIM_B % 256 == 64, "LDS map");
constexpr int slot2(int j) { return j % 14 < 10 ? (j % 14 + 8) % 10 : j % 14; }

#define G2_XP 15
#define G2_NUM 2
#define G2_DEN 3
#define G2_X0 8
constexpr int G1_N0 = 4, G1_N1 = 10, G1_NB = 6;
constexpr int G1_POS[G1_NB + 1] = {6, 8, 10, 12, 14, 16, 18};
constexpr int G1_UPTO[G1_NB] = {13, 16, 18, 20, 22, 24};
constexpr bool g1_bar_before(int ks) {
  for (int i = 0; i < G1_NB; ++i)
    if (G1_POS[i] == ks) return true;
  return false;
}
constexpr int g2_upto(int st) { return st < 2 ? 6 : st + 6 < 17 ? st + 6 : 17; }

constexpr int h_upto(int ks) {
  const int kn = G2_XP - G2_X0 - 1;
  const int v = ks <= kn ? G2_X0 + 1 + 2 * ks : G2_X0 + 1 + 2 * kn + (ks - kn) * G2_NUM / G2_DEN;
  return v < 35 ? v : 35;
}

__global__ __launch_bounds__(512, 2) void k_all(const u16* __restrict__ x, const u16* __restrict__ Are, const u16* __restrict__ Aim,
                                                const u16* __restrict__ Bre, const u16* __restrict__ Bim, const u16* __restrict__ Cre,
                                                const u16* __restrict__ Cim, const u16* __restrict__ logdt, const u16* __restrict__ Dskip,
                                                const u16* __restrict__ w1, const u16* __restrict__ b1, const u16* __restrict__ w2,
                                                const u16* __restrict__ b2, char* ws, u16* __restrict__ out) {
  extern __shared__ __attribute__((aligned(16))) char smem[];
  char LAS* sm = (char LAS*)smem;
  const u32 smb = (u32)(uintptr_t)sm;
  const int tid = threadIdx.x, lane = tid & 63, wid = __builtin_amdgcn_readfirstlane(tid >> 6);
  const int b = blockIdx.x, xg = b & 7, idx = b >> 3, s = xg & 3;
  const int lb = 4 * (xg >> 2) + (idx >> 3), fb = 8 * s + (idx & 7);
  const int l2 = 8 * (xg >> 2) + (idx >> 2), dj = idx & 3, tile = l2 * 4 + dj;
  const int cl = xg * 4 + (idx >> 3);
  const bool conv = idx < 18;
  const int nln = conv ? 0 : idx < 20 ? 2 : 1, lt = xg * 16 + idx - 16;
  const int c = lane & 15, g = lane >> 4;
  const char* pc0;
  u32 code_len;
  asm volatile("s_getpc_b64 %0\n.Lk_all_head:\n\ts_mov_b32 %1, .Lk_all_tail-.Lk_all_head" : "=s"(pc0), "=s"(code_len));
  u64 tag;
  RSTAMP(62);
  LSTAMP(63);
  const __amdgpu_buffer_rsrc_t rs = rsrc(ws + WS_SLAB + (size_t)tile * 16 * SLAB_B, 16 * SLAB_B);
  struct Epi { u32x2 xv[2], bias; u32x4 av; };
  auto load_base = [&](Epi& e, int t) {
    const int d = 144 * dj + 16 * t + 4 * g, l = 128 * l2 + 32 * s + c;
    e.bias = *(const u32x2*)(b2 + d);
    e.av = ld128(rsrc(ws + WS_A2, L * D * 2), (d >> 2) * 16384 + (4 * l2 + s) * 256 + c * 16, 0);
    e.xv[0] = *(const u32x2*)(x + (size_t)l * D + d);
    e.xv[1] = *(const u32x2*)(x + (size_t)(l + 16) * D + d);
  };
  auto finish = [&](auto NT, const Epi* e, const int* t, const f32x4 (*own)[2]) {
    constexpr int nt = decltype(NT)::value;
    u32 base[nt][4];
#pragma unroll
    for (int j = 0; j < nt; ++j)
#pragma unroll
      for (int i = 0; i < 4; ++i) base[j][i] = add2(e[j].xv[i >> 1][i & 1], e[j].av[i]);
    flag_wait(ws, FL_X + tile * 4, 4, tag, lane);
    LSTAMP(53);
    u32x4 part[nt][3];
#pragma unroll
    for (int j = 0; j < nt; ++j)
#pragma unroll
      for (int k = 0; k < 3; ++k) part[j][k] = ld128(rs, lane * 16, (s * 4 + ((s + 1 + k) & 3)) * SLAB_B + t[j] * 1024);
#pragma unroll
    for (int j = 0; j < nt; ++j) {
      u16* dst = out + (size_t)(128 * l2 + 32 * s + c) * D + 144 * dj + 16 * t[j] + 4 * g;
#pragma unroll
      for (int i = 0; i < 2; ++i) {
        u32 o[2];
#pragma unroll
        for (int h = 0; h < 2; ++h) {
          const u32 r = sum4(own[j][i][2 * h], own[j][i][2 * h + 1], part[j][0][2 * i + h], part[j][1][2 * i + h], part[j][2][2 * i + h]);
          o[h] = add2(base[j][2 * i + h], add2(r, e[j].bias[h]));
        }
        *(u32x2*)(dst + i * 16 * D) = u32x2{o[0], o[1]};
      }
    }
  };
  auto finish_lds = [&](auto NT, const Epi* e, const int* t) {
    constexpr int nt = decltype(NT)::value;
    while (__builtin_amdgcn_readfirstlane(*(volatile u32 LAS*)(sm + LM_CNT + 24)) < 8u) __builtin_amdgcn_s_sleep(2);
    asm volatile("" ::: "memory");
    f32x4 own[nt][2];
#pragma unroll
    for (int j = 0; j < nt; ++j)
#pragma unroll
      for (int i = 0; i < 2; ++i) own[j][i] = *(const f32x4 LAS*)(sm + LM_UIM + (2 * t[j] + i) * 1024 + lane * 16);
    finish(NT, e, t, own);
  };
  if (__builtin_expect(wid < 4, 1)) {
    __builtin_amdgcn_s_setprio(2);
    u32x2 bq[9];
    {
      const int r = tid >> 4;
      u32x2 raw[2][9];
#pragma unroll
      for (int j = 0; j < 2; ++j)
        if (j < nln) {
          const u32x2* src = (const u32x2*)(x + (size_t)((lt - 2 * j) * 16 + r) * D) + c;
#pragma unroll
          for (int i = 0; i < 9; ++i) raw[j][i] = src[16 * i];
        }
      const u64 ep = __hip_atomic_load((GAS u64*)(ws + FL_EPOCH), __ATOMIC_RELAXED, __HIP_MEMORY_SCOPE_AGENT);
#pragma unroll
      for (int t = 0; t < 9; ++t) bq[t] = *(const u32x2*)(b1 + fb * 144 + 16 * t + 4 * g);
      if (tid < 2) *(u32x4 LAS*)(sm + LM_CNT + tid * 16) = u32x4{0u, 0u, 0u, 0u};
      waitl<0>();
      bar();
#pragma unroll 1
      for (int j = 0; j < nln; ++j) {
        f32x2 v[18];
#pragma unroll
        for (int i = 0; i < 18; ++i) {
          const u32 t = j ? raw[1][i >> 1][i & 1] : raw[0][i >> 1][i & 1];
          v[i] = f32x2{bl(t), bh(t)};
        }
        f32x2 s2 = v[0];
#pragma unroll
        for (int i = 1; i < 18; ++i) s2 += v[i];
        const float mean = sum16(s2[0] + s2[1]) * (1.f / D);
        LSTAMP(0);
        f32x2 q2 = {0.f, 0.f};
#pragma unroll
        for (int i = 0; i < 18; ++i) { f32x2 d = v[i] - f32x2{mean, mean}; q2 = __builtin_elementwise_fma(d, d, q2); }
        const float q = sum16(q2[0] + q2[1]);
        const float m = rb(mean);
        const float sd = rb(__builtin_amdgcn_sqrtf(rb(rb(q * (1.f / D)) + 1e-5f)));
        const float rs = __builtin_amdgcn_rcpf(sd);
#pragma unroll
        for (int i = 0; i < 9; ++i) {
          u32x2 o;
#pragma unroll
          for (int e = 0; e < 2; ++e) {
            const f32x2 d = v[2 * i + e] - f32x2{m, m};
            const u32 t = pk(d[0], d[1]);
            const f32x2 n = f32x2{bl(t), bh(t)} * f32x2{rs, rs};
            o[e] = pk(n[0], n[1]);
          }
          *(u32x2 LAS*)(sm + LM_LN + (16 * j + r) * LN_S + (c + 16 * i) * 8) = o;
        }
      }
      if (nln) {
        waitl<0>();
        if (lane == 0) lds_add(sm + LM_CNT);
        lds_wait4(sm + LM_CNT);
        LSTAMP(1);
#pragma unroll 1
        for (int j = 0; j < nln; ++j) {
          const __amdgpu_buffer_rsrc_t xo = rsrc(ws + WS_XNF + (size_t)(lt - 2 * j) * 18432, 18432);
          const char LAS* xp = sm + LM_LN + (16 * j + c) * LN_S + wid * 64 + g * 8;
#pragma unroll
          for (int kk = 0; kk < 5; ++kk) {
            const u32x2 a = *(const u32x2 LAS*)(xp + kk * 256), bb = *(const u32x2 LAS*)(xp + kk * 256 + 32);
            st128(u32x4{a[0], a[1], bb[0], bb[1]}, xo, lane * 16, (wid + 4 * kk) * 1024);
          }
        }
      }
      tag = (((u64)__builtin_amdgcn_readfirstlane((u32)(ep >> 32)) << 32) | __builtin_amdgcn_readfirstlane((u32)ep)) * 0x9E3779B97F4A7C15ull +
            0xD1B54A32D192ED03ull;
      if (tid == 0) *(u64 LAS*)(sm + LM_TAG) = tag;
    }
    {
      constexpr int XP = 3, WD = 3, WR = 4;
      const int lt0 = 16 * lb + 4 * wid;
      const __amdgpu_buffer_rsrc_t rx = rsrc(ws + WS_XNF + (size_t)lt0 * 18432, 4 * 18432);
      f32x4 acc[9][4];
#pragma unroll
      for (int t = 0; t < 9; ++t) {
#pragma unroll
        for (int i = 0; i < 4; ++i) {
          acc[t][i] = f32x4{bl(bq[t][0]), bh(bq[t][0]), bl(bq[t][1]), bh(bq[t][1])};
          asm volatile("" : "+v"(acc[t][i]));
        }
      }
      waitv<0>(); waitl<0>();
      LSTAMP(2);
      if (lane == 0 && lds_add(sm + LM_CNT + 4) == 3u && nln) {
        flag_set(ws, FL_LN + lt, tag);
        if (nln == 2) flag_set(ws, FL_LN + lt - 2, tag);
      }
      flag_wait(ws, FL_LN + lt0, 4, tag, lane);
      LSTAMP(3);
      u32x4 xf[XP + 1][4];
#pragma unroll
      for (int k = 0; k < XP; ++k)
#pragma unroll
        for (int i = 0; i < 4; ++i) xf[k][i] = ld128(rx, lane * 16, i * 18432 + k * 1024);
      SB;
      const char LAS* wp = sm + (lane >> 2) * 288 + (lane & 3) * 8;
      u32x2 wa[WR], wb[WR];
      const char LAS* wph = wp + 6 * SLOT;
      asm volatile("" : "+v"(wph));
      auto wptr = [&](int n) {
        const int sl = (n / 9) % 10;
        return (sl < 6 ? wp + sl * SLOT : wph + (sl - 6) * SLOT) + (n % 9) * 32;
      };
      lds_wait4(sm + LM_CNT + 12);
      LSTAMP(4);
      RSTAMP(55);
#pragma unroll
      for (int n = 0; n < WD; ++n) { wa[n] = tr_read(wptr(n)); wb[n] = tr_read(wptr(n) + 16 * 288); }
#pragma unroll
      for (int ks = 0; ks < 18; ++ks) {
#pragma unroll
        for (int t = 0; t < 9; ++t) {
          const int n = 9 * ks + t;
          const u32x4 w = {wa[n % WR][0], wa[n % WR][1], wb[n % WR][0], wb[n % WR][1]};
          acc[t][0] = mfma16(w, xf[ks % (XP + 1)][0], acc[t][0]);
          SB;
          if (t == 9 - WD && g1_bar_before(ks + 1)) bar();
          if (t == 9 - WD && ks + 1 == G1_N0) lds_wait4(sm + LM_CNT + 16);
          if (n == 161) bar();
          if (n + WD < 162) wa[(n + WD) % WR] = tr_read(wptr(n + WD));
          SB;
          acc[t][1] = mfma16(w, xf[ks % (XP + 1)][1], acc[t][1]);
          SB;
          if (n + WD < 162) wb[(n + WD) % WR] = tr_read(wptr(n + WD) + 16 * 288);
          SB;
          acc[t][2] = mfma16(w, xf[ks % (XP + 1)][2], acc[t][2]);
          SB;
          if (t < 4 && ks + XP < 18) xf[(ks + XP) % (XP + 1)][t] = ld128(rx, lane * 16, t * 18432 + (ks + XP) * 1024);
          SB;
          acc[t][3] = mfma16(w, xf[ks % (XP + 1)][3], acc[t][3]);
          SB;
        }
      }
      asm volatile("s_nop 15");
      SB;
      LSTAMP(5);
      RSTAMP(56);
      const __amdgpu_buffer_rsrc_t rh = rsrc(ws + WS_HF + (size_t)(cl * 8 + (idx & 7)) * HB, HB);
#pragma unroll
      for (int i = 0; i < 4; ++i)
#pragma unroll
        for (int r = 0; r < 4; ++r) st128(gelu8(acc[2 * r][i], acc[2 * r + 1][i]), rh, lane * 16, (4 * wid + i) * 4096 + r * 1024);
#pragma unroll
      for (int i = 0; i < 2; ++i) st128(gelu8(acc[8][2 * i], acc[8][2 * i + 1]), rh, lane * 16, 65536 + (2 * wid + i) * 1024);
      LSTAMP(6);
    }
    bar();
    {
      constexpr int XP = G2_XP, X0 = G2_X0, WD = 5, WR = 6, TAIL = 4;
      const __amdgpu_buffer_rsrc_t rh = rsrc(ws + WS_HF + (size_t)cl * 8 * HB, 8 * HB);
      const int pr = 4 * ((idx >> 2) & 1) + wid;
      f32x4 acc[9][2];
#pragma unroll
      for (int t = 0; t < 9; ++t)
#pragma unroll
        for (int i = 0; i < 2; ++i) {
          acc[t][i] = f32x4{0.f, 0.f, 0.f, 0.f};
          asm volatile("" : "+v"(acc[t][i]));
        }
      const char LAS* wp = sm + (lane >> 2) * 288 + (lane & 3) * 8;
      u32x2 wa[WR], wb[WR];
      constexpr auto ord_ks = [](int n) { return n < 9 * (36 - TAIL) ? n / 9 : 36 - TAIL + (n - 9 * (36 - TAIL)) % TAIL; };
      constexpr auto ord_t = [](int n) { return n < 9 * (36 - TAIL) ? n % 9 : (n - 9 * (36 - TAIL)) / TAIL; };
      const char LAS* wph = wp + 7 * SLOT;
      asm volatile("" : "+v"(wph));
      auto wptr = [&](int n) {
        const int sl = slot2(ord_ks(n));
        return (sl < 7 ? wp + sl * SLOT : wph + (sl - 7) * SLOT) + ord_t(n) * 32;
      };
#pragma unroll
      for (int n = 0; n < WD; ++n) { wa[n] = tr_read(wptr(n)); wb[n] = tr_read(wptr(n) + 16 * 288); }
      u32x4 xf[XP + 1][2];
      auto hload = [&](int k, int i) {
        const int r = k % 9, P = ((idx >> 1) + k / 9) & 3;
        const int so = r < 8 ? (2 * P + (((r >> 2) ^ idx) & 1)) * HB + pr * 8192 + i * 4096 : (2 * P + i) * HB + 65536 + pr * 1024;
        xf[k % (XP + 1)][i] = ld128(rh, lane * 16 + (r < 8 ? (r & 3) * 1024 : 0), so);
      };
      waitv<0>();
      LSTAMP(7);
      const int flh = FL_H + cl * 8;
      if (lane == 0 && lds_add(sm + LM_CNT + 8) == 3u) flag_set(ws, flh + (idx & 7), tag);
      lds_wait4(sm + LM_CNT + 8);
#pragma unroll
      for (int k = 0; k < 4; ++k) { hload(k, 0); hload(k, 1); }
      flag_wait(ws, flh, 8, tag, lane);
      LSTAMP(8);
      RSTAMP(57);
#pragma unroll
      for (int k = 4; k < X0; ++k) { hload(k, 0); hload(k, 1); }
      SB;
      Epi epi;
      const int ft = wid == s ? 8 : 4 + ((wid - s - 1) & 3);
      auto flush = [&](auto OWN, int tt) {
        if (decltype(OWN)::value) {
          *(f32x4 LAS*)(sm + LM_UIM + (2 * tt) * 1024 + lane * 16) = acc[tt][0];
          *(f32x4 LAS*)(sm + LM_UIM + (2 * tt + 1) * 1024 + lane * 16) = acc[tt][1];
          asm volatile("" ::: "memory");
          if (tt == 7) *(volatile u32 LAS*)(sm + LM_CNT + 24) = 8u;
        } else {
          const u32x4 v = {pk(acc[tt][0][0], acc[tt][0][1]), pk(acc[tt][0][2], acc[tt][0][3]),
                           pk(acc[tt][1][0], acc[tt][1][1]), pk(acc[tt][1][2], acc[tt][1][3])};
          st128(v, rs, lane * 16, (wid * 4 + s) * SLAB_B + tt * 1024);
        }
      };
      auto step = [&](auto N, auto OWN) {
        constexpr int n = decltype(N)::value;
        constexpr int ks = ord_ks(n), t = ord_t(n);
        if (ks % 9 == 8 && t == 0) {
          const u32x4 a = xf[ks % (XP + 1)][0], bb = xf[ks % (XP + 1)][1];
          xf[ks % (XP + 1)][0] = u32x4{a[0], a[1], bb[0], bb[1]};
          xf[ks % (XP + 1)][1] = u32x4{a[2], a[3], bb[2], bb[3]};
        }
        const u32x4 w = {wa[n % WR][0], wa[n % WR][1], wb[n % WR][0], wb[n % WR][1]};
        acc[t][0] = mfma16(w, xf[ks % (XP + 1)][0], acc[t][0]);
        SB;
        if ((n + WD) % 36 == 18 && n + WD >= 54 && n + WD <= 270) bar();
        if (n + WD < 324) wa[(n + WD) % WR] = tr_read(wptr(n + WD));
        SB;
        acc[t][1] = mfma16(w, xf[ks % (XP + 1)][1], acc[t][1]);
        SB;
        if (n + WD < 324) wb[(n + WD) % WR] = tr_read(wptr(n + WD) + 16 * 288);
        if (n < 9 * (36 - TAIL) && (t & 1) && h_upto(ks - 1) + 1 + (t >> 2) <= h_upto(ks)) hload(h_upto(ks - 1) + 1 + (t >> 2), (t >> 1) & 1);
        if (ks == 30 && t == 2) load_base(epi, ft);
        if (ks == 37 - TAIL && t > 0 && n >= 9 * (36 - TAIL)) flush(OWN, t > 0 ? t - 1 : 0);
        SB;
      };
      static_for<0, 9 * (36 - TAIL) + TAIL>([&](auto N) { step(N, std::false_type{}); });
      if (wid == s) {
        static_for<9 * (36 - TAIL) + TAIL, 324>([&](auto N) { step(N, std::true_type{}); });
        LSTAMP(9);
        RSTAMP(58);
        asm volatile("s_nop 15");
        finish(std::integral_constant<int, 1>{}, &epi, &ft, &acc[8]);
      } else {
        static_for<9 * (36 - TAIL) + TAIL, 324>([&](auto N) { step(N, std::false_type{}); });
        flush(std::false_type{}, 8);
        LSTAMP(9);
        RSTAMP(58);
        waitv<0>();
        if (lane == 0 && lds_add(sm + LM_CNT + 28) == 2u) flag_set(ws, FL_X + tile * 4 + s, tag);
        LSTAMP(52);
        finish_lds(std::integral_constant<int, 1>{}, &epi, &ft);
      }
    }
  } else {
    const int Q = xg * 18 + idx, p = wid - 4, n0 = (p & 1) * 5;
    glds4(pc0, umin((idx + 32 * (8 * p + (lane & 7))) * 128, code_len), smb + LM_SINK);
    const int ch = conv ? 4 * Q + p : 0;
    if (conv) {
      const u16* src[6] = {Are, Aim, Bre, Bim, Cre, Cim};
#pragma unroll
      for (int i = 0; i < 6; ++i) glds4(src[i], ch * (NM * 2) + lane * 4, smb + LM_KIM + p * SLOT + i * 256);
    }
    const u32 vdt = ((const u32*)logdt)[ch >> 1] >> (16 * (ch & 1)), vds = ((const u32*)Dskip)[ch >> 1] >> (16 * (ch & 1));
    bar();
    u32 cr[9], cc[9];
#pragma unroll
    for (int n = 0; n < 9; ++n) { const int ci = 64 * n + lane; cr[n] = ci / 18; cc[n] = (ci % 18) * 16; }
    u32 v1[5], v2[9];
#pragma unroll
    for (int n = 0; n < 5; ++n) v1[n] = n0 ? cr[n < 4 ? 5 + n : 8] * (F * 2) + cc[n < 4 ? 5 + n : 8] : cr[n] * (F * 2) + cc[n];
    const char* w1s = (const char*)(w1 + fb * 144);
    auto issue1 = [&](int k) {
      const char* sp = w1s + (size_t)k * 32 * F * 2;
      const u32 dst = smb + (k % 10) * SLOT + n0 * 1024;
#pragma unroll
      for (int n = 0; n < 4; ++n) glds16(sp, v1[n], dst + n * 1024);
      if (n0 == 0) glds16(sp, v1[4], dst + 4096);
    };
#pragma unroll
    for (int k = 0; k < G1_N0 / 2; ++k) issue1(2 * k + (p >> 1));
    if (conv) {
      const int wv = ((xg >> 2) * 18 + idx) * 4 + p;
      const int ln = wv * 56 + min(lane, 55);
      glds4(w1, ((128 + ln / 18) * F + 1152 * s + (ln % 18) * 64) * 2, smb + LM_SINK);
#pragma unroll
      for (int i = 0; i < 2; ++i) glds4(w2 + (size_t)1152 * s * D, ((wv * 2 + i) * 36 + min(lane, 35)) * 128, smb + LM_SINK);
    }
    LSTAMP(0);
#pragma unroll
    for (int n = 0; n < 9; ++n) v2[n] = cr[n] * (D * 2) + cc[n];
    const u32 v24s = v2[4] + (lane >= 32 ? 128 * D * 2 : 0);
    const char* w2s = (const char*)(w2 + (size_t)1152 * s * D + 144 * dj);
    auto ksrc = [&](int k) {
      const int r = k % 9;
      return w2s + (size_t)(288 * (((idx >> 1) + k / 9) & 3) + (r < 8 ? 144 * (((r >> 2) ^ idx) & 1) + 32 * (r & 3) : 128)) * (D * 2);
    };
    auto issue2 = [&](int k) {
      const char* sp = ksrc(k);
      const u32 dst = smb + slot2(k) * SLOT;
      if (n0 == 0) {
#pragma unroll
        for (int n = 0; n < 4; ++n) glds16(sp, v2[n], dst + n * 1024);
        glds16(sp, k % 9 == 8 ? v24s : v2[4], dst + 4096);
      } else {
#pragma unroll
        for (int n = 5; n < 9; ++n) glds16(sp + (k % 9 == 8 ? 128 * D * 2 : 0), v2[n], dst + n * 1024);
      }
    };
    auto issue_own = [&]() {
      const char* sp = ksrc(10 + p);
#pragma unroll
      for (int n = 0; n < 9; ++n) glds16(sp, v2[n], smb + (10 + p) * SLOT + n * 1024);
    };
    auto batch2 = [&]() {
      waitv<0>();
      if (lane == 0) lds_add(sm + LM_CNT + 12);
      if (!conv) lds_wait4(sm + LM_CNT + 4);
#pragma unroll
      for (int k = G1_N0 / 2; k < G1_N1 / 2; ++k) issue1(2 * k + (p >> 1));
    };
    auto landed2 = [&]() {
      waitv<0>();
      if (lane == 0) lds_add(sm + LM_CNT + 16);
    };
    char LAS* kim = sm + LM_KIM + p * SLOT;
    char LAS* uim = sm + LM_UIM + p * UIM_B;
    if (conv) {
      LSTAMP(3);
      waitv<4 * (G1_N0 / 2) + 3>();
      u32 par[2][6];
#pragma unroll
      for (int i = 0; i < 12; ++i) par[i & 1][i >> 1] = *(const u16 LAS*)(kim + i * 128 + lane * 2);
      const float dt = rb(__expf(bl(vdt)));
      f32x4 ka[2][2];
#pragma unroll
      for (int i = 0; i < 4; ++i) ka[i >> 1][i & 1] = f32x4{0.f, 0.f, 0.f, 0.f};
#pragma unroll 1
      for (int h = 0; h < 2; ++h) {
        float pv[6];
#pragma unroll
        for (int i = 0; i < 6; ++i) pv[i] = bl(h ? par[1][i] : par[0][i]);
        const float p2 = dt * pv[0] * 1.44269504088896340736f;
        const float q = dt * pv[1];
        constexpr float CH = 0.15915494309189535f;
        constexpr float CL = (float)(0.15915494309189533577 - (double)CH);
        const float qh = q * CH;
        const float ql = __builtin_fmaf(q, CL, __builtin_fmaf(q, CH, -qh));
        asm volatile("" ::"v"(ql));
        LSTAMP(50 + h);
        {
          f32x2 Z[16];
          auto put = [&](int i, f32x2 v) { *(u32 LAS*)(kim + i * KG_S + lane * 4) = pk(v[0], v[1]); };
          Z[0] = f32x2{1.f, 0.f};
          Z[1] = zpow2<3>(p2, qh, ql);
          Z[4] = zpow2<5>(p2, qh, ql);
          const f32x2 a7 = zpow2<7>(p2, qh, ql);
          Z[2] = cmul(Z[1], Z[1]);
          Z[8] = cmul(Z[4], Z[4]);
          Z[3] = cmul(Z[2], Z[1]);
#pragma unroll
          for (int i = 1; i < 4; ++i) Z[4 + i] = cmul(Z[4], Z[i]);
#pragma unroll
          for (int i = 1; i < 8; ++i) Z[8 + i] = cmul(Z[8], Z[i]);
#pragma unroll
          for (int i = 0; i < 16; ++i) put(i, Z[i]);
          put(16, a7);
#pragma unroll
          for (int i = 1; i < 16; ++i) put(16 + i, cmul(a7, Z[i]));
        }
        if (h == 1) landed2();
        {
          f32x2 H[16];
          auto put = [&](int i, f32x2 v) { *(u32 LAS*)(uim + i * KG_S + lane * 4) = pk(v[0], -v[1]); };
          H[0] = cmul(f32x2{pv[4], pv[5]}, f32x2{fmul(dt, pv[2]), fmul(dt, pv[3])});
          const f32x2 a0 = zpow2<0>(p2, qh, ql), a8 = zpow2<8>(p2, qh, ql), a10 = zpow2<10>(p2, qh, ql);
          const f32x2 a1 = cmul(a0, a0), a2 = cmul(a1, a1), a9 = cmul(a8, a8), a11 = cmul(a10, a10);
          H[1] = cmul(a0, H[0]);
          H[2] = cmul(a1, H[0]);
          H[3] = cmul(a1, H[1]);
#pragma unroll
          for (int i = 0; i < 4; ++i) H[4 + i] = cmul(a2, H[i]);
#pragma unroll
          for (int i = 0; i < 4; ++i) H[8 + i] = cmul(a8, H[2 * i]);
          H[12] = cmul(a9, H[0]);
          H[13] = cmul(a9, H[4]);
          H[14] = cmul(a9, H[8]);
          H[15] = cmul(a9, H[10]);
#pragma unroll
          for (int i = 0; i < 16; ++i) put(i, H[i]);
          put(16, cmul(a10, H[0]));
          put(17, cmul(a10, H[8]));
          put(18, cmul(a10, H[12]));
          put(19, cmul(a10, H[14]));
          put(20, cmul(a11, H[0]));
        }
#pragma unroll
        for (int k = 0; k < 4; ++k) {
          u32x4 lo[2], hi[2];
#pragma unroll
          for (int i = 0; i < 2; ++i) {
            lo[i] = *(const u32x4 LAS*)(kim + (16 * i + c) * KG_S + k * 64 + g * 16);
            hi[i] = *(const u32x4 LAS*)(uim + min(16 * i + c, 20) * KG_S + k * 64 + g * 16);
          }
#pragma unroll
          for (int i = 0; i < 4; ++i) ka[i >> 1][i & 1] = mfma16(lo[i & 1], hi[i >> 1], ka[i >> 1][i & 1]);
        }
        if (h == 0) batch2();
      }
      LSTAMP(1);
#pragma unroll
      for (int i = 0; i < 4; ++i)
        *(u32x2 LAS*)(uim + (16 * (i >> 1) + c) * 64 + (i & 1) * 32 + g * 8) =
            u32x2{pk(ka[i >> 1][i & 1][0], ka[i >> 1][i & 1][1]), pk(ka[i >> 1][i & 1][2], ka[i >> 1][i & 1][3])};
      waitl<0>();
    } else {
      batch2();
#pragma unroll 1
      for (int j = 0; j < nln; ++j) {
        const __amdgpu_buffer_rsrc_t uo = rsrc(ws + WS_UT + (size_t)(lt - 2 * j) * 18432, 18432);
        const char LAS* tp = sm + LM_LN + (16 * j + 8 * (g & 1) + (c >> 2)) * LN_S + p * 64 + (g >> 1) * 32 + (c & 3) * 8;
#pragma unroll
        for (int kk = 0; kk < 5; ++kk) {
          const u32x2 t0 = tr_read(tp + kk * 256), t1 = tr_read(tp + kk * 256 + 4 * LN_S);
          st128(u32x4{t0[0], t0[1], t1[0], t1[1]}, uo, (16 * (g >> 1) + c) * 32 + (g & 1) * 16, (p + 4 * kk) * 1024);
        }
      }
      landed2();
    }
    LSTAMP(2);
    if (p < 2) {
      static_for<0, G1_NB>([&](auto K) {
        constexpr int i = decltype(K)::value;
        constexpr int prev = i ? G1_UPTO[i ? i - 1 : 0] : G1_N1 - 1, need = G1_POS[i + 1] - 1;
        static_assert(G1_UPTO[i] - 10 < G1_POS[i] - 1 && prev >= need, "ring schedule");
        if (need >= G1_N1) { if (p == 0) waitv<5 * (prev - need)>(); else waitv<4 * (prev - need)>(); }
        LSTAMP(4 + 2 * i);
        bar();
        LSTAMP(5 + 2 * i);
#pragma unroll
        for (int u = prev + 1; u <= G1_UPTO[i]; ++u) { if (u < 18) issue1(u); else issue2(u - 18); }
      });
      tag = *(const u64 LAS*)(sm + LM_TAG);
    } else {
      const int q = p - 2;
      lds_wait4(sm + LM_CNT + 4);
      lds_wait4(sm + LM_CNT + 16);
      tag = *(const u64 LAS*)(sm + LM_TAG);
      if (tid == 384) flag_set(ws + b * 8, FL_LN2, tag);
      if (conv) {
        bar();
        u64 fl[4];
        u32x4 uv[8];
        auto uload = [&](int e) {
#pragma unroll
          for (int i = e; i < e + 2; ++i)
            uv[i] = ld128(rsrc(ws + WS_UT, L * D * 2), (64 * q + (lane >> 3)) * 18432 + Q * 128 + (lane & 7) * 16, i * 8 * 18432);
        };
#pragma unroll
        for (int j = 0; j < 2; ++j) {
          const char LAS* tb = sm + LM_UIM + (2 * q + j) * UIM_B;
          char LAS* ki = sm + LM_KIM + (2 * q + j) * SLOT;
          char LAS* ko = ki + (28 + lane) * 16;
          auto put = [&](int mi, const u32(&kv)[9]) {
            *(u32x4 LAS*)(ko + mi * 1024) = u32x4{kv[1] | (kv[2] << 16), kv[3] | (kv[4] << 16), kv[5] | (kv[6] << 16), kv[7] | (kv[8] << 16)};
            *(u32x4 LAS*)(ko + mi * 1024 + KIM_C1) = u32x4{kv[0] | (kv[1] << 16), kv[2] | (kv[3] << 16), kv[4] | (kv[5] << 16), kv[6] | (kv[7] << 16)};
          };
          {
            u32 kv[9];
#pragma unroll
            for (int e = 0; e < 9; ++e) {
              constexpr int EX[9] = {448, 384, 320, 256, 192, 128, 64, 0, 446}, R2[9] = {450, 640, 576, 576, 576, 512, 448, 448, 448};
              int o = 2 * lane + (lane < (e == 8 ? 33 : 32) ? EX[e] : R2[e]);
              if (e == 0 && lane == 63) o = 768;
              kv[e] = *(const u16 LAS*)(tb + o);
            }
            if (lane == 0) kv[8] = 0u;
            put(0, kv);
          }
          bar();
          if (j == 0) {
#pragma unroll
            for (int e = 0; e < 4; ++e)
              fl[e] = __hip_atomic_load((GAS u64*)(ws + FL_LN2 * 128) + e * 64 + lane, __ATOMIC_RELAXED, __HIP_MEMORY_SCOPE_AGENT);
          } else {
            uload(4);
            uload(6);
          }
          if (lane < 56) *(u32x4 LAS*)(ki + (lane & 1) * KIM_C1 + (lane >> 1) * 16) = u32x4{0u, 0u, 0u, 0u};
          {
            const int oa = 768 + 128 * (lane >> 5) + 2 * (lane & 31), oe = 768 + 128 * ((lane + 1) >> 5) + 2 * ((lane + 1) & 31);
            const u32 a = *(const u16 LAS*)(tb + oa), bb = *(const u16 LAS*)(tb + oa + 64), e = *(const u16 LAS*)(tb + oe);
            const u32 kv[9] = {e, e, bb, bb, bb, a, a, a, a};
            put(1, kv);
          }
#pragma unroll
          for (int mi = 2; mi < 4; ++mi) {
            const u32 a = *(const u16 LAS*)(tb + 768 + 128 * mi + 2 * lane), e = *(const u16 LAS*)(tb + 770 + 128 * mi + 2 * lane);
            const u32 tie = (lane & 1) ? e : a;
            const u32 kv[9] = {e, e, e, tie, a, a, a, a, a};
            put(mi, kv);
          }
          waitl<0>();
          bar();
          if (j == 0) {
            if (!__all(fl[0] == tag && fl[1] == tag && fl[2] == tag && fl[3] == tag)) {
#pragma unroll 1
              for (int e = 0; e < 4; ++e) flag_wait(ws, FL_LN2 + 4 * e, 64, tag, lane, 8);
            }
            asm volatile("" ::: "memory");
          }
          if (j == 0) { uload(0); uload(2); }
        }
#pragma unroll
        for (int e = 0; e < 8; ++e) *(u32x4 LAS*)(sm + LM_UIM + ((lane >> 1) & 3) * UIM_B + 64 + (64 * q + 8 * e + (lane >> 3)) * 32 + (lane & 1) * 16) = uv[e];
        if (lane < 16) *(u32x4 LAS*)(sm + LM_UIM + (lane >> 2) * UIM_B + (lane & 3) * 16) = u32x4{0u, 0u, 0u, 0u};
        waitl<0>();
        bar();
      } else {
        bars<G1_NB>();
      }
    }
    bar();
    LSTAMP(22);
#pragma unroll
    for (int u = G1_UPTO[G1_NB - 1] + 1; u < 28; ++u)
      if ((u & 1) == (p >> 1)) issue2(u - 18);
    if (!conv) {
      waitv<0>();
      bar();
      LSTAMP(23);
      issue_own();
    } else {
      const float dsk = bl(vds);
      const char LAS* kp = kim + (c & 1) * KIM_C1 + (4 * (c >> 1) + g) * 16;
      char LAS* ub = uim + 64;
      const char LAS* up[4];
#pragma unroll
      for (int i = 0; i < 4; ++i) {
        const char LAS* q = ub + 2 * (8 * ((c >> 2) - g) + 2 * (c & 3) - 6) + i * 1024;
        asm volatile("" : "+v"(q));
        up[i] = q + 448;
      }
      char LAS* yp = ub + 2 * (8 * (g + 4 * (c >> 1)) + 4 * (c & 1));
      u32x2 sk[8];
#pragma unroll
      for (int T = 0; T < 8; ++T) {
        const u32x2 uu = *(const u32x2 LAS*)(yp + T * 512);
        sk[T] = u32x2{pk(fmul(bl(uu[0]), dsk), fmul(bh(uu[0]), dsk)), pk(fmul(bl(uu[1]), dsk), fmul(bh(uu[1]), dsk))};
        asm volatile("" : "+v"(sk[T]));
      }
      f32x4 acc[8];
#pragma unroll
      for (int T = 0; T < 8; ++T) { acc[T] = f32x4{0.f, 0.f, 0.f, 0.f}; asm volatile("" : "+v"(acc[T])); }
      u32x4 kf[2][8], uf[2][8];
      auto rdk = [&](int be, int a) { kf[be & 1][a] = *(const u32x4 LAS*)(kp + be * 64 + a * 512); };
      auto rdu = [&](int be, int a) {
        const u32 LAS* q = (const u32 LAS*)(up[a >> 1] - be * 64 + (a & 1) * 512);
        uf[be & 1][a] = u32x4{q[0], q[1], q[2], q[3]};
      };
#pragma unroll
      for (int a = 0; a < 8; ++a) { rdk(0, a); rdu(0, a); }
      waitv<0>(); waitl<0>();
      __builtin_amdgcn_s_setprio(3);
      SB;
      bar();
      SB;
      LSTAMP(23);
      static_for<0, 288>([&](auto N) {
        constexpr int n = decltype(N)::value, be = n / 36, m = n % 36;
        constexpr int a = m < 8 ? 0 : m < 15 ? 1 : m < 21 ? 2 : m < 26 ? 3 : m < 30 ? 4 : m < 33 ? 5 : m < 35 ? 6 : 7;
        constexpr int T = a + m - (8 * a - a * (a - 1) / 2);
        acc[T] = mfma16(uf[be & 1][T - a], kf[be & 1][a], acc[T]);
        SB;
        if (be < 7 && m < 16 && (m & 1) == 0) rdu(be + 1, m >> 1);
        if (be < 7 && m >= 16 && m < 24) rdk(be + 1, (m - 16) & 7);
        if (n == 251) { waitl<0>(); issue_own(); }
        SB;
      });
      const bool odd = c & 1;
#pragma unroll
      for (int T = 0; T < 8; ++T) {
        const u32 p01 = pk(acc[T][0], acc[T][1]), p23 = pk(acc[T][2], acc[T][3]);
        const u32 own = odd ? p23 : p01;
        const u32 rcv = (u32)__builtin_amdgcn_mov_dpp((int)(odd ? p01 : p23), 0xB1, 0xF, 0xF, true);
        const u32 ev = odd ? rcv : own, od = odd ? own : rcv;
        *(u32x2 LAS*)(yp + T * 512) = u32x2{pk(fadd(bl(ev), bl(sk[T][0])), fadd(bl(od), bh(sk[T][0]))),
                                            pk(fadd(bh(ev), bl(sk[T][1])), fadd(bh(od), bh(sk[T][1])))};
      }
      waitl<0>();
      __builtin_amdgcn_s_setprio(0);
    }
    LSTAMP(24);
    u64 fa = 0;
    static_for<1, 8>([&](auto K) {
      constexpr int st = 2 * decltype(K)::value;
      constexpr int yng = st == 14 ? 0 : g2_upto(st - 2) - (st + 2);
      if (st == 14 && wid == 7 && !__all(fa == tag)) flag_wait(ws, FL_A + dj * 36, 36, tag, lane);
      if (st == 6 && conv) waitv<0>();
      if (st > 2) { if (n0 == 0) waitv<5 * yng>(); else waitv<4 * yng>(); }
      LSTAMP(24 + st);
      bar();
      LSTAMP(25 + st);
      if (conv && st == 4) {
        const __amdgpu_buffer_rsrc_t ao = rsrc(ws + WS_A2 + (size_t)Q * 16384 + 4096 * p, 4096);
        const char LAS* tp = sm + LM_UIM + (c >> 2) * UIM_B + 64 + 2 * (512 * p + 32 * g) + (c & 3) * 8;
#pragma unroll
        for (int i = 0; i < 4; ++i) {
          const u32x2 a = tr_read(tp + i * 256), bb = tr_read(tp + i * 256 + 32);
          st128(u32x4{a[0], a[1], bb[0], bb[1]}, ao, lane * 16, i * 1024);
        }
      }
      if (conv && st == 6 && tid == 256) flag_set(ws, FL_A + Q, tag);
#pragma unroll
      for (int j = g2_upto(st - 2) + 1; j <= g2_upto(st); ++j) {
        if (p < 2) issue2(2 * j); else issue2(2 * j + 1);
      }
      if (st == 12 && wid == 7) fa = __hip_atomic_load((GAS u64*)(ws + (FL_A + dj * 36 + min(lane, 35)) * 128), __ATOMIC_RELAXED, __HIP_MEMORY_SCOPE_AGENT);
    });
    if (tid == 256 && b == 0) flag_set(ws, FL_EPOCH, tag);
    Epi epi[2];
    load_base(epi[0], p);
    if (p == 0) load_base(epi[1], 7);
    const int ft[2] = {p, 7};
    if (p == 0) finish_lds(std::integral_constant<int, 2>{}, epi, ft);
    asm volatile(".Lk_all_tail:");
    if (p != 0) finish_lds(std::integral_constant<int, 1>{}, epi, ft);
  }
  LSTAMP(54);
  RSTAMP(59);
}

extern "C" void kernel_launch(void* const* d_in, const int* in_sizes, int n_in, void* d_out, int out_size, void* d_ws,
                              size_t ws_size, hipStream_t stream) {
  const u16* const* in = (const u16* const*)d_in;
  (void)hipFuncSetAttribute((const void*)k_all, hipFuncAttributeMaxDynamicSharedMemorySize, LM_END);
  k_all<<<256, 512, LM_END, stream>>>(in[0], in[1], in[2], in[3], in[4], in[5], in[6], in[7], in[8], in[10], in[11], in[12], in[13],
                                      (char*)d_ws, (u16*)d_out);
}
```

```cpp
#include <hip/hip_runtime.h>
#include <hip/hip_bf16.h>
#include <stdint.h>
#include <type_traits>

typedef unsigned char u8;
typedef unsigned short u16;
typedef unsigned int u32;
typedef unsigned long long u64;
typedef __bf16 bf16x8 __attribute__((ext_vector_type(8)));
typedef __bf16 bf16x2 __attribute__((ext_vector_type(2)));
typedef short s16x4 __attribute__((ext_vector_type(4)));
typedef float f32x4 __attribute__((ext_vector_type(4)));
typedef float f32x2 __attribute__((ext_vector_type(2)));
typedef u32 u32x4 __attribute__((ext_vector_type(4)));
typedef u32 u32x2 __attribute__((ext_vector_type(2)));
#define LAS __attribute__((address_space(3)))
#define GAS __attribute__((address_space(1)))
#define POLL_SLEEP 4
#define SB __builtin_amdgcn_sched_barrier(0)

constexpr int L = 2048, D = 576, NM = 128, F = 4608;

constexpr int FL_EPOCH = 0;
constexpr int FL_LN = 1;
constexpr int FL_A = FL_LN + 128;
constexpr int FL_H = FL_A + 144;
constexpr int FL_X = FL_H + 256;
constexpr int FL_LN2 = FL_X + 256;
constexpr size_t WS_XNF = 131072;
constexpr size_t WS_UT = WS_XNF + (size_t)L * D * 2;
constexpr size_t WS_A2 = WS_UT + (size_t)L * D * 2;
constexpr size_t WS_HF = WS_A2 + (size_t)L * D * 2;
constexpr size_t WS_SLAB = WS_HF + (size_t)L * F * 2;
constexpr int SLAB_B = 9216, HB = 73728;
constexpr size_t WS_END = WS_SLAB + (size_t)64 * 16 * SLAB_B;
#define LSTAMP(i)
#define RSTAMP(i)

__device__ __forceinline__ float bl(u32 v) { return __uint_as_float(v << 16); }
__device__ __forceinline__ float bh(u32 v) { return __uint_as_float(v & 0xffff0000u); }
__device__ __forceinline__ float b2f(u16 v) { return __uint_as_float(((u32)v) << 16); }
__device__ __forceinline__ u32 pk(float lo, float hi) {
  bf16x2 r = {(__bf16)lo, (__bf16)hi};
  return __builtin_bit_cast(u32, r);
}
__device__ __forceinline__ float fadd(float a, float b) {
  float r;
  asm("v_add_f32 %0, %1, %2" : "=v"(r) : "v"(a), "v"(b));
  return r;
}
__device__ __forceinline__ u32 add2(u32 a, u32 b) {
  u32 r, t0, t1;
  asm("v_lshlrev_b32 %0, 16, %3\n\tv_lshlrev_b32 %1, 16, %4\n\tv_add_f32 %0, %0, %1\n\tv_and_b32 %1, 0xffff0000, %3\n\t"
      "v_and_b32 %2, 0xffff0000, %4\n\tv_add_f32 %1, %1, %2\n\tv_cvt_pk_bf16_f32 %0, %0, %1"
      : "=&v"(r), "=&v"(t0), "=&v"(t1) : "v"(a), "v"(b));
  return r;
}
__device__ __forceinline__ u32 sum4(float lo, float hi, u32 a, u32 b, u32 c) {
  u32 r, t0, t1;
  asm("v_lshlrev_b32 %0, 16, %5\n\tv_and_b32 %1, 0xffff0000, %5\n\tv_add_f32 %0, %3, %0\n\tv_add_f32 %1, %4, %1\n\t"
      "v_lshlrev_b32 %2, 16, %6\n\tv_add_f32 %0, %0, %2\n\tv_and_b32 %2, 0xffff0000, %6\n\tv_add_f32 %1, %1, %2\n\t"
      "v_lshlrev_b32 %2, 16, %7\n\tv_add_f32 %0, %0, %2\n\tv_and_b32 %2, 0xffff0000, %7\n\tv_add_f32 %1, %1, %2\n\t"
      "v_cvt_pk_bf16_f32 %0, %0, %1"
      : "=&v"(r), "=&v"(t0), "=&v"(t1) : "v"(lo), "v"(hi), "v"(a), "v"(b), "v"(c));
  return r;
}
__device__ __forceinline__ float rb(float f) { return (float)(__bf16)f; }
__device__ __forceinline__ f32x4 mfma16(u32x4 a, u32x4 b, f32x4 c) {
  return __builtin_amdgcn_mfma_f32_16x16x32_bf16(__builtin_bit_cast(bf16x8, a), __builtin_bit_cast(bf16x8, b), c, 0, 0, 0);
}
template <bool FIRST = false> __device__ __forceinline__ void mma(f32x4& c, u32x4 a, u32x4 b) {
  if (FIRST) asm volatile("v_mfma_f32_16x16x32_bf16 %0, %1, %2, 0" : "=&v"(c) : "v"(a), "v"(b));
  else asm volatile("v_mfma_f32_16x16x32_bf16 %0, %1, %2, %0" : "+v"(c) : "v"(a), "v"(b));
}
template <int N> __device__ __forceinline__ void waitl_seen() { __builtin_amdgcn_s_waitcnt(0xC07F | (N << 8)); }
__device__ __forceinline__ u32x2 tr_read(const char LAS* p) {
  return __builtin_bit_cast(u32x2, __builtin_amdgcn_ds_read_tr16_b64_v4i16((s16x4 LAS*)p));
}
__device__ __forceinline__ __amdgpu_buffer_rsrc_t rsrc(const void* p, int bytes) {
  return __builtin_amdgcn_make_buffer_rsrc((void*)p, 0, bytes, 0x00020000);
}
constexpr int SC1 = 16;
__device__ __forceinline__ u32 umin(u32 a, u32 b) { return a < b ? a : b; }

__device__ __forceinline__ void st128(u32x4 v, __amdgpu_buffer_rsrc_t r, int voff, int soff) {
  __builtin_amdgcn_raw_buffer_store_b128(v, r, voff, soff, SC1);
  asm volatile("s_nop 1" ::"v"(v) : "memory");
}
__device__ __forceinline__ u32x4 ld128(__amdgpu_buffer_rsrc_t r, int voff, int soff) {
  return __builtin_amdgcn_raw_buffer_load_b128(r, voff, soff, SC1);
}
__device__ __forceinline__ void glds16(const char* base, u32 voff, u32 dst) {
  asm volatile("s_mov_b32 m0, %2\n\ts_nop 0\n\tglobal_load_lds_dwordx4 %0, %1" ::"v"(voff), "s"(base), "s"(dst) : "memory");
}
__device__ __forceinline__ void glds4(const void* base, u32 voff, u32 dst) {
  asm volatile("s_mov_b32 m0, %2\n\ts_nop 0\n\tglobal_load_lds_dword %0, %1" ::"v"(voff), "s"(base), "s"(dst) : "memory");
}
template <int N> __device__ __forceinline__ void waitv() { asm volatile("s_waitcnt vmcnt(%0)" ::"i"(N) : "memory"); }
template <int N> __device__ __forceinline__ void waitl() { asm volatile("s_waitcnt lgkmcnt(%0)" ::"i"(N) : "memory"); }
__device__ __forceinline__ void bar() {
  asm volatile("" ::: "memory");
  __builtin_amdgcn_s_barrier();
  asm volatile("" ::: "memory");
}
template <int N> __device__ __forceinline__ void bars() {
#pragma unroll
  for (int i = 0; i < N; ++i) bar();
}
template <int I, int N, class Fn> __device__ __forceinline__ void static_for(Fn&& fn) {
  if constexpr (I < N) {
    fn(std::integral_constant<int, I>{});
    static_for<I + 1, N>(fn);
  }
}
__device__ __forceinline__ void flag_set(char* ws, int off, u64 tag) {
  __hip_atomic_store((GAS u64*)(ws + off * 128), tag, __ATOMIC_RELAXED, __HIP_MEMORY_SCOPE_AGENT);
}
__device__ __forceinline__ void flag_wait(char* ws, int off, int n, u64 tag, int lane) {
  GAS u64* p = (GAS u64*)(ws + off * 128 + min(lane, n - 1) * 128);
#pragma unroll 1
  for (u32 spin = 0; spin < (1u << 20); ++spin) {
    if (__all(__hip_atomic_load(p, __ATOMIC_RELAXED, __HIP_MEMORY_SCOPE_AGENT) == tag)) break;
    __builtin_amdgcn_s_sleep(POLL_SLEEP);
  }
  asm volatile("" ::: "memory");
}
__device__ __forceinline__ u32 lds_add(char LAS* p) {
  return __hip_atomic_fetch_add((u32 LAS*)p, 1u, __ATOMIC_RELAXED, __HIP_MEMORY_SCOPE_WORKGROUP);
}
template <int SLEEP = 1> __device__ __forceinline__ void lds_wait4(char LAS* p) {
  u32 v, sv;
  asm volatile(
      "1:\n\tds_read_b32 %0, %2\n\ts_waitcnt lgkmcnt(0)\n\tv_readfirstlane_b32 %1, %0\n\ts_cmp_lt_u32 %1, 4\n\ts_cbranch_scc0 2f\n\t"
      "s_sleep %3\n\ts_branch 1b\n2:"
      : "=&v"(v), "=&s"(sv) : "v"(p), "i"(SLEEP) : "memory", "scc");
}

template <int NEED> __device__ __forceinline__ void ring_wait(u32& seen, char LAS* p) {
  u32 s0;
  asm volatile(
      "1:\n\tv_readfirstlane_b32 %1, %0\n\tds_read_b32 %0, %2\n\ts_add_u32 %1, %1, %3\n\ts_andn2_b32 %1, 0x80808080, %1\n\ts_cbranch_scc1 1b"
      : "+v"(seen), "=&s"(s0) : "v"(p), "i"((128 - NEED) * 0x01010101) : "memory", "scc");
}
__device__ __forceinline__ float fmul(float a, float b) {
  float r;
  asm("v_mul_f32 %0, %1, %2" : "=v"(r) : "v"(a), "v"(b));
  return r;
}
__device__ __forceinline__ f32x2 cmul(f32x2 a, f32x2 b) {
  float re, im;
  asm("v_mul_f32 %0, %2, %4\n\tv_mul_f32 %1, %2, %5\n\tv_fma_f32 %0, -%3, %5, %0\n\tv_fma_f32 %1, %3, %4, %1"
      : "=&v"(re), "=&v"(im) : "v"(a[0]), "v"(a[1]), "v"(b[0]), "v"(b[1]));
  return f32x2{re, im};
}
template <int E> __device__ __forceinline__ f32x2 zpow2(float p2, float qh, float ql) {
  constexpr float s = (float)(1 << E);
  const float mag = __builtin_amdgcn_exp2f(p2 * s);
  const float ph = __builtin_fmaf(ql, s, __builtin_amdgcn_fractf(qh * s));
  float re, im;
  asm("s_nop 1\n\tv_mul_f32 %0, %2, %4\n\tv_mul_f32 %1, %3, %4"
      : "=&v"(re), "=&v"(im) : "v"(__builtin_amdgcn_cosf(ph)), "v"(__builtin_amdgcn_sinf(ph)), "v"(mag));
  return f32x2{re, im};
}
template <int CTRL> __device__ __forceinline__ float dpp_add(float v) {
  return v + __builtin_bit_cast(float, __builtin_amdgcn_update_dpp(0, __builtin_bit_cast(int, v), CTRL, 0xF, 0xF, true));
}
__device__ __forceinline__ float sum16(float v) { return dpp_add<0x121>(dpp_add<0x122>(dpp_add<0x124>(dpp_add<0x128>(v)))); }
__device__ __forceinline__ u32 gelu2(float a, float b) {
  constexpr float C0 = -2.f * 1.44269504088896340736f * 0.7978845608028654f, C1 = C0 * 0.044715f;
  const f32x2 v = {a, b};
  const f32x2 z = v * __builtin_elementwise_fma(v * v, f32x2{C1, C1}, f32x2{C0, C0});
  const f32x2 d = f32x2{__builtin_amdgcn_exp2f(z[0]), __builtin_amdgcn_exp2f(z[1])} + f32x2{1.f, 1.f};
  const f32x2 r = v * f32x2{__builtin_amdgcn_rcpf(d[0]), __builtin_amdgcn_rcpf(d[1])};
  return pk(r[0], r[1]);
}
__device__ __forceinline__ u32x4 gelu8(f32x4 a, f32x4 b) {
  return u32x4{gelu2(a[0], a[1]), gelu2(a[2], a[3]), gelu2(b[0], b[1]), gelu2(b[2], b[3])};
}

constexpr int SLOT = 9216;
constexpr int KIM_C1 = 4640, UIM_B = 5952, KG_S = 272, LN_S = 1168;
constexpr int LM_KIM = 10 * SLOT;
constexpr int LM_UIM = 14 * SLOT;
constexpr int LM_LN = LM_KIM;
constexpr int LM_CNT = LM_UIM + 4 * UIM_B;
constexpr int W_CONS = LM_CNT, W_LAND = LM_CNT + 16, W_TAG = LM_CNT + 24, W_LNT = LM_CNT + 32, W_LND = LM_CNT + 36, W_HD = LM_CNT + 40,
              W_UT = LM_CNT + 44, W_GEN = LM_CNT + 48, W_STG = LM_CNT + 52, W_OWN = LM_CNT + 56, W_A2 = LM_CNT + 60, W_AOK = LM_CNT + 64,
              W_PART = LM_CNT + 68, W_SND = LM_CNT + 72, W_XGO = LM_CNT + 76;
constexpr int LM_SINK = LM_CNT + 80;
constexpr int LM_END = LM_SINK + 256;
static_assert(LM_END <= 163840 && 4 * UIM_B >= 18432 && 21 * KG_S <= UIM_B && 32 * KG_S <= SLOT && UIM_B % 256 == 64, "LDS map");
__host__ __device__ constexpr int slot2(int j) { return j % 14 < 10 ? (j % 14 + 8) % 10 : j % 14; }

#define U_AT 8
#define LD_SLEEP 2
#define LD_DEPTH 2
#define W2_EARLY 0
#define CV_CHK 179
#define LN_SLEEP 32
#define LD_PRIO 0
#define RB 3
#define RB_AT 18
#define RB_SLEEP 2
#define G2_WD 6
#define G2_W2 1
#define W2_SLEEP 16
#define G1_N0 4
#define G2_XP 15
#define G2_NUM 2
#define G2_DEN 3
#define G2_X0 8
constexpr int h_upto(int ks) {
  const int kn = G2_XP - G2_X0 - 1;
  const int v = ks <= kn ? G2_X0 + 1 + 2 * ks : G2_X0 + 1 + 2 * kn + (ks - kn) * G2_NUM / G2_DEN;
  return v < 35 ? v : 35;
}

__global__ __launch_bounds__(512, 2) void k_all(const u16* __restrict__ x, const u16* __restrict__ Are, const u16* __restrict__ Aim,
                                                const u16* __restrict__ Bre, const u16* __restrict__ Bim, const u16* __restrict__ Cre,
                                                const u16* __restrict__ Cim, const u16* __restrict__ logdt, const u16* __restrict__ Dskip,
                                                const u16* __restrict__ w1, const u16* __restrict__ b1, const u16* __restrict__ w2,
                                                const u16* __restrict__ b2, char* ws, u16* __restrict__ out) {
  extern __shared__ __attribute__((aligned(16))) char smem[];
  char LAS* sm = (char LAS*)smem;
  const u32 smb = (u32)(uintptr_t)sm;
  const int tid = threadIdx.x, lane = tid & 63, wid = __builtin_amdgcn_readfirstlane(tid >> 6);
  const int b = blockIdx.x, xg = b & 7, idx = b >> 3, s = xg & 3;
  const int lb = 4 * (xg >> 2) + (idx >> 3), fb = 8 * s + (idx & 7);
  const int l2 = 8 * (xg >> 2) + (idx >> 2), dj = idx & 3, tile = l2 * 4 + dj;
  const int cl = xg * 4 + (idx >> 3);
  const bool conv = idx < 18;
  const int nln = conv ? 0 : idx >= 30 ? 2 : 1, j1 = xg * 12 + idx - 18;
  const int lt = idx >= 30 ? (xg >> 2) * 64 + 48 + (xg & 3) * 4 + (idx & 1) + 2 : j1 + (j1 >= 48 ? 16 : 0);
  const int c = lane & 15, g = lane >> 4;
  const char* pc0;
  u32 code_len;
  asm volatile("s_getpc_b64 %0\n.Lk_all_head:\n\ts_mov_b32 %1, .Lk_all_tail-.Lk_all_head" : "=s"(pc0), "=s"(code_len));
  u64 tag;
  RSTAMP(62);
  LSTAMP(63);
  const __amdgpu_buffer_rsrc_t rs = rsrc(ws + WS_SLAB + (size_t)tile * 16 * SLAB_B, 16 * SLAB_B);
  struct Epi { u32x2 xv[2], bias; u32x4 av; };
  auto load_base = [&](Epi& e, int t) {
    const int d = 144 * dj + 16 * t + 4 * g, l = 128 * l2 + 32 * s + c;
    e.bias = *(const u32x2*)(b2 + d);
    e.av = ld128(rsrc(ws + WS_A2, L * D * 2), (d >> 2) * 16384 + (4 * l2 + s) * 256 + c * 16, 0);
    e.xv[0] = *(const u32x2*)(x + (size_t)l * D + d);
    e.xv[1] = *(const u32x2*)(x + (size_t)(l + 16) * D + d);
  };
  auto finish = [&](auto NT, const Epi* e, const int* t, const f32x4 (*own)[2]) {
    constexpr int nt = decltype(NT)::value;
    u32 base[nt][4];
#pragma unroll
    for (int j = 0; j < nt; ++j)
#pragma unroll
      for (int i = 0; i < 4; ++i) base[j][i] = add2(e[j].xv[i >> 1][i & 1], e[j].av[i]);
    flag_wait(ws, FL_X + tile * 4, 4, tag, lane);
    LSTAMP(53);
    u32x4 part[nt][3];
#pragma unroll
    for (int j = 0; j < nt; ++j)
#pragma unroll
      for (int k = 0; k < 3; ++k) part[j][k] = ld128(rs, lane * 16, (s * 4 + ((s + 1 + k) & 3)) * SLAB_B + t[j] * 1024);
#pragma unroll
    for (int j = 0; j < nt; ++j) {
      u16* dst = out + (size_t)(128 * l2 + 32 * s + c) * D + 144 * dj + 16 * t[j] + 4 * g;
#pragma unroll
      for (int i = 0; i < 2; ++i) {
        u32 o[2];
#pragma unroll
        for (int h = 0; h < 2; ++h) {
          const u32 r = sum4(own[j][i][2 * h], own[j][i][2 * h + 1], part[j][0][2 * i + h], part[j][1][2 * i + h], part[j][2][2 * i + h]);
          o[h] = add2(base[j][2 * i + h], add2(r, e[j].bias[h]));
        }
        *(u32x2*)(dst + i * 16 * D) = u32x2{o[0], o[1]};
      }
    }
  };
  auto finish_lds = [&](auto NT, const Epi* e, const int* t) {
    constexpr int nt = decltype(NT)::value;
    while (__builtin_amdgcn_readfirstlane(*(volatile u32 LAS*)(sm + W_PART)) < 8u) __builtin_amdgcn_s_sleep(2);
    asm volatile("" ::: "memory");
    f32x4 own[nt][2];
#pragma unroll
    for (int j = 0; j < nt; ++j)
#pragma unroll
      for (int i = 0; i < 2; ++i) own[j][i] = *(const f32x4 LAS*)(sm + LM_UIM + (2 * t[j] + i) * 1024 + lane * 16);
    finish(NT, e, t, own);
  };
  if (__builtin_expect(wid < 4, 1)) {
    __builtin_amdgcn_s_setprio(2);
    u32x2 bq[9];
    char LAS* cons = sm + W_CONS + wid;
    char LAS* land = sm + W_LAND;
    u32 pre = 0;
    asm volatile("s_nop 0" : "+v"(pre));
    {
      const int r = tid >> 4;
      u32x2 raw[2][9];
#pragma unroll
      for (int j = 0; j < 2; ++j)
        if (j < nln) {
          const u32x2* src = (const u32x2*)(x + (size_t)((lt - 2 * j) * 16 + r) * D) + c;
#pragma unroll
          for (int i = 0; i < 9; ++i) raw[j][i] = src[16 * i];
        }
      const u64 ep = __hip_atomic_load((GAS u64*)(ws + FL_EPOCH), __ATOMIC_RELAXED, __HIP_MEMORY_SCOPE_AGENT);
#pragma unroll
      for (int t = 0; t < 9; ++t) bq[t] = *(const u32x2*)(b1 + fb * 144 + 16 * t + 4 * g);
      if (tid < 5) *(u32x4 LAS*)(sm + LM_CNT + tid * 16) = u32x4{tid == 1 ? 0x1c1c0000u : 0u, 0u, 0u, 0u};
      waitl<0>();
      bar();
#pragma unroll 1
      for (int j = 0; j < nln; ++j) {
        f32x2 v[18];
#pragma unroll
        for (int i = 0; i < 18; ++i) {
          const u32 t = j ? raw[1][i >> 1][i & 1] : raw[0][i >> 1][i & 1];
          v[i] = f32x2{bl(t), bh(t)};
        }
        f32x2 s2 = v[0];
#pragma unroll
        for (int i = 1; i < 18; ++i) s2 += v[i];
        const float mean = sum16(s2[0] + s2[1]) * (1.f / D);
        LSTAMP(0);
        f32x2 q2 = {0.f, 0.f};
#pragma unroll
        for (int i = 0; i < 18; ++i) { f32x2 d = v[i] - f32x2{mean, mean}; q2 = __builtin_elementwise_fma(d, d, q2); }
        const float q = sum16(q2[0] + q2[1]);
        const float m = rb(mean);
        const float sd = rb(__builtin_amdgcn_sqrtf(rb(rb(q * (1.f / D)) + 1e-5f)));
        const float rs = __builtin_amdgcn_rcpf(sd);
#pragma unroll
        for (int i = 0; i < 9; ++i) {
          u32x2 o;
#pragma unroll
          for (int e = 0; e < 2; ++e) {
            const f32x2 d = v[2 * i + e] - f32x2{m, m};
            const u32 t = pk(d[0], d[1]);
            const f32x2 n = f32x2{bl(t), bh(t)} * f32x2{rs, rs};
            o[e] = pk(n[0], n[1]);
          }
          *(u32x2 LAS*)(sm + LM_LN + (16 * j + r) * LN_S + (c + 16 * i) * 8) = o;
        }
      }
      if (nln) {
        waitl<0>();
        if (lane == 0) lds_add(sm + W_LNT);
        lds_wait4(sm + W_LNT);
        LSTAMP(1);
#pragma unroll 1
        for (int j = 0; j < nln; ++j) {
          const __amdgpu_buffer_rsrc_t xo = rsrc(ws + WS_XNF + (size_t)(lt - 2 * j) * 18432, 18432);
          const char LAS* xp = sm + LM_LN + (16 * j + c) * LN_S + wid * 64 + g * 8;
#pragma unroll
          for (int kk = 0; kk < 5; ++kk) {
            const u32x2 a = *(const u32x2 LAS*)(xp + kk * 256), bb = *(const u32x2 LAS*)(xp + kk * 256 + 32);
            st128(u32x4{a[0], a[1], bb[0], bb[1]}, xo, lane * 16, (wid + 4 * kk) * 1024);
          }
        }
      }
      tag = (((u64)__builtin_amdgcn_readfirstlane((u32)(ep >> 32)) << 32) | __builtin_amdgcn_readfirstlane((u32)ep)) * 0x9E3779B97F4A7C15ull +
            0xD1B54A32D192ED03ull;
      if (tid == 0) *(u64 LAS*)(sm + W_TAG) = tag;
    }
    {
      constexpr int XP = 3, WD = 3, WR = 4;
      const int lt0 = 16 * lb + 4 * wid;
      const __amdgpu_buffer_rsrc_t rx = rsrc(ws + WS_XNF + (size_t)lt0 * 18432, 4 * 18432);
      f32x4 acc[9][4];
#pragma unroll
      for (int t = 0; t < 9; ++t) {
#pragma unroll
        for (int i = 0; i < 4; ++i) {
          acc[t][i] = f32x4{bl(bq[t][0]), bh(bq[t][0]), bl(bq[t][1]), bh(bq[t][1])};
          asm volatile("" : "+v"(acc[t][i]));
        }
      }
      waitv<0>(); waitl<0>();
      LSTAMP(2);
      if (lane == 0 && lds_add(sm + W_LND) == 3u) {
        if (conv) flag_set(ws + b * 8, FL_LN2, tag); else flag_set(ws, FL_LN + lt, tag);
        if (nln == 2) flag_set(ws, FL_LN + lt - 2, tag);
      }
      if (conv) __builtin_amdgcn_s_sleep(LN_SLEEP);
      flag_wait(ws, FL_LN + lt0, 4, tag, lane);
      LSTAMP(3);
      u32x4 xf[XP + 1][4];
#pragma unroll
      for (int k = 0; k < XP; ++k)
#pragma unroll
        for (int i = 0; i < 4; ++i) xf[k][i] = ld128(rx, lane * 16, i * 18432 + k * 1024);
      SB;
      const char LAS* wp = sm + (lane >> 2) * 288 + (lane & 3) * 8;
      u32x2 wa[WR], wb[WR];
      const char LAS* wph = wp + 6 * SLOT;
      asm volatile("" : "+v"(wph));
      auto wptr = [&](int n) {
        const int sl = (n / 9) % 10;
        return (sl < 6 ? wp + sl * SLOT : wph + (sl - 6) * SLOT) + (n % 9) * 32;
      };
      if (lane == 0) lds_add(sm + W_XGO);
      ring_wait<2>(pre, land);
      LSTAMP(4);
      RSTAMP(55);
#pragma unroll
      for (int n = 0; n < WD; ++n) { wa[n] = tr_read(wptr(n)); wb[n] = tr_read(wptr(n) + 16 * 288); }
      static_for<0, 162>([&](auto N) {
        {
          constexpr int n = decltype(N)::value, ks = n / 9, t = n % 9;
          const u32x4 w = {wa[n % WR][0], wa[n % WR][1], wb[n % WR][0], wb[n % WR][1]};
          mma(acc[t][0], w, xf[ks % (XP + 1)][0]);
          SB;
          if (t == 9 - WD && (ks & 1) && ks < 17) ring_wait<ks + 3>(pre, land);
          if (t == 0 && (ks & 1) == 0 && ks) *(volatile u8 LAS*)cons = ks;
          if (n == 161) *(volatile u8 LAS*)cons = conv ? 18 : 19;
          if (n == 150) asm volatile("" : "+v"(pre));
          if (n + WD < 162) wa[(n + WD) % WR] = tr_read(wptr(n + WD));
          SB;
          mma(acc[t][1], w, xf[ks % (XP + 1)][1]);
          SB;
          if (n + WD < 162) wb[(n + WD) % WR] = tr_read(wptr(n + WD) + 16 * 288);
          SB;
          mma(acc[t][2], w, xf[ks % (XP + 1)][2]);
          SB;
          if (t < 4 && ks + XP < 18) xf[(ks + XP) % (XP + 1)][t & 3] = ld128(rx, lane * 16, t * 18432 + (ks + XP) * 1024);
          SB;
          mma(acc[t][3], w, xf[ks % (XP + 1)][3]);
          SB;
        }
      });
      asm volatile("s_nop 15");
      if (conv) lds_wait4(sm + W_GEN);
      SB;
      LSTAMP(5);
      RSTAMP(56);
      const __amdgpu_buffer_rsrc_t rh = rsrc(ws + WS_HF + (size_t)(cl * 8 + (idx & 7)) * HB, HB);
#pragma unroll
      for (int i = 0; i < 4; ++i)
#pragma unroll
        for (int r = 0; r < 4; ++r) st128(gelu8(acc[2 * r][i], acc[2 * r + 1][i]), rh, lane * 16, (4 * wid + i) * 4096 + r * 1024);
#pragma unroll
      for (int i = 0; i < 2; ++i) st128(gelu8(acc[8][2 * i], acc[8][2 * i + 1]), rh, lane * 16, 65536 + (2 * wid + i) * 1024);
      LSTAMP(6);
    }
    {
      constexpr int XP = G2_XP, X0 = G2_X0, WD = G2_WD, WR = WD + 1, TAIL = 4;
      const __amdgpu_buffer_rsrc_t rh = rsrc(ws + WS_HF + (size_t)cl * 8 * HB, 8 * HB);
      const int pr = 4 * ((idx >> 2) & 1) + wid, flh = FL_H + cl * 8;
      u32x4 xf[XP + 1][2];
      auto hload = [&](int k, int i) {
        const int r = k % 9, P = ((idx >> 1) + k / 9) & 3;
        const int so = r < 8 ? (2 * P + (((r >> 2) ^ idx) & 1)) * HB + pr * 8192 + i * 4096 : (2 * P + i) * HB + 65536 + pr * 1024;
        xf[k % (XP + 1)][i] = ld128(rh, lane * 16 + (r < 8 ? (r & 3) * 1024 : 0), so);
      };
      auto publish = [&]() {
        waitv<0>();
        LSTAMP(7);
        if (lane == 0 && lds_add(sm + W_HD) == 3u) flag_set(ws, flh + (idx & 7), tag);
      };
      if (conv) {
        const char LAS* kp = sm + LM_KIM + wid * SLOT + (c & 1) * KIM_C1 + (4 * (c >> 1) + g) * 16;
        char LAS* ub = sm + LM_UIM + wid * UIM_B + 64;
        const char LAS* up[4];
#pragma unroll
        for (int i = 0; i < 4; ++i) {
          const char LAS* q = ub + 2 * (8 * ((c >> 2) - g) + 2 * (c & 3) - 6) + i * 1024;
          asm volatile("" : "+v"(q));
          up[i] = q + 448;
        }
        f32x4 ya[8];
        u32x4 kf[2][8], uf[2][8];
        auto rdk = [&](int be, int a) { kf[be & 1][a] = *(const u32x4 LAS*)(kp + be * 64 + a * 512); };
        auto rdu = [&](int be, int a) {
          const u32 LAS* q = (const u32 LAS*)(up[a >> 1] - be * 64 + (a & 1) * 512);
          uf[be & 1][a] = u32x4{q[0], q[1], q[2], q[3]};
        };
        lds_wait4(sm + W_STG);
        rdk(0, 0);
#pragma unroll
        for (int a = 0; a < 8; ++a) rdu(0, a);
#pragma unroll
        for (int a = 1; a < 8; ++a) rdk(0, a);
        SB;
        u64 fh = 0;
        static_for<0, 288>([&](auto N) {
          constexpr int n = decltype(N)::value, be = n / 36, m = n % 36;
          constexpr int a = m < 8 ? 0 : m < 15 ? 1 : m < 21 ? 2 : m < 26 ? 3 : m < 30 ? 4 : m < 33 ? 5 : m < 35 ? 6 : 7;
          constexpr int T = a + m - (8 * a - a * (a - 1) / 2);
          mma<(n < 8)>(ya[T], uf[be & 1][T - a], kf[be & 1][a]);
          SB;
          if (be < 7 && m < 16 && (m & 1) == 0) rdu(be + 1, m >> 1);
          if (be < 7 && m >= 16 && m < 24) rdk(be + 1, (m - 16) & 7);
          if (n == 35) publish();
          if (n == CV_CHK - 44) fh = __hip_atomic_load((GAS u64*)(ws + (flh + (lane & 7)) * 128), __ATOMIC_RELAXED, __HIP_MEMORY_SCOPE_AGENT);
          if (n == CV_CHK) {
            asm volatile("" : "+v"(fh));
            if (!__all(fh == tag)) flag_wait(ws, flh, 8, tag, lane);
            asm volatile("" ::: "memory");
            LSTAMP(8);
          }
          if (n > CV_CHK && (n - CV_CHK) % 4 == 1 && (n - CV_CHK) / 4 < 2 * X0) hload((n - CV_CHK) / 8, ((n - CV_CHK) / 4) & 1);
          if (n == 252) { waitl<0>(); *(volatile u8 LAS*)cons = 19; }
          SB;
        });
        asm volatile("s_nop 7");
        SB;
        char LAS* yp = ub + 2 * (8 * (g + 4 * (c >> 1)) + 4 * (c & 1));
#pragma unroll
        for (int T = 0; T < 8; ++T) *(u32x2 LAS*)(yp + T * 512) = u32x2{pk(ya[T][0], ya[T][1]), pk(ya[T][2], ya[T][3])};
        waitl<0>();
        if (lane == 0) lds_add(sm + W_OWN);
      } else {
        publish();
        lds_wait4(sm + W_HD);
#pragma unroll
        for (int k = 0; k < 4; ++k) { hload(k, 0); hload(k, 1); }
        flag_wait(ws, flh, 8, tag, lane);
        LSTAMP(8);
#pragma unroll
        for (int k = 4; k < X0; ++k) { hload(k, 0); hload(k, 1); }
      }
      f32x4 acc[9][2];
      const char LAS* wp = sm + (lane >> 2) * 288 + (lane & 3) * 8;
      u32x2 wa[WR], wb[WR];
      constexpr auto ord_ks = [](int n) { return n < 9 * (36 - TAIL) ? n / 9 : 36 - TAIL + (n - 9 * (36 - TAIL)) % TAIL; };
      constexpr auto ord_t = [](int n) { return n < 9 * (36 - TAIL) ? n % 9 : (n - 9 * (36 - TAIL)) / TAIL; };
      const char LAS* wph = wp + 7 * SLOT;
      asm volatile("" : "+v"(wph));
      auto wptr = [&](int n) {
        const int sl = slot2(ord_ks(n));
        return (sl < 7 ? wp + sl * SLOT : wph + (sl - 7) * SLOT) + ord_t(n) * 32;
      };
      ring_wait<28>(pre, land);
      LSTAMP(10);
      RSTAMP(57);
#pragma unroll
      for (int n = 0; n < WD; ++n) { wa[n] = tr_read(wptr(n)); wb[n] = tr_read(wptr(n) + 16 * 288); }
      SB;
      Epi epi;
      const int ft = wid == s ? 8 : 4 + ((wid - s - 1) & 3);
      auto flush = [&](auto OWN, int tt) {
        if (decltype(OWN)::value) {
          *(f32x4 LAS*)(sm + LM_UIM + (2 * tt) * 1024 + lane * 16) = acc[tt][0];
          *(f32x4 LAS*)(sm + LM_UIM + (2 * tt + 1) * 1024 + lane * 16) = acc[tt][1];
          asm volatile("" ::: "memory");
          if (tt == 7) *(volatile u32 LAS*)(sm + W_PART) = 8u;
        } else {
          const u32x4 v = {pk(acc[tt][0][0], acc[tt][0][1]), pk(acc[tt][0][2], acc[tt][0][3]),
                           pk(acc[tt][1][0], acc[tt][1][1]), pk(acc[tt][1][2], acc[tt][1][3])};
          st128(v, rs, lane * 16, (wid * 4 + s) * SLAB_B + tt * 1024);
        }
      };
      auto step = [&](auto N, auto OWN) {
        constexpr int n = decltype(N)::value;
        constexpr int ks = ord_ks(n), t = ord_t(n);
        if (ks % 9 == 8 && t == 0) {
          const u32x4 a = xf[ks % (XP + 1)][0], bb = xf[ks % (XP + 1)][1];
          xf[ks % (XP + 1)][0] = u32x4{a[0], a[1], bb[0], bb[1]};
          xf[ks % (XP + 1)][1] = u32x4{a[2], a[3], bb[2], bb[3]};
          asm volatile("s_nop 1" : "+v"(xf[ks % (XP + 1)][0]), "+v"(xf[ks % (XP + 1)][1]));
        }
        const u32x4 w = {wa[n % WR][0], wa[n % WR][1], wb[n % WR][0], wb[n % WR][1]};
        if (G2_W2 && n % 2 == 0 && n + WD < 324) waitl_seen<2 * WD - 4>();
        if (n < 9 * (36 - TAIL) + TAIL) mma<(n < 9)>(acc[t][0], w, xf[ks % (XP + 1)][0]);
        else acc[t][0] = mfma16(w, xf[ks % (XP + 1)][0], acc[t][0]);
        SB;
        if ((n + WD) % 18 == 0 && n + WD >= 90 && n + WD <= 288) ring_wait<n + WD == 288 ? 54 : 20 + (n + WD) / 9>(pre, land);
        if (n % 18 == 0 && n && n <= 198) *(volatile u8 LAS*)cons = 18 + n / 9;
        if (n + WD < 324) wa[(n + WD) % WR] = tr_read(wptr(n + WD));
        SB;
        if (n < 9 * (36 - TAIL) + TAIL) mma<(n < 9)>(acc[t][1], w, xf[ks % (XP + 1)][1]);
        else acc[t][1] = mfma16(w, xf[ks % (XP + 1)][1], acc[t][1]);
        SB;
        if (n + WD < 324) wb[(n + WD) % WR] = tr_read(wptr(n + WD) + 16 * 288);
        if (n < 9 * (36 - TAIL) && (t & 1) && h_upto(ks - 1) + 1 + (t >> 2) <= h_upto(ks)) hload(h_upto(ks - 1) + 1 + (t >> 2), (t >> 1) & 1);
        if (n == 9 * (36 - TAIL) + TAIL - 1) asm volatile("" ::"v"(pre));
        if (ks == 30 && t == 2) { lds_wait4(sm + W_AOK); load_base(epi, ft); }
        if (ks == 37 - TAIL && t > 0 && n >= 9 * (36 - TAIL)) flush(OWN, t > 0 ? t - 1 : 0);
        SB;
      };
      static_for<0, 9 * (36 - TAIL) + TAIL>([&](auto N) { step(N, std::false_type{}); });
      if (wid == s) {
        static_for<9 * (36 - TAIL) + TAIL, 324>([&](auto N) { step(N, std::true_type{}); });
        LSTAMP(9);
        RSTAMP(58);
        asm volatile("s_nop 15");
        SB;
        finish(std::integral_constant<int, 1>{}, &epi, &ft, &acc[8]);
      } else {
        static_for<9 * (36 - TAIL) + TAIL, 324>([&](auto N) { step(N, std::false_type{}); });
        asm volatile("s_nop 7");
        SB;
        flush(std::false_type{}, 8);
        LSTAMP(9);
        RSTAMP(58);
        waitv<0>();
        if (lane == 0 && lds_add(sm + W_SND) == 2u) flag_set(ws, FL_X + tile * 4 + s, tag);
        LSTAMP(52);
        finish_lds(std::integral_constant<int, 1>{}, &epi, &ft);
      }
    }
  } else {
    const int Q = xg * 18 + idx, p = wid - 4, q = p & 1;
    char LAS* kim = sm + LM_KIM + p * SLOT;
    char LAS* uim = sm + LM_UIM + p * UIM_B;
    glds4(pc0, umin((idx + 32 * (8 * p + (lane & 7))) * 128, code_len), smb + LM_SINK);
    if (conv) {
      const u16* src[6] = {Are, Aim, Bre, Bim, Cre, Cim};
#pragma unroll
      for (int i = 0; i < 6; ++i) glds4(src[i], (4 * Q + p) * (NM * 2) + lane * 4, smb + LM_KIM + p * SLOT + i * 256);
    }
    const u32 vdt = ((const u32*)logdt)[conv ? 2 * Q + (p >> 1) : 0];
    bar();
    u32 v1[5], v2[5], v2s;
    auto freed = [&]() {
      const u32 cs = __builtin_amdgcn_readfirstlane(*(volatile u32 LAS*)(sm + W_CONS));
      u32 m0, m1;
      asm("s_and_b32 %0, %2, 0xff\n\ts_bfe_u32 %1, %2, 0x80008\n\ts_min_u32 %0, %0, %1\n\ts_bfe_u32 %1, %2, 0x80010\n\t"
          "s_min_u32 %0, %0, %1\n\ts_lshr_b32 %1, %2, 24\n\ts_min_u32 %0, %0, %1" : "=&s"(m0), "=&s"(m1) : "s"(cs) : "scc");
      return (int)m0;
    };
    char LAS* myland = sm + W_LAND + p;
    asm volatile("" : "+v"(myland));
    int pub = 0;
    auto room = [&](int j, int need) {
      if (freed() < need) {
        waitv<0>();
        pub = 2 * j;
        *(volatile u8 LAS*)myland = pub;
        while (freed() < need) __builtin_amdgcn_s_sleep(LD_SLEEP);
      }
      asm volatile("" ::: "memory");
    };
    auto landed = [&](int j) {
      waitv<8 * LD_DEPTH>();
      pub = max(pub, 2 * (j + 1 - LD_DEPTH));
      *(volatile u8 LAS*)myland = pub;
    };
    auto stream = [&](int j0, int j1, bool track) {
#pragma unroll 1
      for (int j = j0; j < j1; ++j) {
        if (j >= 5) room(j, 2 * j - 8);
        const char* sp = (const char*)(w1 + fb * 144) + (size_t)j * 64 * F * 2;
        const u32 dst = smb + (j < 5 ? j : j - 5) * 2 * SLOT + q * 5120;
#pragma unroll
        for (int n = 0; n < 8; ++n) glds16(sp + (n >> 2) * 32 * F * 2, v1[n & 3], dst + (n >> 2) * SLOT + (n & 3) * 1024);
        if (q == 0) {
          glds16(sp, v1[4], dst + 4096);
          glds16(sp + 32 * F * 2, v1[4], dst + SLOT + 4096);
        }
        if (track) landed(j);
      }
    };
    auto stream2 = [&]() {
      const u64 lo4 = 0ull | (2ull << 6) | (4ull << 12) | (6ull << 18), hi4 = 9ull | (11ull << 6) | (13ull << 12) | (15ull << 18);
      const u64 tab = ((idx & 1) ? hi4 | (lo4 << 24) : lo4 | (hi4 << 24)) | ((u64)(8 + 8 * q) << 48);
      const char* base = (const char*)(w2 + (size_t)1152 * s * D + 144 * dj);
      int r = 0, P = idx >> 1, ps = 4;
#pragma unroll 1
      for (int j = 9; j < 27; ++j) {
        const bool mine = j < 14 || j >= 14 + RB;
        if (mine) room(j, j < 14 ? 2 * j - 8 : 2 * j - 12);
        const u32 dst = smb + ps * 2 * SLOT + q * 5120;
        ps = (0x4605321 >> (4 * ps)) & 7;
        const char* sp[2];
        bool two[2];
#pragma unroll
        for (int e = 0; e < 2; ++e) {
          sp[e] = base + (u32)(18 * (P & 3) + (int)((tab >> (6 * r)) & 63)) * (16 * D * 2);
          two[e] = r == 8;
          P += two[e];
          r = two[e] ? 0 : r + 1;
        }
        if (!mine) continue;
#pragma unroll
        for (int n = 0; n < 8; ++n) glds16(sp[n >> 2], v2[n & 3], dst + (n >> 2) * SLOT + (n & 3) * 1024);
        if (q == 0) {
          glds16(sp[0], two[0] ? v2s : v2[4], dst + 4096);
          glds16(sp[1], two[1] ? v2s : v2[4], dst + SLOT + 4096);
        }
        landed(j);
      }
      waitv<0>();
      *(volatile u8 LAS*)myland = 54;
    };
    if (p < 2) {
#pragma unroll
      for (int n = 0; n < 5; ++n) {
        const int ci = 64 * min(5 * q + n, 8) + lane;
        v1[n] = (ci / 18) * (F * 2) + (ci % 18) * 16;
        v2[n] = (ci / 18) * (D * 2) + (ci % 18) * 16;
      }
      v2s = v2[4] + (lane >= 32 ? 128 * D * 2 : 0);
      stream(0, G1_N0 / 2, false);
      waitv<4 * G1_N0>();
      if (!conv) {
        waitv<0>();
        pub = G1_N0;
        *(volatile u8 LAS*)myland = pub;
      }
    } else if (conv) {
      const int wv = ((xg >> 2) * 18 + idx) * 2 + q;
#pragma unroll
      for (int i = 0; i < 2; ++i) {
        const int ln = (wv * 2 + i) * 56 + min(lane, 55);
        glds4(w1, ((128 + ln / 18) * F + 1152 * s + (ln % 18) * 64) * 2, smb + LM_SINK);
      }
#pragma unroll
      for (int i = 0; i < 3; ++i)
        if (W2_EARLY) glds4(w2 + (size_t)1152 * s * D, ((wv * 3 + i) * 48 + min(lane, 47)) * 128, smb + LM_SINK);
      waitv<W2_EARLY ? 5 : 2>();
    }
    LSTAMP(0);
    if (conv) {
      const float dt = rb(__expf(q ? bh(vdt) : bl(vdt)));
      u32 par[2][6];
#pragma unroll
      for (int i = 0; i < 12; ++i) par[i & 1][i >> 1] = *(const u16 LAS*)(kim + i * 128 + lane * 2);
      f32x4 ka[2][2];
#pragma unroll
      for (int i = 0; i < 4; ++i) ka[i >> 1][i & 1] = f32x4{0.f, 0.f, 0.f, 0.f};
#pragma unroll 1
      for (int h = 0; h < 2; ++h) {
        float pv[6];
#pragma unroll
        for (int i = 0; i < 6; ++i) pv[i] = bl(h ? par[1][i] : par[0][i]);
        const float p2 = dt * pv[0] * 1.44269504088896340736f;
        const float q = dt * pv[1];
        constexpr float CH = 0.15915494309189535f;
        constexpr float CL = (float)(0.15915494309189533577 - (double)CH);
        const float qh = q * CH;
        const float ql = __builtin_fmaf(q, CL, __builtin_fmaf(q, CH, -qh));
        {
          f32x2 Z[16];
          auto put = [&](int i, f32x2 v) { *(u32 LAS*)(kim + i * KG_S + lane * 4) = pk(v[0], v[1]); };
          Z[0] = f32x2{1.f, 0.f};
          Z[1] = zpow2<3>(p2, qh, ql);
          Z[4] = zpow2<5>(p2, qh, ql);
          const f32x2 a7 = zpow2<7>(p2, qh, ql);
          Z[2] = cmul(Z[1], Z[1]);
          Z[8] = cmul(Z[4], Z[4]);
          Z[3] = cmul(Z[2], Z[1]);
#pragma unroll
          for (int i = 1; i < 4; ++i) Z[4 + i] = cmul(Z[4], Z[i]);
#pragma unroll
          for (int i = 1; i < 8; ++i) Z[8 + i] = cmul(Z[8], Z[i]);
#pragma unroll
          for (int i = 0; i < 16; ++i) put(i, Z[i]);
          put(16, a7);
#pragma unroll
          for (int i = 1; i < 16; ++i) put(16 + i, cmul(a7, Z[i]));
        }
        if (p < 2) {
          waitv<0>();
          pub = h ? 10 : G1_N0;
          *(volatile u8 LAS*)myland = pub;
          if (h == 0) stream(G1_N0 / 2, 5, false);
        }
        {
          f32x2 H[16];
          auto put = [&](int i, f32x2 v) { *(u32 LAS*)(uim + i * KG_S + lane * 4) = pk(v[0], -v[1]); };
          H[0] = cmul(f32x2{pv[4], pv[5]}, f32x2{fmul(dt, pv[2]), fmul(dt, pv[3])});
          const f32x2 a0 = zpow2<0>(p2, qh, ql), a8 = zpow2<8>(p2, qh, ql), a10 = zpow2<10>(p2, qh, ql);
          const f32x2 a1 = cmul(a0, a0), a2 = cmul(a1, a1), a9 = cmul(a8, a8), a11 = cmul(a10, a10);
          H[1] = cmul(a0, H[0]);
          H[2] = cmul(a1, H[0]);
          H[3] = cmul(a1, H[1]);
#pragma unroll
          for (int i = 0; i < 4; ++i) H[4 + i] = cmul(a2, H[i]);
#pragma unroll
          for (int i = 0; i < 4; ++i) H[8 + i] = cmul(a8, H[2 * i]);
          H[12] = cmul(a9, H[0]);
          H[13] = cmul(a9, H[4]);
          H[14] = cmul(a9, H[8]);
          H[15] = cmul(a9, H[10]);
#pragma unroll
          for (int i = 0; i < 16; ++i) put(i, H[i]);
          put(16, cmul(a10, H[0]));
          put(17, cmul(a10, H[8]));
          put(18, cmul(a10, H[12]));
          put(19, cmul(a10, H[14]));
          put(20, cmul(a11, H[0]));
        }
        u32x4 lo[4][2], hi[4][2];
#pragma unroll
        for (int k = 0; k < 4; ++k)
#pragma unroll
          for (int i = 0; i < 2; ++i) {
            lo[k][i] = *(const u32x4 LAS*)(kim + (16 * i + c) * KG_S + k * 64 + g * 16);
            hi[k][i] = *(const u32x4 LAS*)(uim + min(16 * i + c, 20) * KG_S + k * 64 + g * 16);
          }
        SB;
#pragma unroll
        for (int k = 0; k < 4; ++k)
#pragma unroll
          for (int i = 0; i < 4; ++i) ka[i >> 1][i & 1] = mfma16(lo[k][i & 1], hi[k][i >> 1], ka[i >> 1][i & 1]);
      }
#pragma unroll
      for (int i = 0; i < 4; ++i)
        *(u32x2 LAS*)(uim + (16 * (i >> 1) + c) * 64 + (i & 1) * 32 + g * 8) =
            u32x2{pk(ka[i >> 1][i & 1][0], ka[i >> 1][i & 1][1]), pk(ka[i >> 1][i & 1][2], ka[i >> 1][i & 1][3])};
      waitl<0>();
      if (lane == 0) lds_add(sm + W_GEN);
      LSTAMP(1);
    }
    lds_wait4<4>(sm + W_LND);
    tag = *(const u64 LAS*)(sm + W_TAG);
    if (p < 2) {
      __builtin_amdgcn_s_setprio(LD_PRIO);
      stream(conv ? 5 : G1_N0 / 2, 9, true);
      stream2();
      __builtin_amdgcn_s_setprio(0);
      LSTAMP(26);
    } else {
      u32x4 wreg[RB][9];
      auto wfetch = [&]() {
        while (freed() < RB_AT) __builtin_amdgcn_s_sleep(8);
        const __amdgpu_buffer_rsrc_t rw = rsrc(w2 + (size_t)1152 * s * D + 144 * dj, 1152 * D * 2 - 288 * dj);
#pragma unroll
        for (int n = 0; n < 9; ++n) {
          const int ci = 64 * n + lane, vo = (ci / 18) * (D * 2) + (ci % 18) * 16;
#pragma unroll
          for (int i = 0; i < RB; ++i) {
            const int k = 10 + 2 * i + q, r = k % 9;
            const int row = 288 * (((idx >> 1) + k / 9) & 3) + (r < 8 ? 144 * (((r >> 2) ^ idx) & 1) + 32 * (r & 3) : 128);
            wreg[i][n] = __builtin_amdgcn_raw_buffer_load_b128(rw, vo + (r == 8 && ci >= 288 ? 128 * D * 2 : 0), row * (D * 2), 0);
            __builtin_amdgcn_s_sleep(RB_SLEEP);
          }
        }
      };
      auto wput = [&](int i0, int i1) {
#pragma unroll
        for (int i = i0; i < i1; ++i) {
          while (freed() < max(2 * (14 + i) - 12, 19)) __builtin_amdgcn_s_sleep(LD_SLEEP);
          asm volatile("" ::: "memory");
          char LAS* dst = sm + (slot2(10 + 2 * i) + q) * SLOT + lane * 16;
#pragma unroll
          for (int n = 0; n < 9; ++n) *(u32x4 LAS*)(dst + n * 1024) = wreg[i][n];
          waitl<0>();
          *(volatile u8 LAS*)myland = i == RB - 1 ? 54 : 30 + 2 * i;
        }
      };
      if (conv) {
        lds_wait4<4>(sm + W_GEN);
#pragma unroll 1
        for (int j = 0; j < 2; ++j) {
          const char LAS* tb = sm + LM_UIM + (2 * q + j) * UIM_B;
          char LAS* ki = sm + LM_KIM + (2 * q + j) * SLOT;
          char LAS* ko = ki + (28 + lane) * 16;
          auto put = [&](int mi, const u32(&kv)[9]) {
            *(u32x4 LAS*)(ko + mi * 1024) = u32x4{kv[1] | (kv[2] << 16), kv[3] | (kv[4] << 16), kv[5] | (kv[6] << 16), kv[7] | (kv[8] << 16)};
            *(u32x4 LAS*)(ko + mi * 1024 + KIM_C1) = u32x4{kv[0] | (kv[1] << 16), kv[2] | (kv[3] << 16), kv[4] | (kv[5] << 16), kv[6] | (kv[7] << 16)};
          };
          {
            u32 kv[9];
#pragma unroll
            for (int e = 0; e < 9; ++e) {
              constexpr int EX[9] = {448, 384, 320, 256, 192, 128, 64, 0, 446}, R2[9] = {450, 640, 576, 576, 576, 512, 448, 448, 448};
              int o = 2 * lane + (lane < (e == 8 ? 33 : 32) ? EX[e] : R2[e]);
              if (e == 0 && lane == 63) o = 768;
              kv[e] = *(const u16 LAS*)(tb + o);
            }
            if (lane == 0) kv[8] = 0u;
            put(0, kv);
          }
          if (lane < 56) *(u32x4 LAS*)(ki + (lane & 1) * KIM_C1 + (lane >> 1) * 16) = u32x4{0u, 0u, 0u, 0u};
          {
            const int oa = 768 + 128 * (lane >> 5) + 2 * (lane & 31), oe = 768 + 128 * ((lane + 1) >> 5) + 2 * ((lane + 1) & 31);
            const u32 a = *(const u16 LAS*)(tb + oa), bb = *(const u16 LAS*)(tb + oa + 64), e = *(const u16 LAS*)(tb + oe);
            const u32 kv[9] = {e, e, bb, bb, bb, a, a, a, a};
            put(1, kv);
          }
#pragma unroll
          for (int mi = 2; mi < 4; ++mi) {
            const u32 a = *(const u16 LAS*)(tb + 768 + 128 * mi + 2 * lane), e = *(const u16 LAS*)(tb + 770 + 128 * mi + 2 * lane);
            const u32 tie = (lane & 1) ? e : a;
            const u32 kv[9] = {e, e, e, tie, a, a, a, a, a};
            put(mi, kv);
          }
        }
        waitl<0>();
        if (lane < 2) lds_add(sm + W_UT);
        LSTAMP(2);
        while (freed() < U_AT) __builtin_amdgcn_s_sleep(8);
#pragma unroll 1
        for (u32 spin = 0; spin < (1u << 20); ++spin) {
          bool ok = true;
#pragma unroll
          for (int e = 0; e < 4; ++e)
            ok &= __hip_atomic_load((GAS u64*)(ws + FL_LN2 * 128) + e * 64 + lane, __ATOMIC_RELAXED, __HIP_MEMORY_SCOPE_AGENT) == tag;
          if (__all(ok)) break;
          __builtin_amdgcn_s_sleep(32);
        }
        asm volatile("" ::: "memory");
        u32x4 uv[8];
#pragma unroll
        for (int i = 0; i < 8; ++i) {
          uv[i] = ld128(rsrc(ws + WS_UT, L * D * 2), (64 * q + (lane >> 3)) * 18432 + Q * 128 + (lane & 7) * 16, i * 8 * 18432);
          __builtin_amdgcn_s_sleep(2);
        }
        const u32x2 dk = *(const u32x2*)(Dskip + 4 * Q);
        lds_wait4<4>(sm + W_UT);
#pragma unroll
        for (int e = 0; e < 8; ++e)
          *(u32x4 LAS*)(sm + LM_UIM + ((lane >> 1) & 3) * UIM_B + 64 + (64 * q + 8 * e + (lane >> 3)) * 32 + (lane & 1) * 16) = uv[e];
        if (lane < 16) *(u32x4 LAS*)(sm + LM_UIM + (lane >> 2) * UIM_B + (lane & 3) * 16) = u32x4{0u, 0u, 0u, 0u};
        const char LAS* tp = sm + LM_UIM + (c >> 2) * UIM_B + 64 + 2 * (1024 * q + 32 * g) + (c & 3) * 8;
        const int cp = (c & 8) + 2 * (c & 3) + ((c >> 2) & 1);
        u32x2 sk[16];
#pragma unroll
        for (int i = 0; i < 16; ++i) {
          const u32x2 a = tr_read(tp + (i >> 1) * 256 + (i & 1) * 32);
#pragma unroll
          for (int e = 0; e < 2; ++e)
            sk[i][e] = (u32)__builtin_amdgcn_ds_bpermute((16 * g + cp) * 4, (int)pk(fmul(bl(a[e]), bl(dk[e])), fmul(bh(a[e]), bh(dk[e]))));
        }
        waitl<0>();
        if (lane < 2) lds_add(sm + W_STG);
        LSTAMP(3);
        wfetch();
        LSTAMP(5);
        wput(0, 2);
        lds_wait4<4>(sm + W_OWN);
        const __amdgpu_buffer_rsrc_t ao = rsrc(ws + WS_A2 + (size_t)Q * 16384 + 8192 * q, 8192);
#pragma unroll
        for (int i = 0; i < 8; ++i) {
          const u32x2 a = tr_read(tp + i * 256), bb = tr_read(tp + i * 256 + 32);
          st128(u32x4{add2(a[0], sk[2 * i][0]), add2(a[1], sk[2 * i][1]), add2(bb[0], sk[2 * i + 1][0]), add2(bb[1], sk[2 * i + 1][1])}, ao,
                (16 * g + cp) * 16, i * 1024);
        }
        wput(2, RB);
        waitv<0>();
        if (lane == 0 && lds_add(sm + W_A2) == 1u) flag_set(ws, FL_A + Q, tag);
        LSTAMP(4);
      } else {
#pragma unroll 1
        for (int j = 0; j < nln; ++j) {
          const __amdgpu_buffer_rsrc_t uo = rsrc(ws + WS_UT + (size_t)(lt - 2 * j) * 18432, 18432);
          const char LAS* tp = sm + LM_LN + (16 * j + 8 * (g & 1) + (c >> 2)) * LN_S + q * 64 + (g >> 1) * 32 + (c & 3) * 8;
#pragma unroll
          for (int kk = 0; kk < 9; ++kk) {
            const u32x2 t0 = tr_read(tp + kk * 128), t1 = tr_read(tp + kk * 128 + 4 * LN_S);
            st128(u32x4{t0[0], t0[1], t1[0], t1[1]}, uo, (16 * (g >> 1) + c) * 32 + (g & 1) * 16, (q + 2 * kk) * 1024);
          }
        }
        waitv<0>();
        if (lane == 0 && lds_add(sm + W_A2) == 1u) flag_set(ws + b * 8, FL_LN2, tag);
        if (lane < 2) lds_add(sm + W_UT);
        if (!W2_EARLY) {
          lds_wait4<4>(sm + W_XGO);
          __builtin_amdgcn_s_sleep(W2_SLEEP);
#pragma unroll
          for (int i = 0; i < 3; ++i)
            glds4(w2 + (size_t)1152 * s * D, min((((((xg >> 2) * 14 + idx - 18) * 2 + q) * 3 + i) * 62 + min(lane, 61)), 1152 * 9 - 1) * 128, smb + LM_SINK);
        }
        wfetch();
        LSTAMP(5);
        lds_wait4<4>(sm + W_UT);
        wput(0, RB);
      }
      if (q) {
        while (freed() < 40) __builtin_amdgcn_s_sleep(8);
        flag_wait(ws, FL_A + dj * 36, 36, tag, lane);
      }
      if (lane < 2) lds_add(sm + W_AOK);
    }
    lds_wait4<4>(sm + W_AOK);
    if (tid == 256 && b == 0) flag_set(ws, FL_EPOCH, tag);
    Epi epi[2];
    load_base(epi[0], p);
    if (p == 0) load_base(epi[1], 7);
    const int ft[2] = {p, 7};
    if (p == 0) finish_lds(std::integral_constant<int, 2>{}, epi, ft);
    asm volatile(".Lk_all_tail:");
    if (p != 0) finish_lds(std::integral_constant<int, 1>{}, epi, ft);
  }
  LSTAMP(54);
  RSTAMP(59);
}

extern "C" void kernel_launch(void* const* d_in, const int* in_sizes, int n_in, void* d_out, int out_size, void* d_ws,
                              size_t ws_size, hipStream_t stream) {
  const u16* const* in = (const u16* const*)d_in;
  (void)hipFuncSetAttribute((const void*)k_all, hipFuncAttributeMaxDynamicSharedMemorySize, LM_END);
  k_all<<<256, 512, LM_END, stream>>>(in[0], in[1], in[2], in[3], in[4], in[5], in[6], in[7], in[8], in[10], in[11], in[12], in[13],
                                      (char*)d_ws, (u16*)d_out);
}
```

```cpp
#include <hip/hip_runtime.h>
#include <hip/hip_bf16.h>
#include <stdint.h>
#include <type_traits>

typedef unsigned char u8;
typedef unsigned short u16;
typedef unsigned int u32;
typedef unsigned long long u64;
typedef __bf16 bf16x8 __attribute__((ext_vector_type(8)));
typedef __bf16 bf16x2 __attribute__((ext_vector_type(2)));
typedef short s16x4 __attribute__((ext_vector_type(4)));
typedef float f32x4 __attribute__((ext_vector_type(4)));
typedef float f32x2 __attribute__((ext_vector_type(2)));
typedef u32 u32x4 __attribute__((ext_vector_type(4)));
typedef u32 u32x2 __attribute__((ext_vector_type(2)));
#define LAS __attribute__((address_space(3)))
#define GAS __attribute__((address_space(1)))
#define SB __builtin_amdgcn_sched_barrier(0)

constexpr int L = 2048, D = 576, NM = 128, F = 4608;

constexpr int POLL_SLEEP = 4, LD_SLEEP = 2, LN_SLEEP = 32, W2_SLEEP = 16, RB_SLEEP = 2;
constexpr int G1_N0 = 4, LD_DEPTH = 2, U_AT = 8, RB = 3, RB_AT = 18, RB_AT2 = 10, A_AT = 38, CV_PUB = 46, CV_CHK = 215;
constexpr int G2_WD = 6, G2_XP = 15, G2_X0 = 8;

constexpr int FL_EPOCH = 0;
constexpr int FL_LN = 1;
constexpr int FL_A = FL_LN + 128;
constexpr int FL_H = FL_A + 144;
constexpr int FL_X = FL_H + 256;
constexpr int FL_LN2 = FL_X + 256;
constexpr size_t WS_XNF = 131072;
constexpr size_t WS_UT = WS_XNF + (size_t)L * D * 2;
constexpr size_t WS_A2 = WS_UT + (size_t)L * D * 2;
constexpr size_t WS_HF = WS_A2 + (size_t)L * D * 2;
constexpr size_t WS_SLAB = WS_HF + (size_t)L * F * 2;
constexpr int SLAB_B = 9216, HB = 73728;
constexpr size_t WS_END = WS_SLAB + (size_t)64 * 16 * SLAB_B;
static_assert((FL_LN2 + 16) * 128 <= WS_XNF && WS_END <= (256u << 20), "workspace map");

__device__ __forceinline__ float bl(u32 v) { return __uint_as_float(v << 16); }
__device__ __forceinline__ float bh(u32 v) { return __uint_as_float(v & 0xffff0000u); }
__device__ __forceinline__ u32 pk(float lo, float hi) {
  bf16x2 r = {(__bf16)lo, (__bf16)hi};
  return __builtin_bit_cast(u32, r);
}
__device__ __forceinline__ u32 add2(u32 a, u32 b) {
  u32 r, t0, t1;
  asm("v_lshlrev_b32 %0, 16, %3\n\tv_lshlrev_b32 %1, 16, %4\n\tv_add_f32 %0, %0, %1\n\tv_and_b32 %1, 0xffff0000, %3\n\t"
      "v_and_b32 %2, 0xffff0000, %4\n\tv_add_f32 %1, %1, %2\n\tv_cvt_pk_bf16_f32 %0, %0, %1"
      : "=&v"(r), "=&v"(t0), "=&v"(t1) : "v"(a), "v"(b));
  return r;
}
__device__ __forceinline__ u32 sum4(float lo, float hi, u32 a, u32 b, u32 c) {
  u32 r, t0, t1;
  asm("v_lshlrev_b32 %0, 16, %5\n\tv_and_b32 %1, 0xffff0000, %5\n\tv_add_f32 %0, %3, %0\n\tv_add_f32 %1, %4, %1\n\t"
      "v_lshlrev_b32 %2, 16, %6\n\tv_add_f32 %0, %0, %2\n\tv_and_b32 %2, 0xffff0000, %6\n\tv_add_f32 %1, %1, %2\n\t"
      "v_lshlrev_b32 %2, 16, %7\n\tv_add_f32 %0, %0, %2\n\tv_and_b32 %2, 0xffff0000, %7\n\tv_add_f32 %1, %1, %2\n\t"
      "v_cvt_pk_bf16_f32 %0, %0, %1"
      : "=&v"(r), "=&v"(t0), "=&v"(t1) : "v"(lo), "v"(hi), "v"(a), "v"(b), "v"(c));
  return r;
}
__device__ __forceinline__ float rb(float f) { return (float)(__bf16)f; }
__device__ __forceinline__ f32x4 mfma16(u32x4 a, u32x4 b, f32x4 c) {
  return __builtin_amdgcn_mfma_f32_16x16x32_bf16(__builtin_bit_cast(bf16x8, a), __builtin_bit_cast(bf16x8, b), c, 0, 0, 0);
}
template <bool FIRST = false> __device__ __forceinline__ void mma(f32x4& c, u32x4 a, u32x4 b) {
  if (FIRST) asm volatile("v_mfma_f32_16x16x32_bf16 %0, %1, %2, 0" : "=&v"(c) : "v"(a), "v"(b));
  else asm volatile("v_mfma_f32_16x16x32_bf16 %0, %1, %2, %0" : "+v"(c) : "v"(a), "v"(b));
}
template <int N> __device__ __forceinline__ void waitl_seen() { __builtin_amdgcn_s_waitcnt(0xC07F | (N << 8)); }
__device__ __forceinline__ u32x2 tr_read(const char LAS* p) {
  return __builtin_bit_cast(u32x2, __builtin_amdgcn_ds_read_tr16_b64_v4i16((s16x4 LAS*)p));
}
__device__ __forceinline__ __amdgpu_buffer_rsrc_t rsrc(const void* p, int bytes) {
  return __builtin_amdgcn_make_buffer_rsrc((void*)p, 0, bytes, 0x00020000);
}
constexpr int SC1 = 16;
__device__ __forceinline__ u32 umin(u32 a, u32 b) { return a < b ? a : b; }

__device__ __forceinline__ void st128(u32x4 v, __amdgpu_buffer_rsrc_t r, int voff, int soff) {
  __builtin_amdgcn_raw_buffer_store_b128(v, r, voff, soff, SC1);
  asm volatile("s_nop 1" ::"v"(v) : "memory");
}
__device__ __forceinline__ u32x4 ld128(__amdgpu_buffer_rsrc_t r, int voff, int soff) {
  return __builtin_amdgcn_raw_buffer_load_b128(r, voff, soff, SC1);
}
__device__ __forceinline__ void glds16x4(const char* base, const u32* v, u32 dst) {
  asm volatile("s_mov_b32 m0, %5\n\ts_nop 0\n\tglobal_load_lds_dwordx4 %0, %4\n\tglobal_load_lds_dwordx4 %1, %4 offset:1024\n\t"
               "global_load_lds_dwordx4 %2, %4 offset:2048\n\tglobal_load_lds_dwordx4 %3, %4 offset:3072"
               ::"v"(v[0]), "v"(v[1]), "v"(v[2]), "v"(v[3]), "s"(base), "s"(dst) : "memory");
}
__device__ __forceinline__ void glds16(const char* base, u32 voff, u32 dst) {
  asm volatile("s_mov_b32 m0, %2\n\ts_nop 0\n\tglobal_load_lds_dwordx4 %0, %1" ::"v"(voff), "s"(base), "s"(dst) : "memory");
}
__device__ __forceinline__ void glds4(const void* base, u32 voff, u32 dst) {
  asm volatile("s_mov_b32 m0, %2\n\ts_nop 0\n\tglobal_load_lds_dword %0, %1" ::"v"(voff), "s"(base), "s"(dst) : "memory");
}
template <int N> __device__ __forceinline__ void waitv() { asm volatile("s_waitcnt vmcnt(%0)" ::"i"(N) : "memory"); }
template <int N> __device__ __forceinline__ void waitl() { asm volatile("s_waitcnt lgkmcnt(%0)" ::"i"(N) : "memory"); }
__device__ __forceinline__ void bar() {
  asm volatile("" ::: "memory");
  __builtin_amdgcn_s_barrier();
  asm volatile("" ::: "memory");
}
template <int I, int N, class Fn> __device__ __forceinline__ void static_for(Fn&& fn) {
  if constexpr (I < N) {
    fn(std::integral_constant<int, I>{});
    static_for<I + 1, N>(fn);
  }
}
__device__ __forceinline__ void flag_set(char* ws, int off, u64 tag) {
  __hip_atomic_store((GAS u64*)(ws + off * 128), tag, __ATOMIC_RELAXED, __HIP_MEMORY_SCOPE_AGENT);
}
template <int SLEEP = POLL_SLEEP> __device__ __forceinline__ void flag_wait(char* ws, int off, int n, u64 tag, int lane) {
  GAS u64* p = (GAS u64*)(ws + off * 128 + min(lane, n - 1) * 128);
#pragma unroll 1
  for (u32 spin = 0; spin < (1u << 20); ++spin) {
    if (__all(__hip_atomic_load(p, __ATOMIC_RELAXED, __HIP_MEMORY_SCOPE_AGENT) == tag)) break;
    __builtin_amdgcn_s_sleep(SLEEP);
  }
  asm volatile("" ::: "memory");
}
__device__ __forceinline__ u32 lds_add(char LAS* p) {
  return __hip_atomic_fetch_add((u32 LAS*)p, 1u, __ATOMIC_RELAXED, __HIP_MEMORY_SCOPE_WORKGROUP);
}
__device__ __forceinline__ void lds_inc(char LAS* p) {
  u64 sv;
  asm volatile("s_mov_b64 %0, exec\n\ts_mov_b64 exec, 1\n\tds_add_u32 %1, %2\n\ts_mov_b64 exec, %0" : "=&s"(sv) : "v"(p), "v"(1u) : "memory");
}
template <int SLEEP = 1> __device__ __forceinline__ void lds_wait4(char LAS* p) {
  u32 v, sv;
  asm volatile(
      "1:\n\tds_read_b32 %0, %2\n\ts_waitcnt lgkmcnt(0)\n\tv_readfirstlane_b32 %1, %0\n\ts_cmp_lt_u32 %1, 4\n\ts_cbranch_scc0 2f\n\t"
      "s_sleep %3\n\ts_branch 1b\n2:"
      : "=&v"(v), "=&s"(sv) : "v"(p), "i"(SLEEP) : "memory", "scc");
}

template <int NEED> __device__ __forceinline__ void ring_wait(u32& seen, char LAS* p) {
  u32 s0;
  asm volatile(
      "1:\n\tv_readfirstlane_b32 %1, %0\n\tds_read_b32 %0, %2\n\ts_add_u32 %1, %1, %3\n\ts_andn2_b32 %1, 0x80808080, %1\n\ts_cbranch_scc1 1b"
      : "+v"(seen), "=&s"(s0) : "v"(p), "i"((128 - NEED) * 0x01010101) : "memory", "scc");
}
__device__ __forceinline__ float fmul(float a, float b) {
  float r;
  asm("v_mul_f32 %0, %1, %2" : "=v"(r) : "v"(a), "v"(b));
  return r;
}
__device__ __forceinline__ f32x2 cmul(f32x2 a, f32x2 b) {
  float re, im;
  asm("v_mul_f32 %0, %2, %4\n\tv_mul_f32 %1, %2, %5\n\tv_fma_f32 %0, -%3, %5, %0\n\tv_fma_f32 %1, %3, %4, %1"
      : "=&v"(re), "=&v"(im) : "v"(a[0]), "v"(a[1]), "v"(b[0]), "v"(b[1]));
  return f32x2{re, im};
}
template <int E> __device__ __forceinline__ f32x2 zpow2(float p2, float qh, float ql) {
  constexpr float s = (float)(1 << E);
  const float mag = __builtin_amdgcn_exp2f(p2 * s);
  const float ph = __builtin_fmaf(ql, s, __builtin_amdgcn_fractf(qh * s));
  float re, im;
  asm("s_nop 1\n\tv_mul_f32 %0, %2, %4\n\tv_mul_f32 %1, %3, %4"
      : "=&v"(re), "=&v"(im) : "v"(__builtin_amdgcn_cosf(ph)), "v"(__builtin_amdgcn_sinf(ph)), "v"(mag));
  return f32x2{re, im};
}
template <int CTRL> __device__ __forceinline__ float dpp_add(float v) {
  return v + __builtin_bit_cast(float, __builtin_amdgcn_update_dpp(0, __builtin_bit_cast(int, v), CTRL, 0xF, 0xF, true));
}
__device__ __forceinline__ float sum16(float v) { return dpp_add<0x121>(dpp_add<0x122>(dpp_add<0x124>(dpp_add<0x128>(v)))); }
__device__ __forceinline__ u32 gelu2(float a, float b) {
  constexpr float C0 = -2.f * 1.44269504088896340736f * 0.7978845608028654f, C1 = C0 * 0.044715f;
  const f32x2 v = {a, b};
  const f32x2 z = v * __builtin_elementwise_fma(v * v, f32x2{C1, C1}, f32x2{C0, C0});
  const f32x2 d = f32x2{__builtin_amdgcn_exp2f(z[0]), __builtin_amdgcn_exp2f(z[1])} + f32x2{1.f, 1.f};
  const f32x2 r = v * f32x2{__builtin_amdgcn_rcpf(d[0]), __builtin_amdgcn_rcpf(d[1])};
  return pk(r[0], r[1]);
}
__device__ __forceinline__ u32x4 gelu8(f32x4 a, f32x4 b) {
  return u32x4{gelu2(a[0], a[1]), gelu2(a[2], a[3]), gelu2(b[0], b[1]), gelu2(b[2], b[3])};
}

constexpr int SLOT = 9216;
constexpr int KIM_C1 = 4640, UIM_B = 5952, KG_S = 272, LN_S = 1168;
constexpr int LM_KIM = 10 * SLOT;
constexpr int LM_UIM = 14 * SLOT;
constexpr int LM_LN = LM_KIM;
constexpr int LM_CNT = LM_UIM + 4 * UIM_B;
constexpr int W_CONS = LM_CNT, W_HOK = LM_CNT + 4, W_XOK = LM_CNT + 8, W_LNT2 = LM_CNT + 12, W_LAND = LM_CNT + 16, W_LND2 = LM_CNT + 20,
              W_TAG = LM_CNT + 24, W_LNT = LM_CNT + 32, W_LND = LM_CNT + 36, W_HD = LM_CNT + 40, W_UT = LM_CNT + 44, W_GEN = LM_CNT + 48,
              W_STG = LM_CNT + 52, W_OWN = LM_CNT + 56, W_A2 = LM_CNT + 60, W_AOK = LM_CNT + 64, W_PART = LM_CNT + 68,
              W_SND = LM_CNT + 72, W_XGO = LM_CNT + 76;
constexpr int LM_SINK = LM_CNT + 80;
constexpr int LM_END = LM_SINK + 256;
static_assert(LM_END <= 163840 && 4 * UIM_B >= 18432 && 21 * KG_S <= UIM_B && 32 * KG_S <= SLOT && UIM_B % 256 == 64, "LDS map");
__host__ __device__ constexpr int slot2(int j) { return j % 14 < 10 ? (j % 14 + 8) % 10 : j % 14; }

constexpr int h_upto(int ks) {
  const int kn = G2_XP - G2_X0 - 1;
  const int v = ks <= kn ? G2_X0 + 1 + 2 * ks : G2_X0 + 1 + 2 * kn + (ks - kn) * 2 / 3;
  return v < 35 ? v : 35;
}

__global__ __launch_bounds__(512, 2) void k_all(const u16* __restrict__ x, const u16* __restrict__ Are, const u16* __restrict__ Aim,
                                                const u16* __restrict__ Bre, const u16* __restrict__ Bim, const u16* __restrict__ Cre,
                                                const u16* __restrict__ Cim, const u16* __restrict__ logdt, const u16* __restrict__ Dskip,
                                                const u16* __restrict__ w1, const u16* __restrict__ b1, const u16* __restrict__ w2,
                                                const u16* __restrict__ b2, char* ws, u16* __restrict__ out) {
  extern __shared__ __attribute__((aligned(16))) char smem[];
  asm volatile("" ::"s"(x), "s"(Are), "s"(Aim), "s"(Bre), "s"(Bim), "s"(Cre), "s"(Cim), "s"(logdt), "s"(Dskip), "s"(w1), "s"(b1), "s"(w2),
               "s"(b2), "s"(ws), "s"(out));
  char LAS* sm = (char LAS*)smem;
  const u32 smb = (u32)(uintptr_t)sm;
  const int tid = threadIdx.x, lane = tid & 63, wid = __builtin_amdgcn_readfirstlane(tid >> 6);
  const int b = blockIdx.x, xg = b & 7, idx = b >> 3, s = xg & 3;
  const int lb = 4 * (xg >> 2) + (idx >> 3), fb = 8 * s + (idx & 7);
  const int l2 = 8 * (xg >> 2) + (idx >> 2), dj = idx & 3, tile = l2 * 4 + dj;
  const int cl = xg * 4 + (idx >> 3);
  const bool conv = idx < 18;
  const int nln = conv ? 0 : idx >= 30 ? 2 : 1, j1 = xg * 12 + idx - 18;
  const int lt = idx >= 30 ? (xg >> 2) * 64 + 48 + (xg & 3) * 4 + (idx & 1) + 2 : j1 + (j1 >= 48 ? 16 : 0);
  const int c = lane & 15, g = lane >> 4;
  const char* pc0;
  u32 code_len;
  asm volatile("s_getpc_b64 %0\n.Lk_all_head:\n\ts_mov_b32 %1, .Lk_all_tail-.Lk_all_head" : "=s"(pc0), "=s"(code_len));
  u64 tag;
  const __amdgpu_buffer_rsrc_t rs = rsrc(ws + WS_SLAB + (size_t)tile * 16 * SLAB_B, 16 * SLAB_B);
  struct Epi { u32x2 xv[2], bias; u32x4 av; };
  const int xo = (c * D + 4 * g) * 2, ao = g * 16384 + c * 16;
  auto load_base = [&](Epi& e, int t) {
    const int d = 144 * dj + 16 * t, l = 128 * l2 + 32 * s;
    e.bias = __builtin_amdgcn_raw_buffer_load_b64(rsrc(b2, D * 2), g * 8, d * 2, 0);
    e.av = ld128(rsrc(ws + WS_A2, L * D * 2), ao, d * 4096 + (4 * l2 + s) * 256);
    e.xv[0] = __builtin_amdgcn_raw_buffer_load_b64(rsrc(x, L * D * 2), xo, (l * D + d) * 2, 0);
    e.xv[1] = __builtin_amdgcn_raw_buffer_load_b64(rsrc(x, L * D * 2), xo, ((l + 16) * D + d) * 2, 0);
  };
  auto finish = [&](auto NT, const Epi* e, const int* t, const f32x4 (*own)[2]) {
    constexpr int nt = decltype(NT)::value;
    u32 base[nt][4];
#pragma unroll
    for (int j = 0; j < nt; ++j)
#pragma unroll
      for (int i = 0; i < 4; ++i) base[j][i] = add2(e[j].xv[i >> 1][i & 1], e[j].av[i]);
    flag_wait<1>(ws, FL_X + tile * 4, 4, tag, (lane & 3) == s ? s ^ 1 : lane & 3);
    u32x4 part[nt][3];
#pragma unroll
    for (int j = 0; j < nt; ++j)
#pragma unroll
      for (int k = 0; k < 3; ++k) part[j][k] = ld128(rs, lane * 16, (s * 4 + ((s + 1 + k) & 3)) * SLAB_B + t[j] * 1024);
#pragma unroll
    for (int j = 0; j < nt; ++j) {
#pragma unroll
      for (int i = 0; i < 2; ++i) {
        u32 o[2];
#pragma unroll
        for (int h = 0; h < 2; ++h) {
          const u32 r = sum4(own[j][i][2 * h], own[j][i][2 * h + 1], part[j][0][2 * i + h], part[j][1][2 * i + h], part[j][2][2 * i + h]);
          o[h] = add2(base[j][2 * i + h], add2(r, e[j].bias[h]));
        }
        __builtin_amdgcn_raw_buffer_store_b64(u32x2{o[0], o[1]}, rsrc(out, L * D * 2), xo, ((128 * l2 + 32 * s + 16 * i) * D + 144 * dj + 16 * t[j]) * 2, 0);
      }
    }
  };
  auto finish_lds = [&](auto NT, const Epi* e, const int* t) {
    constexpr int nt = decltype(NT)::value;
    while (__builtin_amdgcn_readfirstlane(*(volatile u32 LAS*)(sm + W_PART)) < 8u) __builtin_amdgcn_s_sleep(2);
    asm volatile("" ::: "memory");
    f32x4 own[nt][2];
#pragma unroll
    for (int j = 0; j < nt; ++j)
#pragma unroll
      for (int i = 0; i < 2; ++i) own[j][i] = *(const f32x4 LAS*)(sm + LM_UIM + (2 * t[j] + i) * 1024 + lane * 16);
    finish(NT, e, t, own);
  };
  const int team = wid >> 2;
  u32x2 raw[9];
  if (team < nln) {
    const u32x2* src = (const u32x2*)(x + (size_t)((lt - 2 * team) * 16 + ((tid >> 4) & 15)) * D) + c;
#pragma unroll
    for (int i = 0; i < 9; ++i) raw[i] = src[16 * i];
  }
  if (team) glds4(pc0, umin((idx + 32 * (8 * (wid & 3) + (lane & 7))) * 128, code_len), smb + LM_SINK);
  const bool genw = conv && ((wid + 2) & 7) < 4;
  const int ch = 4 * (xg * 18 + idx) + (wid & 3);
  if (genw) {
    const u16* src[6] = {Are, Aim, Bre, Bim, Cre, Cim};
#pragma unroll
    for (int i = 0; i < 6; ++i) glds4(src[i], ch * (NM * 2) + lane * 4, smb + LM_KIM + (wid & 3) * SLOT + i * 256);
  }
  u32 vdt = ((const u32*)logdt)[conv ? ch >> 1 : 0];
  u64 ep = 0;
  u32x2 bq[9];
  if (wid < 4) {
    ep = __hip_atomic_load((GAS u64*)(ws + FL_EPOCH), __ATOMIC_RELAXED, __HIP_MEMORY_SCOPE_AGENT);
#pragma unroll
    for (int t = 0; t < 9; ++t) bq[t] = *(const u32x2*)(b1 + fb * 144 + 16 * t + 4 * g);
  }
  if (tid < 5) *(u32x4 LAS*)(sm + LM_CNT + tid * 16) = u32x4{tid == 1 ? 0x1c1c0000u : 0u, 0u, 0u, 0u};
  waitl<0>();
  bar();
  if (genw) {
    char LAS* kim = sm + LM_KIM + (wid & 3) * SLOT;
    char LAS* uim = sm + LM_UIM + (wid & 3) * UIM_B;
    waitv<0>();
    const float dt = rb(__expf((wid & 1) ? bh(vdt) : bl(vdt)));
    u32 par[2][6];
#pragma unroll
    for (int i = 0; i < 12; ++i) par[i & 1][i >> 1] = *(const u16 LAS*)(kim + i * 128 + lane * 2);
    f32x4 ka[2][2];
#pragma unroll
    for (int i = 0; i < 4; ++i) ka[i >> 1][i & 1] = f32x4{0.f, 0.f, 0.f, 0.f};
#pragma unroll 1
    for (int h = 0; h < 2; ++h) {
      float pv[6];
#pragma unroll
      for (int i = 0; i < 6; ++i) pv[i] = bl(h ? par[1][i] : par[0][i]);
      const float p2 = dt * pv[0] * 1.44269504088896340736f;
      const float q = dt * pv[1];
      constexpr float CH = 0.15915494309189535f;
      constexpr float CL = (float)(0.15915494309189533577 - (double)CH);
      const float qh = q * CH;
      const float ql = __builtin_fmaf(q, CL, __builtin_fmaf(q, CH, -qh));
      {
        f32x2 Z[16];
        auto put = [&](int i, f32x2 v) { *(u32 LAS*)(kim + i * KG_S + lane * 4) = pk(v[0], v[1]); };
        Z[0] = f32x2{1.f, 0.f};
        Z[1] = zpow2<3>(p2, qh, ql);
        Z[4] = zpow2<5>(p2, qh, ql);
        const f32x2 a7 = zpow2<7>(p2, qh, ql);
        Z[2] = cmul(Z[1], Z[1]);
        Z[8] = cmul(Z[4], Z[4]);
        Z[3] = cmul(Z[2], Z[1]);
#pragma unroll
        for (int i = 1; i < 4; ++i) Z[4 + i] = cmul(Z[4], Z[i]);
#pragma unroll
        for (int i = 1; i < 8; ++i) Z[8 + i] = cmul(Z[8], Z[i]);
#pragma unroll
        for (int i = 0; i < 16; ++i) put(i, Z[i]);
        put(16, a7);
#pragma unroll
        for (int i = 1; i < 16; ++i) put(16 + i, cmul(a7, Z[i]));
      }
      {
        f32x2 H[16];
        auto put = [&](int i, f32x2 v) { *(u32 LAS*)(uim + i * KG_S + lane * 4) = pk(v[0], -v[1]); };
        H[0] = cmul(f32x2{pv[4], pv[5]}, f32x2{fmul(dt, pv[2]), fmul(dt, pv[3])});
        const f32x2 a0 = zpow2<0>(p2, qh, ql), a8 = zpow2<8>(p2, qh, ql), a10 = zpow2<10>(p2, qh, ql);
        const f32x2 a1 = cmul(a0, a0), a2 = cmul(a1, a1), a9 = cmul(a8, a8), a11 = cmul(a10, a10);
        H[1] = cmul(a0, H[0]);
        H[2] = cmul(a1, H[0]);
        H[3] = cmul(a1, H[1]);
#pragma unroll
        for (int i = 0; i < 4; ++i) H[4 + i] = cmul(a2, H[i]);
#pragma unroll
        for (int i = 0; i < 4; ++i) H[8 + i] = cmul(a8, H[2 * i]);
        H[12] = cmul(a9, H[0]);
        H[13] = cmul(a9, H[4]);
        H[14] = cmul(a9, H[8]);
        H[15] = cmul(a9, H[10]);
#pragma unroll
        for (int i = 0; i < 16; ++i) put(i, H[i]);
        put(16, cmul(a10, H[0]));
        put(17, cmul(a10, H[8]));
        put(18, cmul(a10, H[12]));
        put(19, cmul(a10, H[14]));
        put(20, cmul(a11, H[0]));
      }
      u32x4 lo[4][2], hi[4][2];
#pragma unroll
      for (int k = 0; k < 4; ++k)
#pragma unroll
        for (int i = 0; i < 2; ++i) {
          lo[k][i] = *(const u32x4 LAS*)(kim + (16 * i + c) * KG_S + k * 64 + g * 16);
          hi[k][i] = *(const u32x4 LAS*)(uim + min(16 * i + c, 20) * KG_S + k * 64 + g * 16);
        }
      SB;
#pragma unroll
      for (int k = 0; k < 4; ++k)
#pragma unroll
        for (int i = 0; i < 4; ++i) ka[i >> 1][i & 1] = mfma16(lo[k][i & 1], hi[k][i >> 1], ka[i >> 1][i & 1]);
    }
#pragma unroll
    for (int i = 0; i < 4; ++i)
      *(u32x2 LAS*)(uim + (16 * (i >> 1) + c) * 64 + (i & 1) * 32 + g * 8) =
          u32x2{pk(ka[i >> 1][i & 1][0], ka[i >> 1][i & 1][1]), pk(ka[i >> 1][i & 1][2], ka[i >> 1][i & 1][3])};
    waitl<0>();
    lds_inc(sm + W_GEN);
  }
  if (team < nln) {
    f32x2 v[18];
#pragma unroll
    for (int i = 0; i < 18; ++i) v[i] = f32x2{bl(raw[i >> 1][i & 1]), bh(raw[i >> 1][i & 1])};
    f32x2 s2 = v[0];
#pragma unroll
    for (int i = 1; i < 18; ++i) s2 += v[i];
    const float mean = sum16(s2[0] + s2[1]) * (1.f / D);
    f32x2 q2 = {0.f, 0.f};
#pragma unroll
    for (int i = 0; i < 18; ++i) { f32x2 d = v[i] - f32x2{mean, mean}; q2 = __builtin_elementwise_fma(d, d, q2); }
    const float q = sum16(q2[0] + q2[1]);
    const float m = rb(mean);
    const float sd = rb(__builtin_amdgcn_sqrtf(rb(rb(q * (1.f / D)) + 1e-5f)));
    const float rs = __builtin_amdgcn_rcpf(sd);
#pragma unroll
    for (int i = 0; i < 9; ++i) {
      u32x2 o;
#pragma unroll
      for (int e = 0; e < 2; ++e) {
        const f32x2 d = v[2 * i + e] - f32x2{m, m};
        const u32 t = pk(d[0], d[1]);
        const f32x2 n = f32x2{bl(t), bh(t)} * f32x2{rs, rs};
        o[e] = pk(n[0], n[1]);
      }
      *(u32x2 LAS*)(sm + LM_LN + (tid >> 4) * LN_S + (c + 16 * i) * 8) = o;
    }
    waitl<0>();
    char LAS* tw = sm + (team ? W_LNT2 : W_LNT);
    lds_inc(tw);
    lds_wait4(tw);
    const __amdgpu_buffer_rsrc_t rn = rsrc(ws + WS_XNF + (size_t)(lt - 2 * team) * 18432, 18432);
    const char LAS* xp = sm + LM_LN + (16 * team + c) * LN_S + (wid & 3) * 64 + g * 8;
#pragma unroll
    for (int kk = 0; kk < 5; ++kk) {
      const u32x2 a = *(const u32x2 LAS*)(xp + kk * 256), bb = *(const u32x2 LAS*)(xp + kk * 256 + 32);
      st128(u32x4{a[0], a[1], bb[0], bb[1]}, rn, lane * 16, ((wid & 3) + 4 * kk) * 1024);
    }
    if (team) {
      waitv<0>();
      lds_inc(sm + W_LND2);
    }
  }
  if (__builtin_expect(wid < 4, 1)) {
    __builtin_amdgcn_s_setprio(2);
    char LAS* cons = sm + W_CONS + wid;
    char LAS* land = sm + W_LAND;
    u32 pre = 0;
    asm volatile("s_nop 0" : "+v"(pre));
    tag = (((u64)__builtin_amdgcn_readfirstlane((u32)(ep >> 32)) << 32) | __builtin_amdgcn_readfirstlane((u32)ep)) * 0x9E3779B97F4A7C15ull +
          0xD1B54A32D192ED03ull;
    if (tid == 128) *(u64 LAS*)(sm + W_TAG) = tag;
    f32x4 last[4];
    auto gelu_last = [&]() {
#pragma unroll
      for (int i = 0; i < 2; ++i)
        st128(gelu8(last[2 * i], last[2 * i + 1]), rsrc(ws + WS_HF + (size_t)(cl * 8 + (idx & 7)) * HB, HB), lane * 16, 65536 + (2 * wid + i) * 1024);
    };
    {
      constexpr int XP = 3, WD = 3, WR = 4;
      const int lt0 = 16 * lb + 4 * wid;
      const __amdgpu_buffer_rsrc_t rx = rsrc(ws + WS_XNF + (size_t)lt0 * 18432, 4 * 18432);
      f32x4 acc[9][4];
      auto announce = [&]() {
        if (lane == 0 && lds_add(sm + W_LND) == 3u) {
          if (conv) flag_set(ws + b * 8, FL_LN2, tag); else flag_set(ws, FL_LN + lt, tag);
          if (nln == 2) flag_set(ws, FL_LN + lt - 2, tag);
        }
      };
      if (genw) {
        lds_wait4(sm + W_XOK);
      } else {
        waitv<0>(); waitl<0>();
        if (nln == 2) lds_wait4(sm + W_LND2);
        announce();
        if (conv) {
          __builtin_amdgcn_s_sleep(LN_SLEEP);
          flag_wait(ws, FL_LN + 16 * lb, 16, tag, lane);
          *(volatile u32 LAS*)(sm + W_XOK) = 4u;
        } else {
          flag_wait(ws, FL_LN + lt0, 4, tag, lane);
        }
      }
      u32x4 xf[XP + 1][4];
#pragma unroll
      for (int k = 0; k < XP; ++k)
#pragma unroll
        for (int i = 0; i < 4; ++i) xf[k][i] = ld128(rx, lane * 16, i * 18432 + k * 1024);
      SB;
      if (genw) announce();
#pragma unroll
      for (int t = 0; t < 9; ++t) {
#pragma unroll
        for (int i = 0; i < 4; ++i) {
          acc[t][i] = f32x4{bl(bq[t][0]), bh(bq[t][0]), bl(bq[t][1]), bh(bq[t][1])};
          asm volatile("" : "+v"(acc[t][i]));
        }
      }
      const char LAS* wp = sm + (lane >> 2) * 288 + (lane & 3) * 8;
      u32x2 wa[WR], wb[WR];
      const char LAS* wph = wp + 6 * SLOT;
      asm volatile("" : "+v"(wph));
      auto wptr = [&](int n) {
        const int sl = (n / 9) % 10;
        return (sl < 6 ? wp + sl * SLOT : wph + (sl - 6) * SLOT) + (n % 9) * 32;
      };
      lds_inc(sm + W_XGO);
      ring_wait<2>(pre, land);
#pragma unroll
      for (int n = 0; n < WD; ++n) { wa[n] = tr_read(wptr(n)); wb[n] = tr_read(wptr(n) + 16 * 288); }
      static_for<0, 162>([&](auto N) {
        {
          constexpr int n = decltype(N)::value, ks = n / 9, t = n % 9;
          const u32x4 w = {wa[n % WR][0], wa[n % WR][1], wb[n % WR][0], wb[n % WR][1]};
          mma(acc[t][0], w, xf[ks % (XP + 1)][0]);
          SB;
          if (t == 9 - WD && (ks & 1) && ks < 17) ring_wait<ks + 3>(pre, land);
          if (t == 0 && (ks & 1) == 0 && ks) *(volatile u8 LAS*)cons = ks;
          if (n == 161) *(volatile u8 LAS*)cons = conv ? 18 : 19;
          if (n == 150) asm volatile("" : "+v"(pre));
          if (n + WD < 162) wa[(n + WD) % WR] = tr_read(wptr(n + WD));
          SB;
          mma(acc[t][1], w, xf[ks % (XP + 1)][1]);
          SB;
          if (n + WD < 162) wb[(n + WD) % WR] = tr_read(wptr(n + WD) + 16 * 288);
          SB;
          mma(acc[t][2], w, xf[ks % (XP + 1)][2]);
          SB;
          if (t < 4 && ks + XP < 18) xf[(ks + XP) % (XP + 1)][t & 3] = ld128(rx, lane * 16, t * 18432 + (ks + XP) * 1024);
          SB;
          mma(acc[t][3], w, xf[ks % (XP + 1)][3]);
          SB;
        }
      });
      asm volatile("s_nop 15");
      if (conv) lds_wait4(sm + W_GEN);
      SB;
      const __amdgpu_buffer_rsrc_t rh = rsrc(ws + WS_HF + (size_t)(cl * 8 + (idx & 7)) * HB, HB);
#pragma unroll
      for (int i = 0; i < 4; ++i)
#pragma unroll
        for (int r = 0; r < 4; ++r) st128(gelu8(acc[2 * r][i], acc[2 * r + 1][i]), rh, lane * 16, (4 * wid + i) * 4096 + r * 1024);
#pragma unroll
      for (int i = 0; i < 4; ++i) last[i] = acc[8][i];
    }
    {
      constexpr int XP = G2_XP, X0 = G2_X0, WD = G2_WD, WR = WD + 1, TAIL = 4;
      const __amdgpu_buffer_rsrc_t rh = rsrc(ws + WS_HF + (size_t)cl * 8 * HB, 8 * HB);
      const int pr = 4 * ((idx >> 2) & 1) + wid;
      u32x4 xf[XP + 1][2];
      auto hload = [&](int k, int i) {
        const int r = k % 9, P = ((idx >> 1) + k / 9) & 3;
        const int so = r < 8 ? (2 * P + (((r >> 2) ^ idx) & 1)) * HB + pr * 8192 + i * 4096 : (2 * P + i) * HB + 65536 + pr * 1024;
        xf[k % (XP + 1)][i] = ld128(rh, lane * 16 + (r < 8 ? (r & 3) * 1024 : 0), so);
      };
      auto publish = [&]() {
        waitv<0>();
        lds_inc(sm + W_HD);
      };
      if (conv) {
        const char LAS* kp = sm + LM_KIM + wid * SLOT + (c & 1) * KIM_C1 + (4 * (c >> 1) + g) * 16;
        char LAS* ub = sm + LM_UIM + wid * UIM_B + 64;
        const char LAS* up[4];
#pragma unroll
        for (int i = 0; i < 4; ++i) {
          const char LAS* q = ub + 2 * (8 * ((c >> 2) - g) + 2 * (c & 3) - 6) + i * 1024;
          asm volatile("" : "+v"(q));
          up[i] = q + 448;
        }
        f32x4 ya[8];
        u32x4 kf[2][8], uf[2][8];
        auto rdk = [&](int be, int a) { kf[be & 1][a] = *(const u32x4 LAS*)(kp + be * 64 + a * 512); };
        auto rdu = [&](int be, int a, int hh) {
          const u32 LAS* q = (const u32 LAS*)(up[a >> 1] - be * 64 + (a & 1) * 512) + 2 * hh;
          uf[be & 1][a][2 * hh] = q[0];
          uf[be & 1][a][2 * hh + 1] = q[1];
        };
        lds_wait4(sm + W_STG);
        rdk(0, 0);
#pragma unroll
        for (int a = 0; a < 16; ++a) rdu(0, a >> 1, a & 1);
#pragma unroll
        for (int a = 1; a < 8; ++a) rdk(0, a);
        SB;
        gelu_last();
        SB;
        static_for<0, 288>([&](auto N) {
          constexpr int n = decltype(N)::value, be = n / 36, m = n % 36;
          constexpr int a = m < 8 ? 0 : m < 15 ? 1 : m < 21 ? 2 : m < 26 ? 3 : m < 30 ? 4 : m < 33 ? 5 : m < 35 ? 6 : 7;
          constexpr int T = a + m - (8 * a - a * (a - 1) / 2);
          mma<(n < 8)>(ya[T], uf[be & 1][T - a], kf[be & 1][a]);
          SB;
          if (be < 7 && m < 16) rdu(be + 1, m >> 1, m & 1);
          if (be < 7 && m >= 16 && m < 24) rdk(be + 1, (m - 16) & 7);
          if (n == CV_PUB) publish();
          if (n == CV_CHK) lds_wait4(sm + W_HOK);
          if (n > CV_CHK && (n - CV_CHK) % 4 == 1 && (n - CV_CHK) / 4 < 2 * X0) hload((n - CV_CHK) / 8, ((n - CV_CHK) / 4) & 1);
          if (n == 252) { waitl<0>(); *(volatile u8 LAS*)cons = 19; }
          SB;
        });
        asm volatile("s_nop 7");
        SB;
        char LAS* yp = ub + 2 * (8 * (g + 4 * (c >> 1)) + 4 * (c & 1));
#pragma unroll
        for (int T = 0; T < 8; ++T) *(u32x2 LAS*)(yp + T * 512) = u32x2{pk(ya[T][0], ya[T][1]), pk(ya[T][2], ya[T][3])};
        waitl<0>();
        lds_inc(sm + W_OWN);
      }
      int idx2 = idx;
      asm volatile("" : "+s"(idx2));
      if (idx2 >= 18) {
        gelu_last();
        publish();
        lds_wait4(sm + W_HD);
#pragma unroll
        for (int k = 0; k < 4; ++k) { hload(k, 0); hload(k, 1); }
        lds_wait4(sm + W_HOK);
#pragma unroll
        for (int k = 4; k < X0; ++k) { hload(k, 0); hload(k, 1); }
      }
      f32x4 acc[9][2];
      const char LAS* wp = sm + (lane >> 2) * 288 + (lane & 3) * 8;
      u32x2 wa[WR], wb[WR];
      constexpr auto ord_ks = [](int n) { return n < 9 * (36 - TAIL) ? n / 9 : 36 - TAIL + (n - 9 * (36 - TAIL)) % TAIL; };
      constexpr auto ord_t = [](int n) { return n < 9 * (36 - TAIL) ? n % 9 : (n - 9 * (36 - TAIL)) / TAIL; };
      const char LAS* wph = wp + 7 * SLOT;
      asm volatile("" : "+v"(wph));
      auto wptr = [&](int n) {
        const int sl = slot2(ord_ks(n));
        return (sl < 7 ? wp + sl * SLOT : wph + (sl - 7) * SLOT) + ord_t(n) * 32;
      };
      ring_wait<28>(pre, land);
#pragma unroll
      for (int n = 0; n < WD; ++n) { wa[n] = tr_read(wptr(n)); wb[n] = tr_read(wptr(n) + 16 * 288); }
      SB;
      Epi epi;
      const int ft = wid == s ? 8 : 4 + ((wid - s - 1) & 3);
      u32x4 fv;
      auto flush = [&](auto OWN, int tt, int part) {
        if (decltype(OWN)::value) {
          if (part < 2) *(f32x4 LAS*)(sm + LM_UIM + (2 * tt + part) * 1024 + lane * 16) = acc[tt][part & 1];
          asm volatile("" ::: "memory");
          if (part == 2 && tt == 7) *(volatile u32 LAS*)(sm + W_PART) = 8u;
        } else if (part < 2) {
          fv[2 * part] = pk(acc[tt][part & 1][0], acc[tt][part & 1][1]);
          fv[2 * part + 1] = pk(acc[tt][part & 1][2], acc[tt][part & 1][3]);
        } else {
          st128(fv, rs, lane * 16, (wid * 4 + s) * SLAB_B + tt * 1024);
        }
      };
      auto step = [&](auto N, auto OWN) {
        constexpr int n = decltype(N)::value;
        constexpr int ks = ord_ks(n), t = ord_t(n);
        if (ks % 9 == 8 && t == 0) {
          const u32x4 a = xf[ks % (XP + 1)][0], bb = xf[ks % (XP + 1)][1];
          xf[ks % (XP + 1)][0] = u32x4{a[0], a[1], bb[0], bb[1]};
          xf[ks % (XP + 1)][1] = u32x4{a[2], a[3], bb[2], bb[3]};
          asm volatile("s_nop 1" : "+v"(xf[ks % (XP + 1)][0]), "+v"(xf[ks % (XP + 1)][1]));
        }
        const u32x4 w = {wa[n % WR][0], wa[n % WR][1], wb[n % WR][0], wb[n % WR][1]};
        if (n % 2 == 0 && n + WD < 324) waitl_seen<2 * WD - 4>();
        if (n < 9 * (36 - TAIL) + TAIL) mma<(n < 9)>(acc[t][0], w, xf[ks % (XP + 1)][0]);
        else acc[t][0] = mfma16(w, xf[ks % (XP + 1)][0], acc[t][0]);
        SB;
        if ((n + WD) % 18 == 0 && n + WD >= 90 && n + WD <= 288) ring_wait<n + WD == 288 ? 54 : 20 + (n + WD) / 9>(pre, land);
        if (n % 18 == 0 && n && n <= 198) *(volatile u8 LAS*)cons = 18 + n / 9;
        if (n + WD < 324) wa[(n + WD) % WR] = tr_read(wptr(n + WD));
        SB;
        if (n < 9 * (36 - TAIL) + TAIL) mma<(n < 9)>(acc[t][1], w, xf[ks % (XP + 1)][1]);
        else acc[t][1] = mfma16(w, xf[ks % (XP + 1)][1], acc[t][1]);
        SB;
        if (n + WD < 324) wb[(n + WD) % WR] = tr_read(wptr(n + WD) + 16 * 288);
        if (n < 9 * (36 - TAIL) && (t & 1) && h_upto(ks - 1) + 1 + (t >> 2) <= h_upto(ks)) hload(h_upto(ks - 1) + 1 + (t >> 2), (t >> 1) & 1);
        if (n == 9 * (36 - TAIL) + TAIL - 1) asm volatile("" ::"v"(pre));
        if (ks == 30 && t == 2) load_base(epi, ft);
        if (ks >= 33 && t > 0) flush(OWN, t > 0 ? t - 1 : 0, ks - 33);
        SB;
      };
      static_for<0, 9 * (36 - TAIL) + TAIL>([&](auto N) { step(N, std::false_type{}); });
      if (wid == s) {
        static_for<9 * (36 - TAIL) + TAIL, 324>([&](auto N) { step(N, std::true_type{}); });
        asm volatile("s_nop 15");
        SB;
        finish(std::integral_constant<int, 1>{}, &epi, &ft, &acc[8]);
      } else {
        static_for<9 * (36 - TAIL) + TAIL, 324>([&](auto N) { step(N, std::false_type{}); });
        asm volatile("s_nop 7");
        SB;
#pragma unroll
        for (int i = 0; i < 3; ++i) flush(std::false_type{}, 8, i);
        waitv<0>();
        if (lane == 0 && lds_add(sm + W_SND) == 2u) flag_set(ws, FL_X + tile * 4 + s, tag);
        finish_lds(std::integral_constant<int, 1>{}, &epi, &ft);
      }
    }
  } else {
    const int Q = xg * 18 + idx, p = wid - 4, q = p & 1;
    u32 v1[5], v2[5], v2s;
    auto freed = [&]() {
      const u32 cs = __builtin_amdgcn_readfirstlane(*(volatile u32 LAS*)(sm + W_CONS));
      u32 m0, m1;
      asm("s_and_b32 %0, %2, 0xff\n\ts_bfe_u32 %1, %2, 0x80008\n\ts_min_u32 %0, %0, %1\n\ts_bfe_u32 %1, %2, 0x80010\n\t"
          "s_min_u32 %0, %0, %1\n\ts_lshr_b32 %1, %2, 24\n\ts_min_u32 %0, %0, %1" : "=&s"(m0), "=&s"(m1) : "s"(cs) : "scc");
      return (int)m0;
    };
    char LAS* myland = sm + W_LAND + p;
    asm volatile("" : "+v"(myland));
    int pub = 0;
    auto room = [&](int j, int need) {
      if (freed() < need) {
        waitv<0>();
        pub = 2 * j;
        *(volatile u8 LAS*)myland = pub;
        while (freed() < need) __builtin_amdgcn_s_sleep(LD_SLEEP);
      }
      asm volatile("" ::: "memory");
    };
    auto landed = [&](int j) {
      waitv<9 * LD_DEPTH>();
      pub = max(pub, 2 * (j + 1 - LD_DEPTH));
      *(volatile u8 LAS*)myland = pub;
    };
    auto stream = [&](int j0, int j1, bool track) {
#pragma unroll 1
      for (int j = j0; j < j1; ++j) {
        if (j >= 5) room(j, 2 * j - 8);
        const char* sp = (const char*)(w1 + fb * 144) + (size_t)j * 64 * F * 2;
        const u32 dst = smb + (j < 5 ? j : j - 5) * 2 * SLOT + q * 5120;
        glds16x4(sp, v1, dst);
        glds16x4(sp + 32 * F * 2, v1, dst + SLOT);
        glds16(sp + q * 32 * F * 2, v1[4], dst + q * (SLOT - 5120) + 4096);
        if (track) landed(j);
      }
    };
    auto stream2 = [&]() {
      const u64 lo4 = 0ull | (2ull << 6) | (4ull << 12) | (6ull << 18), hi4 = 9ull | (11ull << 6) | (13ull << 12) | (15ull << 18);
      const u64 tab = ((idx & 1) ? hi4 | (lo4 << 24) : lo4 | (hi4 << 24)) | ((u64)(8 + 8 * q) << 48);
      const char* base = (const char*)(w2 + (size_t)1152 * s * D + 144 * dj);
      int r = 0, P = idx >> 1, ps = 4;
#pragma unroll 1
      for (int j = 9; j < 27; ++j) {
        const bool mine = j < 14 || j >= 14 + RB;
        if (j == 14) {
          waitv<0>();
          pub = 2 * (14 + RB);
          *(volatile u8 LAS*)myland = pub;
          if (q == 0) {
            __builtin_amdgcn_s_setprio(3);
            lds_wait4<2>(sm + W_HD);
            if (lane == 0) flag_set(ws, FL_H + cl * 8 + (idx & 7), tag);
            flag_wait<2>(ws, FL_H + cl * 8, 8, tag, lane);
            *(volatile u32 LAS*)(sm + W_HOK) = 4u;
            __builtin_amdgcn_s_setprio(0);
          }
        }
        if (mine) room(j, j < 14 ? 2 * j - 8 : 2 * j - 12);
        const u32 dst = smb + ps * 2 * SLOT + q * 5120;
        ps = (0x4605321 >> (4 * ps)) & 7;
        const char* sp[2];
        bool two[2];
#pragma unroll
        for (int e = 0; e < 2; ++e) {
          sp[e] = base + (u32)(18 * (P & 3) + (int)((tab >> (6 * r)) & 63)) * (16 * D * 2);
          two[e] = r == 8;
          P += two[e];
          r = two[e] ? 0 : r + 1;
        }
        if (!mine) continue;
        glds16x4(sp[0], v2, dst);
        glds16x4(sp[1], v2, dst + SLOT);
        glds16((q ? sp[1] : sp[0]) - 128 * D * 2, (q ? two[1] : two[0]) ? v2s : v2[4] + 128 * D * 2, dst + q * (SLOT - 5120) + 4096);
        landed(j);
      }
      waitv<0>();
      *(volatile u8 LAS*)myland = 54;
    };
    if (p < 2) {
#pragma unroll
      for (int n = 0; n < 5; ++n) {
        const int ci = 64 * (n < 4 ? 5 * q + n : 4) + lane, btw = n < 4 ? n * 1024 : 0;
        v1[n] = (ci / 18) * (F * 2) + (ci % 18) * 16 - btw;
        v2[n] = (ci / 18) * (D * 2) + (ci % 18) * 16 - btw;
      }
      v2s = v2[4] + (lane >= 32 ? 128 * D * 2 : 0) - q * 128 * D * 2 + 128 * D * 2;
      stream(0, G1_N0 / 2, false);
      if (conv) {
        const int wv = ((xg >> 2) * 18 + idx) * 2 + q;
#pragma unroll
        for (int i = 0; i < 2; ++i) {
          const int ln = (wv * 2 + i) * 56 + min(lane, 55);
          glds4(w1, ((128 + ln / 18) * F + 1152 * s + (ln % 18) * 64) * 2, smb + LM_SINK);
        }
      }
      waitv<0>();
      pub = G1_N0;
      *(volatile u8 LAS*)myland = pub;
    }
    if (p >= 2 || !conv) lds_wait4<4>(sm + W_LND);
    if (p < 2) {
      stream(G1_N0 / 2, 9, true);
      tag = *(const u64 LAS*)(sm + W_TAG);
      stream2();
    } else {
      tag = *(const u64 LAS*)(sm + W_TAG);
      u32x4 wreg[RB][9];
      auto wfetch = [&]() {
        while (freed() < (conv ? RB_AT : RB_AT2)) __builtin_amdgcn_s_sleep(8);
        const __amdgpu_buffer_rsrc_t rw = rsrc(w2 + (size_t)1152 * s * D + 144 * dj, 1152 * D * 2 - 288 * dj);
#pragma unroll
        for (int n = 0; n < 9; ++n) {
          const int ci = 64 * n + lane, vo = (ci / 18) * (D * 2) + (ci % 18) * 16;
#pragma unroll
          for (int i = 0; i < RB; ++i) {
            const int k = 10 + 2 * i + q, r = k % 9;
            const int row = 288 * (((idx >> 1) + k / 9) & 3) + (r < 8 ? 144 * (((r >> 2) ^ idx) & 1) + 32 * (r & 3) : 128);
            wreg[i][n] = __builtin_amdgcn_raw_buffer_load_b128(rw, vo + (r == 8 && ci >= 288 ? 128 * D * 2 : 0), row * (D * 2), 0);
            __builtin_amdgcn_s_sleep(RB_SLEEP);
          }
        }
      };
      auto wput = [&](int i0, int i1) {
#pragma unroll
        for (int i = i0; i < i1; ++i) {
          while (freed() < max(2 * (14 + i) - 12, 19)) __builtin_amdgcn_s_sleep(LD_SLEEP);
          asm volatile("" ::: "memory");
          char LAS* dst = sm + (slot2(10 + 2 * i) + q) * SLOT + lane * 16;
#pragma unroll
          for (int n = 0; n < 9; ++n) *(u32x4 LAS*)(dst + n * 1024) = wreg[i][n];
          waitl<0>();
          *(volatile u8 LAS*)myland = i == RB - 1 ? 48 : 30 + 2 * i;
        }
      };
      if (conv) {
        lds_wait4<4>(sm + W_GEN);
#pragma unroll 1
        for (int j = 0; j < 2; ++j) {
          const char LAS* tb = sm + LM_UIM + (2 * q + j) * UIM_B;
          char LAS* ki = sm + LM_KIM + (2 * q + j) * SLOT;
          char LAS* ko = ki + (28 + lane) * 16;
          auto put = [&](int mi, const u32(&kv)[9]) {
            *(u32x4 LAS*)(ko + mi * 1024) = u32x4{kv[1] | (kv[2] << 16), kv[3] | (kv[4] << 16), kv[5] | (kv[6] << 16), kv[7] | (kv[8] << 16)};
            *(u32x4 LAS*)(ko + mi * 1024 + KIM_C1) = u32x4{kv[0] | (kv[1] << 16), kv[2] | (kv[3] << 16), kv[4] | (kv[5] << 16), kv[6] | (kv[7] << 16)};
          };
          {
            u32 kv[9];
#pragma unroll
            for (int e = 0; e < 9; ++e) {
              constexpr int EX[9] = {448, 384, 320, 256, 192, 128, 64, 0, 446}, R2[9] = {450, 640, 576, 576, 576, 512, 448, 448, 448};
              int o = 2 * lane + (lane < (e == 8 ? 33 : 32) ? EX[e] : R2[e]);
              if (e == 0 && lane == 63) o = 768;
              kv[e] = *(const u16 LAS*)(tb + o);
            }
            if (lane == 0) kv[8] = 0u;
            put(0, kv);
          }
          if (lane < 56) *(u32x4 LAS*)(ki + (lane & 1) * KIM_C1 + (lane >> 1) * 16) = u32x4{0u, 0u, 0u, 0u};
          {
            const int oa = 768 + 128 * (lane >> 5) + 2 * (lane & 31), oe = 768 + 128 * ((lane + 1) >> 5) + 2 * ((lane + 1) & 31);
            const u32 a = *(const u16 LAS*)(tb + oa), bb = *(const u16 LAS*)(tb + oa + 64), e = *(const u16 LAS*)(tb + oe);
            const u32 kv[9] = {e, e, bb, bb, bb, a, a, a, a};
            put(1, kv);
          }
#pragma unroll
          for (int mi = 2; mi < 4; ++mi) {
            const u32 a = *(const u16 LAS*)(tb + 768 + 128 * mi + 2 * lane), e = *(const u16 LAS*)(tb + 770 + 128 * mi + 2 * lane);
            const u32 tie = (lane & 1) ? e : a;
            const u32 kv[9] = {e, e, e, tie, a, a, a, a, a};
            put(mi, kv);
          }
        }
        waitl<0>();
        if (lane < 2) lds_add(sm + W_UT);
        while (freed() < U_AT) __builtin_amdgcn_s_sleep(8);
#pragma unroll 1
        for (u32 spin = 0; spin < (1u << 20); ++spin) {
          bool ok = true;
#pragma unroll
          for (int e = 0; e < 4; ++e)
            ok &= __hip_atomic_load((GAS u64*)(ws + FL_LN2 * 128) + e * 64 + lane, __ATOMIC_RELAXED, __HIP_MEMORY_SCOPE_AGENT) == tag;
          if (__all(ok)) break;
          __builtin_amdgcn_s_sleep(32);
        }
        asm volatile("" ::: "memory");
        u32x4 uv[8];
#pragma unroll
        for (int i = 0; i < 8; ++i) {
          uv[i] = ld128(rsrc(ws + WS_UT, L * D * 2), (64 * q + (lane >> 3)) * 18432 + Q * 128 + (lane & 7) * 16, i * 8 * 18432);
          __builtin_amdgcn_s_sleep(2);
        }
        const u32x2 dk = *(const u32x2*)(Dskip + 4 * Q);
        lds_wait4<4>(sm + W_UT);
#pragma unroll
        for (int e = 0; e < 8; ++e)
          *(u32x4 LAS*)(sm + LM_UIM + ((lane >> 1) & 3) * UIM_B + 64 + (64 * q + 8 * e + (lane >> 3)) * 32 + (lane & 1) * 16) = uv[e];
        if (lane < 16) *(u32x4 LAS*)(sm + LM_UIM + (lane >> 2) * UIM_B + (lane & 3) * 16) = u32x4{0u, 0u, 0u, 0u};
        const char LAS* tp = sm + LM_UIM + (c >> 2) * UIM_B + 64 + 2 * (1024 * q + 32 * g) + (c & 3) * 8;
        const int cp = (c & 8) + 2 * (c & 3) + ((c >> 2) & 1);
        u32x2 sk[16];
#pragma unroll
        for (int i = 0; i < 16; ++i) {
          const u32x2 a = tr_read(tp + (i >> 1) * 256 + (i & 1) * 32);
#pragma unroll
          for (int e = 0; e < 2; ++e)
            sk[i][e] = (u32)__builtin_amdgcn_ds_bpermute((16 * g + cp) * 4, (int)pk(fmul(bl(a[e]), bl(dk[e])), fmul(bh(a[e]), bh(dk[e]))));
        }
        waitl<0>();
        if (lane < 2) lds_add(sm + W_STG);
        wfetch();
        wput(0, RB);
        lds_wait4<4>(sm + W_OWN);
        const __amdgpu_buffer_rsrc_t ra = rsrc(ws + WS_A2 + (size_t)Q * 16384 + 8192 * q, 8192);
#pragma unroll
        for (int i = 0; i < 8; ++i) {
          const u32x2 a = tr_read(tp + i * 256), bb = tr_read(tp + i * 256 + 32);
          st128(u32x4{add2(a[0], sk[2 * i][0]), add2(a[1], sk[2 * i][1]), add2(bb[0], sk[2 * i + 1][0]), add2(bb[1], sk[2 * i + 1][1])}, ra,
                (16 * g + cp) * 16, i * 1024);
        }
        waitv<0>();
        if (lane == 0 && lds_add(sm + W_A2) == 1u) flag_set(ws, FL_A + Q, tag);
      } else {
#pragma unroll 1
        for (int j = 0; j < nln; ++j) {
          const __amdgpu_buffer_rsrc_t uo = rsrc(ws + WS_UT + (size_t)(lt - 2 * j) * 18432, 18432);
          const char LAS* tp = sm + LM_LN + (16 * j + 8 * (g & 1) + (c >> 2)) * LN_S + q * 64 + (g >> 1) * 32 + (c & 3) * 8;
#pragma unroll
          for (int kk = 0; kk < 9; ++kk) {
            const u32x2 t0 = tr_read(tp + kk * 128), t1 = tr_read(tp + kk * 128 + 4 * LN_S);
            st128(u32x4{t0[0], t0[1], t1[0], t1[1]}, uo, (16 * (g >> 1) + c) * 32 + (g & 1) * 16, (q + 2 * kk) * 1024);
          }
        }
        waitv<0>();
        if (lane == 0 && lds_add(sm + W_A2) == 1u) flag_set(ws + b * 8, FL_LN2, tag);
        if (lane < 2) lds_add(sm + W_UT);
        lds_wait4<4>(sm + W_XGO);
        __builtin_amdgcn_s_sleep(W2_SLEEP);
#pragma unroll
        for (int i = 0; i < 3; ++i)
          glds4(w2 + (size_t)1152 * s * D, min((((((xg >> 2) * 14 + idx - 18) * 2 + q) * 3 + i) * 62 + min(lane, 61)), 1152 * 9 - 1) * 128, smb + LM_SINK);
        wfetch();
        lds_wait4<4>(sm + W_UT);
        wput(0, RB);
      }
      if (q) {
        while (freed() < A_AT) __builtin_amdgcn_s_sleep(8);
        flag_wait(ws, FL_A + dj * 36, 36, tag, lane);
      }
      *(volatile u8 LAS*)myland = 54;
      if (lane < 2) lds_add(sm + W_AOK);
    }
    lds_wait4<4>(sm + W_AOK);
    if (tid == 256 && b == 0) flag_set(ws, FL_EPOCH, tag);
    Epi epi[2];
    load_base(epi[0], p);
    if (p == 0) load_base(epi[1], 7);
    const int ft[2] = {p, 7};
    if (p == 0) finish_lds(std::integral_constant<int, 2>{}, epi, ft);
    asm volatile(".Lk_all_tail:");
    if (p != 0) finish_lds(std::integral_constant<int, 1>{}, epi, ft);
  }
}

extern "C" void kernel_launch(void* const* d_in, const int* in_sizes, int n_in, void* d_out, int out_size, void* d_ws,
                              size_t ws_size, hipStream_t stream) {
  const u16* const* in = (const u16* const*)d_in;
  (void)hipFuncSetAttribute((const void*)k_all, hipFuncAttributeMaxDynamicSharedMemorySize, LM_END);
  k_all<<<256, 512, LM_END, stream>>>(in[0], in[1], in[2], in[3], in[4], in[5], in[6], in[7], in[8], in[10], in[11], in[12], in[13],
                                      (char*)d_ws, (u16*)d_out);
}
```
